```python
import torch

def _softmax(x, axis=-1):
    return torch.softmax(x, dim=axis)
def _gelu(x):
    return torch.nn.functional.gelu(x, approximate="tanh")
def _silu(x):
    return torch.nn.functional.silu(x)
def _sigmoid(x):
    return torch.sigmoid(x)
def _layernorm(x, axis=-1, eps=1e-5):
    m = x.mean(dim=axis, keepdim=True)
    v = x.var(dim=axis, keepdim=True, unbiased=False)
    return (x - m) / torch.sqrt(v + eps)
def _scatter_add(tgt, idx, src, axis=0):
    return tgt.clone().index_add_(axis, idx, src)
def _segment_sum(src, seg, n):
    out = torch.zeros((n,) + src.shape[1:], dtype=src.dtype)
    return out.index_add_(0, seg, src)
def _pad(x, pw):
    flat = tuple(v for pair in reversed(pw) for v in pair)
    return torch.nn.functional.pad(x, flat)

kernel_name = 'concat_heads__gqa_block_cached__mha_block_cached__near_ridge'

def setup_inputs():

    _w = (lambda a: (lambda t: t.to(torch.bfloat16) if t.is_floating_point() else t)(torch.as_tensor(a)))
    def _A_setup():
        B, Sq, D = 16, 80, 576
        Hq, Hkv, Sk, Dh = 32, 16, 512, 128
        assert Hq % Hkv == 0, (Hq, Hkv)
        return {
            "x": _w((lambda *s: torch.randn(*s, dtype=torch.bfloat16) * 0.8)(B, Sq, D)),
            "k_cache": _w((lambda *s: torch.randn(*s, dtype=torch.bfloat16) * 0.8)(B, Hkv, Sk, Dh)),
            "v_cache": _w((lambda *s: torch.randn(*s, dtype=torch.bfloat16) * 0.05)(B, Hkv, Sk, Dh)),
            "wq": _w((lambda *s: torch.randn(*s, dtype=torch.bfloat16) * 0.8)(D, Hq * Dh) * (D ** -0.5)),
            "wo": _w((lambda *s: torch.randn(*s, dtype=torch.bfloat16) * 0.05)(Hq * Dh, D) * ((Hq * Dh) ** -0.5)),
        }
    _A_in = _A_setup()

    def _B_setup():
        B, Sq, D = 16, 80, 576
        H, Sk, Dh = 32, 512, 128
        return {
            "x": _w((lambda *s: torch.randn(*s, dtype=torch.bfloat16) * 0.8)(B, Sq, D)),
            "k_cache": _w((lambda *s: torch.randn(*s, dtype=torch.bfloat16) * 0.8)(B, H, Sk, Dh)),
            "v_cache": _w((lambda *s: torch.randn(*s, dtype=torch.bfloat16) * 0.05)(B, H, Sk, Dh)),
            "wq": _w((lambda *s: torch.randn(*s, dtype=torch.bfloat16) * 0.8)(D, H * Dh) * (D ** -0.5)),
            "wo": _w((lambda *s: torch.randn(*s, dtype=torch.bfloat16) * 0.05)(H * Dh, D) * ((H * Dh) ** -0.5)),
        }
    _B_in = _B_setup()

    return {
        "A_x": _A_in["x"],
        "A_k_cache": _A_in["k_cache"],
        "A_v_cache": _A_in["v_cache"],
        "A_wq": _A_in["wq"],
        "A_wo": _A_in["wo"],
        "B_x": _B_in["x"],
        "B_k_cache": _B_in["k_cache"],
        "B_v_cache": _B_in["v_cache"],
        "B_wq": _B_in["wq"],
        "B_wo": _B_in["wo"],
    }

def reference(A_x, A_k_cache, A_v_cache, A_wq, A_wo, B_x, B_k_cache, B_v_cache, B_wq, B_wo):
    def _A_ref(x, k_cache, v_cache, wq, wo):
        B, Sq, D = x.shape
        Hq, Dh = 32, 128
        Hkv = k_cache.shape[1]
        rep = Hq // Hkv
        q = (lambda x, *d: x.permute(*d))((x @ wq).reshape(B, Sq, Hq, Dh), 0, 2, 1, 3)
        k = (lambda x, n, axis: torch.repeat_interleave(x, n, dim=axis))(k_cache, rep, axis=1)
        v = (lambda x, n, axis: torch.repeat_interleave(x, n, dim=axis))(v_cache, rep, axis=1)
        scores = torch.einsum("bhqd,bhkd->bhqk", q, k) * (Dh ** -0.5)
        p = _softmax(scores, axis=-1)
        o = torch.einsum("bhqk,bhkd->bhqd", p, v)
        return (lambda x, *d: x.permute(*d))(o, 0, 2, 1, 3).reshape(B, Sq, Hq * Dh) @ wo

    def _B_ref(x, k_cache, v_cache, wq, wo):
        B, Sq, D = x.shape
        H, Dh = 32, 128
        q = (lambda x, *d: x.permute(*d))((x @ wq).reshape(B, Sq, H, Dh), 0, 2, 1, 3)
        scores = torch.einsum("bhqd,bhkd->bhqk", q, k_cache) * (Dh ** -0.5)
        p = _softmax(scores, axis=-1)
        o = torch.einsum("bhqk,bhkd->bhqd", p, v_cache)
        return (lambda x, *d: x.permute(*d))(o, 0, 2, 1, 3).reshape(B, Sq, H * Dh) @ wo

    a = _A_ref(A_x, A_k_cache, A_v_cache, A_wq, A_wo)
    b = _B_ref(B_x, B_k_cache, B_v_cache, B_wq, B_wo)
    return torch.concatenate([a, b], axis=-1)
```

```cpp
#include <hip/hip_runtime.h>
#include <hip/hip_bf16.h>
typedef unsigned short u16;
__device__ __forceinline__ float bf2f(u16 x){ return __uint_as_float(((unsigned)x)<<16); }
__device__ __forceinline__ u16 f2bf(float f){ __hip_bfloat16 h = __float2bfloat16(f); return *reinterpret_cast<u16*>(&h); }

__global__ void gemm_naive(const u16* A, const u16* B, u16* C, int M, int N, int K, int ldc){
  __shared__ float As[16][17], Bs[16][17];
  int tx = threadIdx.x & 15, ty = threadIdx.x >> 4;
  int row = blockIdx.y*16+ty, col = blockIdx.x*16+tx;
  float acc = 0;
  for(int k0=0;k0<K;k0+=16){
    As[ty][tx] = bf2f(A[(size_t)row*K+k0+tx]);
    Bs[ty][tx] = bf2f(B[(size_t)(k0+ty)*N+col]);
    __syncthreads();
    for(int k=0;k<16;k++) acc += As[ty][k]*Bs[k][tx];
    __syncthreads();
  }
  C[(size_t)row*ldc+col] = f2bf(acc);
}
__global__ void attn_naive(const u16* Q, const u16* Kc, const u16* Vc, u16* O, int Hkv){
  int q = blockIdx.x, h = blockIdx.y, b = blockIdx.z;
  int rep = 32/Hkv; int hk = h/rep;
  __shared__ float qs[128], ps[512], red[128];
  int t = threadIdx.x;
  qs[t] = bf2f(Q[((size_t)(b*80+q))*4096 + h*128 + t]);
  __syncthreads();
  const u16* Kb = Kc + ((size_t)(b*Hkv+hk))*512*128;
  const u16* Vb = Vc + ((size_t)(b*Hkv+hk))*512*128;
  float mx = -1e30f;
  for(int j=t;j<512;j+=128){ float s=0; for(int d=0;d<128;d++) s += qs[d]*bf2f(Kb[j*128+d]); s *= 0.08838834764831845f; ps[j]=s; mx = fmaxf(mx,s);}
  red[t]=mx; __syncthreads();
  for(int o=64;o>0;o>>=1){ if(t<o) red[t]=fmaxf(red[t],red[t+o]); __syncthreads(); }
  mx = red[0]; __syncthreads();
  float sum=0; for(int j=t;j<512;j+=128){ float e=__expf(ps[j]-mx); ps[j]=e; sum+=e; }
  red[t]=sum; __syncthreads();
  for(int o=64;o>0;o>>=1){ if(t<o) red[t]+=red[t+o]; __syncthreads(); }
  sum = red[0];
  float acc=0; for(int j=0;j<512;j++) acc += ps[j]*bf2f(Vb[j*128+t]);
  O[((size_t)(b*80+q))*4096 + h*128 + t] = f2bf(acc/sum);
}
extern "C" void kernel_launch(void* const* d_in, const int* in_sizes, int n_in,
                              void* d_out, int out_size, void* d_ws, size_t ws_size,
                              hipStream_t stream) {
  u16* Q = (u16*)d_ws; u16* O = Q + (size_t)1280*4096;
  for(int br=0;br<2;br++){
    const u16* x=(const u16*)d_in[br*5+0]; const u16* k=(const u16*)d_in[br*5+1]; const u16* v=(const u16*)d_in[br*5+2];
    const u16* wq=(const u16*)d_in[br*5+3]; const u16* wo=(const u16*)d_in[br*5+4];
    gemm_naive<<<dim3(4096/16,1280/16),256,0,stream>>>(x,wq,Q,1280,4096,576,4096);
    attn_naive<<<dim3(80,32,16),128,0,stream>>>(Q,k,v,O,br==0?16:32);
    gemm_naive<<<dim3(576/16,1280/16),256,0,stream>>>(O,wo,(u16*)d_out+br*576,1280,576,4096,1152);
  }
}
```

```cpp
#include <hip/hip_runtime.h>
#include <hip/hip_bf16.h>

__global__ __launch_bounds__(256, 1) void fused_attn_blocks(
    const char* __restrict__ xA, const char* __restrict__ kA, const char* __restrict__ vA, const char* __restrict__ wqA, const char* __restrict__ woA,
    const char* __restrict__ xB, const char* __restrict__ kB, const char* __restrict__ vB, const char* __restrict__ wqB, const char* __restrict__ woB,
    char* out, char* ws) {
  __shared__ __attribute__((aligned(16))) char lds[163840];
  const unsigned wg = blockIdx.x;
  const unsigned w = __builtin_amdgcn_readfirstlane(threadIdx.x >> 6);
  const unsigned ldsb = (unsigned)(size_t)lds;
  asm volatile(
    "s_load_dwordx16 s[8:23], %0, 0x0\n"
    "s_load_dwordx8 s[24:31], %0, 0x40\n"
    "s_and_b32 s5, %1, 7\n"
    "s_bfe_u32 s7, %1, 0x10005\n"
    "s_bfe_u32 s33, %1, 0x20003\n"
    "s_lshr_b32 s6, %1, 6\n"
    "s_lshl2_add_u32 s33, s6, s33\n"
    "s_lshl_b32 s6, s5, 2\n"
    "s_add_u32 s6, s6, %2\n"
    "v_mbcnt_lo_u32_b32 v240, -1, 0\n"
    "v_mbcnt_hi_u32_b32 v240, -1, v240\n"
    "s_add_u32 s91, s33, s5\n"
    "s_sub_u32 s90, s91, 9\n"
    "s_min_u32 s91, s91, s90\n"
    "s_sub_u32 s90, s91, 9\n"
    "s_min_u32 s91, s91, s90\n"
    "v_and_b32 v0, 15, v240\n"
    "v_lshrrev_b32 v1, 4, v240\n"
    "v_lshl_add_u32 v2, v240, 2, %2\n"
    "v_mul_u32_u24 v3, 52, v2\n"
    "v_lshrrev_b32 v3, 8, v3\n"
    "v_mul_u32_u24 v4, 5, v3\n"
    "v_sub_u32 v4, v2, v4\n"
    "v_add_u32 v3, s91, v3\n"
    "v_subrev_u32 v5, 9, v3\n"
    "v_min_u32 v3, v3, v5\n"
    "v_mul_u32_u24 v4, 18432, v4\n"
    "v_lshl_add_u32 v3, v3, 7, v4\n"
    "v_mov_b32 v4, 0x7ff00000\n"
    "v_cmp_gt_u32 vcc, 45, v2\n"
    "s_mov_b32 s94, 0\n"
    "s_lshl_b32 s98, %2, 11\n"
    "v_cndmask_b32 v252, v4, v3, vcc\n"
    "v_lshrrev_b32 v2, 3, v240\n"
    "v_mul_u32_u24 v2, 1152, v2\n"
    "v_and_b32 v3, 7, v240\n"
    "v_xor_b32 v3, v3, v1\n"
    "v_lshl_add_u32 v245, v3, 4, v2\n"
    "v_xor_b32 v3, 4, v3\n"
    "v_lshl_add_u32 v2, v3, 4, v2\n"
    "v_add_u32 v241, 8192, v2\n"
    "s_getpc_b64 s[56:57]\n"
    "s_and_b32 s57, s57, 0xffff\n"
    "s_mov_b32 s58, L_end%=-L_pc%=\n"
    "s_mov_b32 s59, 0x20000\n"
    "L_pc%=:\n"
    "s_lshl_b32 s90, s33, 1\n"
    "s_add_u32 s90, s90, s7\n"
    "s_lshl_b32 s90, s90, 2\n"
    "s_add_u32 s90, s90, %2\n"
    "s_lshl_b32 s90, s90, 9\n"
    "v_lshlrev_b32 v2, 7, v1\n"
    "buffer_load_dword v254, v2, s[56:59], s90 offen\n"
    "s_mul_i32 s90, s33, 92160\n"
    "s_lshl_b32 s92, s6, 8\n"
    "s_waitcnt lgkmcnt(0)\n"
    "s_cmp_lg_u32 s7, 0\n"
    "s_cselect_b64 s[8:9], s[18:19], s[8:9]\n"
    "s_cselect_b64 s[10:11], s[20:21], s[10:11]\n"
    "s_cselect_b64 s[12:13], s[22:23], s[12:13]\n"
    "s_cselect_b64 s[14:15], s[24:25], s[14:15]\n"
    "s_cselect_b64 s[16:17], s[26:27], s[16:17]\n"
    "s_add_u32 s8, s8, s90\n"
    "s_addc_u32 s9, s9, 0\n"
    "s_add_u32 s18, s18, s90\n"
    "s_addc_u32 s19, s19, 0\n"
    "s_add_u32 s24, s24, s92\n"
    "s_addc_u32 s25, s25, 0\n"
    "s_add_u32 s14, s14, s92\n"
    "s_addc_u32 s15, s15, 0\n"
    "s_cmp_lg_u32 s7, 0\n"
    "s_cbranch_scc1 L_nohelp%=\n"
    "s_mov_b64 s[36:37], s[18:19]\n"
    "s_and_b32 s37, s37, 0xffff\n"
    "s_mov_b32 s38, 92160\n"
    "s_mov_b32 s39, 0x20000\n"
    "s_mul_i32 s90, %2, 23040\n"
    "s_add_u32 s92, s91, 4\n"
    "v_add_u32 v2, 0, v240\n"
    "v_min_u32 v2, 99, v2\n"
    "v_mul_u32_u24 v3, 820, v2\n"
    "v_lshrrev_b32 v3, 12, v3\n"
    "v_mul_u32_u24 v4, 5, v3\n"
    "v_sub_u32 v2, v2, v4\n"
    "v_add_u32 v2, s92, v2\n"
    "v_subrev_u32 v4, 9, v2\n"
    "v_min_u32 v2, v2, v4\n"
    "v_mul_u32_u24 v3, 1152, v3\n"
    "v_lshl_add_u32 v3, v2, 7, v3\n"
    "buffer_load_dword v254, v3, s[36:39], s90 offen\n"
    "v_add_u32 v2, 64, v240\n"
    "v_min_u32 v2, 99, v2\n"
    "v_mul_u32_u24 v3, 820, v2\n"
    "v_lshrrev_b32 v3, 12, v3\n"
    "v_mul_u32_u24 v4, 5, v3\n"
    "v_sub_u32 v2, v2, v4\n"
    "v_add_u32 v2, s92, v2\n"
    "v_subrev_u32 v4, 9, v2\n"
    "v_min_u32 v2, v2, v4\n"
    "v_mul_u32_u24 v3, 1152, v3\n"
    "v_lshl_add_u32 v3, v2, 7, v3\n"
    "buffer_load_dword v254, v3, s[36:39], s90 offen\n"
    "s_mov_b64 s[40:41], s[24:25]\n"
    "s_and_b32 s41, s41, 0xffff\n"
    "s_mov_b32 s42, 4710656\n"
    "s_mov_b32 s43, 0x20000\n"
    "s_mul_i32 s90, s33, 294912\n"
    "v_lshrrev_b32 v6, 1, v240\n"
    "v_lshlrev_b32 v6, 13, v6\n"
    "v_and_b32 v7, 1, v240\n"
    "v_lshl_add_u32 v6, v7, 7, v6\n"
    "buffer_load_dword v254, v6, s[40:43], s90 offen\n"
    "v_and_b32 v6, 24704, v6\n"
    "s_add_u32 s90, s90, 262144\n"
    "buffer_load_dword v253, v6, s[40:43], s90 offen\n"
    "s_lshl_b32 s90, s33, 5\n"
    "s_add_u32 s90, s90, s6\n"
    "s_lshl_b32 s90, s90, 17\n"
    "v_mov_b32 v2, 0\n"
    "s_mov_b64 s[44:45], s[20:21]\n"
    "s_and_b32 s45, s45, 0xffff\n"
    "s_mov_b32 s46, 67108864\n"
    "s_mov_b32 s47, 0x20000\n"
    "buffer_load_dword v254, v2, s[44:47], s90 offen\n"
    "s_mov_b64 s[44:45], s[22:23]\n"
    "s_and_b32 s45, s45, 0xffff\n"
    "s_mov_b32 s46, 67108864\n"
    "s_mov_b32 s47, 0x20000\n"
    "buffer_load_dword v254, v2, s[44:47], s90 offen\n"
    "L_nohelp%=:\n"
    "s_mov_b64 s[36:37], s[8:9]\n"
    "s_and_b32 s37, s37, 0xffff\n"
    "s_mov_b32 s38, 92160\n"
    "s_mov_b32 s39, 0x20000\n"
    "v_readlane_b32 s85, v252, s94\n"
    "s_add_u32 s94, s94, 1\n"
    "s_min_u32 m0, s98, 92160\n"
    "s_add_u32 s98, s98, 8192\n"
    "s_nop 1\n"
    "buffer_load_dwordx4 v245, s[36:39], s85 offen offset:0 lds\n"
    "buffer_load_dwordx4 v241, s[36:39], s85 offen offset:1024 lds\n"
    "v_readlane_b32 s85, v252, s94\n"
    "s_add_u32 s94, s94, 1\n"
    "s_min_u32 m0, s98, 92160\n"
    "s_add_u32 s98, s98, 8192\n"
    "s_nop 1\n"
    "buffer_load_dwordx4 v245, s[36:39], s85 offen offset:0 lds\n"
    "buffer_load_dwordx4 v241, s[36:39], s85 offen offset:1024 lds\n"
    "v_readlane_b32 s85, v252, s94\n"
    "s_add_u32 s94, s94, 1\n"
    "s_min_u32 m0, s98, 92160\n"
    "s_add_u32 s98, s98, 8192\n"
    "s_nop 1\n"
    "buffer_load_dwordx4 v245, s[36:39], s85 offen offset:0 lds\n"
    "buffer_load_dwordx4 v241, s[36:39], s85 offen offset:1024 lds\n"
    "v_readlane_b32 s85, v252, s94\n"
    "s_add_u32 s94, s94, 1\n"
    "s_min_u32 m0, s98, 92160\n"
    "s_add_u32 s98, s98, 8192\n"
    "s_nop 1\n"
    "buffer_load_dwordx4 v245, s[36:39], s85 offen offset:0 lds\n"
    "buffer_load_dwordx4 v241, s[36:39], s85 offen offset:1024 lds\n"
    "s_lshl1_add_u32 s90, s33, s7\n"
    "s_lshl_b32 s92, s90, 11\n"
    "s_add_u32 s22, s30, s92\n"
    "s_addc_u32 s23, s31, 0\n"
    "s_mul_i32 s90, s90, 737280\n"
    "s_add_u32 s90, s90, 65536\n"
    "s_add_u32 s20, s30, s90\n"
    "s_addc_u32 s21, s31, 0\n"
    "s_lshl_b32 s90, s33, 5\n"
    "s_add_u32 s90, s90, s6\n"
    "s_xor_b32 s92, s7, 1\n"
    "s_lshr_b32 s90, s90, s92\n"
    "s_lshl_b32 s90, s90, 17\n"
    "s_add_u32 s10, s10, s90\n"
    "s_addc_u32 s11, s11, 0\n"
    "s_add_u32 s12, s12, s90\n"
    "s_addc_u32 s13, s13, 0\n"
    "s_mul_i32 s90, s5, 589824\n"
    "s_add_u32 s16, s16, s90\n"
    "s_addc_u32 s17, s17, 0\n"
    "s_add_u32 s26, s26, s90\n"
    "s_addc_u32 s27, s27, 0\n"
    "s_lshl_b32 s90, s33, 3\n"
    "s_add_u32 s90, s90, s5\n"
    "s_mul_i32 s90, s90, 23040\n"
    "s_mul_i32 s92, s7, 1152\n"
    "s_add_u32 s90, s90, s92\n"
    "s_add_u32 s28, s28, s90\n"
    "s_addc_u32 s29, s29, 0\n"
    "s_mov_b64 s[60:61], s[22:23]\n"
    "s_and_b32 s61, s61, 0xffff\n"
    "s_mov_b32 s62, 2048\n"
    "s_mov_b32 s63, 0x20000\n"
    "v_mov_b32 v2, 1024\n"
    "s_lshl_b32 s90, s5, 7\n"
    "v_writelane_b32 v2, s90, 63\n"
    "buffer_load_dwordx2 v[250:251], v2, s[60:63], 0 offen sc1\n"
    "s_mov_b64 s[40:41], s[14:15]\n"
    "s_and_b32 s41, s41, 0xffff\n"
    "s_mov_b32 s42, 4710656\n"
    "s_mov_b32 s43, 0x20000\n"
    "s_mov_b64 s[44:45], s[10:11]\n"
    "s_and_b32 s45, s45, 0xffff\n"
    "v_readlane_b32 s85, v252, s94\n"
    "s_add_u32 s94, s94, 1\n"
    "s_min_u32 m0, s98, 92160\n"
    "s_add_u32 s98, s98, 8192\n"
    "s_nop 1\n"
    "buffer_load_dwordx4 v245, s[36:39], s85 offen offset:0 lds\n"
    "buffer_load_dwordx4 v241, s[36:39], s85 offen offset:1024 lds\n"
    "s_mov_b32 s46, 131072\n"
    "s_mov_b32 s47, 0x20000\n"
    "s_mov_b64 s[48:49], s[12:13]\n"
    "s_and_b32 s49, s49, 0xffff\n"
    "s_mov_b32 s50, 131072\n"
    "s_mov_b32 s51, 0x20000\n"
    "v_lshlrev_b32 v2, 4, v0\n"
    "v_lshl_add_u32 v2, v1, 16, v2\n"
    "s_lshl_b32 s90, s91, 19\n"
    "v_add_u32 v244, s90, v2\n"
    "s_mov_b32 s68, 8192\n"
    "s_mov_b32 s69, 16384\n"
    "s_mov_b32 s70, 24576\n"
    "s_mov_b32 s71, 32768\n"
    "s_mov_b32 s72, 40960\n"
    "s_mov_b32 s73, 49152\n"
    "s_mov_b32 s74, 57344\n"
    "v_subrev_u32 v2, 31, v240\n"
    "s_lshl_b32 s90, s91, 1\n"
    "v_add_u32 v3, s90, v2\n"
    "v_mov_b32 v4, 262144\n"
    "v_mov_b32 v5, 4290510848\n"
    "v_cmp_eq_u32 vcc, 18, v3\n"
    "s_nop 1\n"
    "v_cndmask_b32 v4, v4, v5, vcc\n"
    "v_cmp_lt_u32 vcc, 31, v240\n"
    "s_nop 1\n"
    "v_cndmask_b32 v252, v252, v4, vcc\n"
    "s_mov_b32 s95, 32\n"
    "v_lshrrev_b32 v2, 1, v0\n"
    "v_xor_b32 v2, v2, v1\n"
    "v_lshlrev_b32 v2, 5, v2\n"
    "v_lshl_add_u32 v2, v1, 8, v2\n"
    "v_and_b32 v3, 1, v0\n"
    "v_lshl_add_u32 v242, v3, 4, v2\n"
    "v_lshrrev_b32 v2, 1, v0\n"
    "v_xor_b32 v2, v2, v1\n"
    "v_xor_b32 v2, 4, v2\n"
    "v_readlane_b32 s85, v252, s94\n"
    "s_add_u32 s94, s94, 1\n"
    "s_min_u32 m0, s98, 92160\n"
    "s_add_u32 s98, s98, 8192\n"
    "s_nop 1\n"
    "buffer_load_dwordx4 v245, s[36:39], s85 offen offset:0 lds\n"
    "buffer_load_dwordx4 v241, s[36:39], s85 offen offset:1024 lds\n"
    "v_lshlrev_b32 v2, 5, v2\n"
    "v_lshl_add_u32 v2, v1, 8, v2\n"
    "v_and_b32 v3, 1, v0\n"
    "v_lshl_add_u32 v243, v3, 4, v2\n"
    "s_lshl_b32 s87, %2, 13\n"
    "s_add_u32 s99, s87, 131072\n"
    "s_add_u32 s87, s87, 65536\n"
    "s_and_b32 s90, %2, 2\n"
    "s_add_u32 s90, s90, s33\n"
    "s_lshl_b32 s92, s33, 2\n"
    "s_add_u32 s92, s92, %2\n"
    "s_cmp_eq_u32 s7, 0\n"
    "s_cselect_b32 s90, s90, s92\n"
    "s_add_u32 s90, s90, s5\n"
    "s_and_b32 s90, s90, 15\n"
    "s_lshl_b32 s86, s90, 13\n"
    "s_xor_b32 s90, s33, s5\n"
    "s_and_b32 s90, s90, s7\n"
    "s_lshl_b32 s90, s90, 12\n"
    "s_add_u32 s86, s86, s90\n"
    "s_mov_b64 s[52:53], s[16:17]\n"
    "s_and_b32 s53, s53, 0xffff\n"
    "s_mov_b32 s54, 589824\n"
    "s_mov_b32 s55, 0x20000\n"
    "s_mov_b64 s[56:57], s[20:21]\n"
    "s_and_b32 s57, s57, 0xffff\n"
    "s_mov_b32 s58, 737280\n"
    "s_mov_b32 s59, 0x20000\n"
    "s_mov_b64 s[64:65], s[28:29]\n"
    "s_and_b32 s65, s65, 0xffff\n"
    "s_mov_b32 s66, 21888\n"
    "s_mov_b32 s67, 0x20000\n"
    "s_mov_b32 s78, 1040356334\n"
    "s_mov_b32 s79, s78\n"
    "s_mov_b32 s77, 1149239296\n"
    "s_mov_b32 s75, 0x05040100\n"
    "s_mov_b32 s76, 0x07060302\n"
    "v_readlane_b32 s85, v252, s94\n"
    "s_add_u32 s94, s94, 1\n"
    "s_min_u32 m0, s98, 92160\n"
    "s_add_u32 s98, s98, 8192\n"
    "s_nop 1\n"
    "buffer_load_dwordx4 v245, s[36:39], s85 offen offset:0 lds\n"
    "buffer_load_dwordx4 v241, s[36:39], s85 offen offset:1024 lds\n"
    "s_mov_b32 s83, 0\n"
    "s_mov_b32 s82, 5\n"
    "v_lshrrev_b32 v2, 1, v0\n"
    "v_xor_b32 v2, v2, v1\n"
    "v_lshlrev_b32 v2, 4, v2\n"
    "v_lshl_add_u32 v246, v0, 7, v2\n"
    "v_mul_u32_u24 v2, 4608, v1\n"
    "v_lshl_add_u32 v2, v0, 4, v2\n"
    "s_lshl_b32 s90, %2, 8\n"
    "v_add_u32 v248, s90, v2\n"
    "v_lshrrev_b32 v2, 3, v240\n"
    "v_mul_u32_u24 v3, 1152, v2\n"
    "v_bfe_u32 v4, v240, 1, 2\n"
    "v_xor_b32 v4, v4, v1\n"
    "v_lshl_add_u32 v3, v4, 5, v3\n"
    "v_and_b32 v4, 1, v240\n"
    "v_lshl_add_u32 v3, v4, 4, v3\n"
    "v_add_u32 v247, 1024, v3\n"
    "s_mul_i32 s88, %2, 147456\n"
    "s_lshl_b32 s89, %2, 14\n"
    "v_lshrrev_b32 v2, 2, v240\n"
    "v_bfe_u32 v3, v2, 1, 2\n"
    "v_xor_b32 v3, %2, v3\n"
    "v_lshlrev_b32 v3, 5, v3\n"
    "v_lshl_add_u32 v3, v2, 7, v3\n"
    "v_and_b32 v4, 3, v240\n"
    "v_lshl_add_u32 v249, v4, 3, v3\n"
    "v_accvgpr_write_b32 a176, 0\n"
    "v_accvgpr_write_b32 a177, 0\n"
    "v_accvgpr_write_b32 a178, 0\n"
    "v_accvgpr_write_b32 a179, 0\n"
    "v_accvgpr_write_b32 a180, 0\n"
    "v_accvgpr_write_b32 a181, 0\n"
    "v_accvgpr_write_b32 a182, 0\n"
    "v_accvgpr_write_b32 a183, 0\n"
    "v_accvgpr_write_b32 a184, 0\n"
    "v_readlane_b32 s85, v252, s94\n"
    "s_add_u32 s94, s94, 1\n"
    "s_min_u32 m0, s98, 92160\n"
    "s_add_u32 s98, s98, 8192\n"
    "s_nop 1\n"
    "buffer_load_dwordx4 v245, s[36:39], s85 offen offset:0 lds\n"
    "buffer_load_dwordx4 v241, s[36:39], s85 offen offset:1024 lds\n"
    "v_accvgpr_write_b32 a185, 0\n"
    "v_accvgpr_write_b32 a186, 0\n"
    "v_accvgpr_write_b32 a187, 0\n"
    "v_accvgpr_write_b32 a188, 0\n"
    "v_accvgpr_write_b32 a189, 0\n"
    "v_accvgpr_write_b32 a190, 0\n"
    "v_accvgpr_write_b32 a191, 0\n"
    "v_accvgpr_write_b32 a192, 0\n"
    "v_accvgpr_write_b32 a193, 0\n"
    "v_accvgpr_write_b32 a194, 0\n"
    "v_accvgpr_write_b32 a195, 0\n"
    "v_accvgpr_write_b32 a196, 0\n"
    "v_accvgpr_write_b32 a197, 0\n"
    "v_accvgpr_write_b32 a198, 0\n"
    "v_accvgpr_write_b32 a199, 0\n"
    "v_accvgpr_write_b32 a200, 0\n"
    "v_accvgpr_write_b32 a201, 0\n"
    "v_accvgpr_write_b32 a202, 0\n"
    "v_accvgpr_write_b32 a203, 0\n"
    "v_accvgpr_write_b32 a204, 0\n"
    "v_accvgpr_write_b32 a205, 0\n"
    "v_accvgpr_write_b32 a206, 0\n"
    "v_accvgpr_write_b32 a207, 0\n"
    "v_mov_b32 v8, 0\n"
    "v_mov_b32 v9, 0\n"
    "v_mov_b32 v10, 0\n"
    "v_mov_b32 v11, 0\n"
    "v_mov_b32 v12, 0\n"
    "v_mov_b32 v13, 0\n"
    "v_mov_b32 v14, 0\n"
    "v_mov_b32 v15, 0\n"
    "v_mov_b32 v16, 0\n"
    "v_mov_b32 v17, 0\n"
    "v_mov_b32 v18, 0\n"
    "v_mov_b32 v19, 0\n"
    "v_mov_b32 v20, 0\n"
    "v_readlane_b32 s85, v252, s94\n"
    "s_add_u32 s94, s94, 1\n"
    "s_min_u32 m0, s98, 92160\n"
    "s_add_u32 s98, s98, 8192\n"
    "s_nop 1\n"
    "buffer_load_dwordx4 v245, s[36:39], s85 offen offset:0 lds\n"
    "buffer_load_dwordx4 v241, s[36:39], s85 offen offset:1024 lds\n"
    "v_mov_b32 v21, 0\n"
    "v_mov_b32 v22, 0\n"
    "v_mov_b32 v23, 0\n"
    "v_mov_b32 v24, 0\n"
    "v_mov_b32 v25, 0\n"
    "v_mov_b32 v26, 0\n"
    "v_mov_b32 v27, 0\n"
    "v_mov_b32 v28, 0\n"
    "v_mov_b32 v29, 0\n"
    "v_mov_b32 v30, 0\n"
    "v_mov_b32 v31, 0\n"
    "v_mov_b32 v32, 0\n"
    "v_mov_b32 v33, 0\n"
    "v_mov_b32 v34, 0\n"
    "v_mov_b32 v35, 0\n"
    "v_mov_b32 v36, 0\n"
    "v_mov_b32 v37, 0\n"
    "v_mov_b32 v38, 0\n"
    "v_mov_b32 v39, 0\n"
    "v_mov_b32 v40, 0\n"
    "v_mov_b32 v41, 0\n"
    "v_mov_b32 v42, 0\n"
    "v_mov_b32 v43, 0\n"
    "v_mov_b32 v44, 0\n"
    "v_mov_b32 v45, 0\n"
    "v_mov_b32 v46, 0\n"
    "v_mov_b32 v47, 0\n"
    "v_mov_b32 v48, 0\n"
    "v_mov_b32 v49, 0\n"
    "v_mov_b32 v50, 0\n"
    "v_mov_b32 v51, 0\n"
    "v_mov_b32 v52, 0\n"
    "v_mov_b32 v53, 0\n"
    "v_mov_b32 v54, 0\n"
    "v_mov_b32 v55, 0\n"
    "v_mov_b32 v56, 0\n"
    "v_readlane_b32 s85, v252, s94\n"
    "s_add_u32 s94, s94, 1\n"
    "s_min_u32 m0, s98, 92160\n"
    "s_add_u32 s98, s98, 8192\n"
    "s_nop 1\n"
    "buffer_load_dwordx4 v245, s[36:39], s85 offen offset:0 lds\n"
    "buffer_load_dwordx4 v241, s[36:39], s85 offen offset:1024 lds\n"
    "v_mov_b32 v57, 0\n"
    "v_mov_b32 v58, 0\n"
    "v_mov_b32 v59, 0\n"
    "v_mov_b32 v60, 0\n"
    "v_mov_b32 v61, 0\n"
    "v_mov_b32 v62, 0\n"
    "v_mov_b32 v63, 0\n"
    "v_mov_b32 v64, 0\n"
    "v_mov_b32 v65, 0\n"
    "v_mov_b32 v66, 0\n"
    "v_mov_b32 v67, 0\n"
    "v_mov_b32 v68, 0\n"
    "v_mov_b32 v69, 0\n"
    "v_mov_b32 v70, 0\n"
    "v_mov_b32 v71, 0\n"
    "v_mov_b32 v72, 0\n"
    "v_mov_b32 v73, 0\n"
    "v_mov_b32 v74, 0\n"
    "v_mov_b32 v75, 0\n"
    "v_mov_b32 v76, 0\n"
    "v_mov_b32 v77, 0\n"
    "v_mov_b32 v78, 0\n"
    "v_mov_b32 v79, 0\n"
    "v_mov_b32 v80, 0\n"
    "v_mov_b32 v81, 0\n"
    "v_mov_b32 v82, 0\n"
    "v_mov_b32 v83, 0\n"
    "v_mov_b32 v84, 0\n"
    "v_mov_b32 v85, 0\n"
    "v_mov_b32 v86, 0\n"
    "v_mov_b32 v87, 0\n"
    "v_mov_b32 v88, 0\n"
    "v_mov_b32 v89, 0\n"
    "v_mov_b32 v90, 0\n"
    "v_mov_b32 v91, 0\n"
    "v_mov_b32 v92, 0\n"
    "v_readlane_b32 s85, v252, s94\n"
    "s_add_u32 s94, s94, 1\n"
    "s_min_u32 m0, s98, 92160\n"
    "s_add_u32 s98, s98, 8192\n"
    "s_nop 1\n"
    "buffer_load_dwordx4 v245, s[36:39], s85 offen offset:0 lds\n"
    "buffer_load_dwordx4 v241, s[36:39], s85 offen offset:1024 lds\n"
    "v_mov_b32 v93, 0\n"
    "v_mov_b32 v94, 0\n"
    "v_mov_b32 v95, 0\n"
    "v_mov_b32 v96, 0\n"
    "v_mov_b32 v97, 0\n"
    "v_mov_b32 v98, 0\n"
    "v_mov_b32 v99, 0\n"
    "v_mov_b32 v100, 0\n"
    "v_mov_b32 v101, 0\n"
    "v_mov_b32 v102, 0\n"
    "v_mov_b32 v103, 0\n"
    "v_mov_b32 v104, 0\n"
    "v_mov_b32 v105, 0\n"
    "v_mov_b32 v106, 0\n"
    "v_mov_b32 v107, 0\n"
    "v_mov_b32 v108, 0\n"
    "v_mov_b32 v109, 0\n"
    "v_mov_b32 v110, 0\n"
    "v_mov_b32 v111, 0\n"
    "v_mov_b32 v112, 0\n"
    "v_mov_b32 v113, 0\n"
    "v_mov_b32 v114, 0\n"
    "v_mov_b32 v115, 0\n"
    "v_mov_b32 v116, 0\n"
    "v_mov_b32 v117, 0\n"
    "v_mov_b32 v118, 0\n"
    "v_mov_b32 v119, 0\n"
    "v_mov_b32 v120, 0\n"
    "v_mov_b32 v121, 0\n"
    "v_mov_b32 v122, 0\n"
    "v_mov_b32 v123, 0\n"
    "v_mov_b32 v124, 0\n"
    "v_mov_b32 v125, 0\n"
    "v_mov_b32 v126, 0\n"
    "v_mov_b32 v127, 0\n"
    "v_readlane_b32 s85, v252, s94\n"
    "s_add_u32 s94, s94, 1\n"
    "s_min_u32 m0, s98, 92160\n"
    "s_add_u32 s98, s98, 8192\n"
    "s_nop 1\n"
    "buffer_load_dwordx4 v245, s[36:39], s85 offen offset:0 lds\n"
    "buffer_load_dwordx4 v241, s[36:39], s85 offen offset:1024 lds\n"
    "s_mov_b32 s6, s7\n"
    "s_cmp_lg_u32 s7, 0\n"
    "s_cbranch_scc1 L_notouch%=\n"
    "s_memtime s[96:97]\n"
    "s_memrealtime s[34:35]\n"
    "s_mov_b64 s[36:37], s[26:27]\n"
    "s_and_b32 s37, s37, 0xffff\n"
    "s_mov_b32 s38, 589824\n"
    "s_mov_b32 s39, 0x20000\n"
    "s_and_b32 s90, s33, 3\n"
    "s_lshl_b32 s90, s90, 2\n"
    "s_lshr_b32 s90, 8448, s90\n"
    "s_and_b32 s90, s90, 15\n"
    "s_and_b32 s92, s33, 3\n"
    "s_lshl_b32 s92, s92, 2\n"
    "s_lshr_b32 s92, 8464, s92\n"
    "s_and_b32 s92, s92, 15\n"
    "s_and_b32 s93, s33, 12\n"
    "s_add_u32 s90, s90, s93\n"
    "s_lshl_b32 s90, s90, 2\n"
    "s_add_u32 s90, s90, %2\n"
    "s_mul_i32 s90, s90, 9216\n"
    "v_lshrrev_b32 v6, 3, v240\n"
    "v_mul_u32_u24 v6, 1152, v6\n"
    "v_and_b32 v7, 7, v240\n"
    "v_lshl_add_u32 v7, v7, 7, v6\n"
    "L_wot%=:\n"
    "s_cmp_eq_u32 s92, 0\n"
    "s_cbranch_scc1 L_wotx%=\n"
    "buffer_load_dword v254, v7, s[52:55], s90 offen\n"
    "buffer_load_dword v254, v6, s[52:55], s90 offen offset:1024\n"
    "buffer_load_dword v254, v7, s[36:39], s90 offen\n"
    "buffer_load_dword v254, v6, s[36:39], s90 offen offset:1024\n"
    "s_add_u32 s90, s90, 36864\n"
    "s_sub_u32 s92, s92, 1\n"
    "s_branch L_wot%=\n"
    "L_wotx%=:\n"
    "s_and_b32 s90, %2, 1\n"
    "s_cmp_eq_u32 s90, 0\n"
    "s_cselect_b64 s[36:37], s[44:45], s[48:49]\n"
    "s_cselect_b32 s93, 1, 1\n"
    "s_mov_b32 s38, 131072\n"
    "v_lshlrev_b32 v6, 7, v240\n"
    "s_and_b32 s92, s33, 3\n"
    "s_lshl_b32 s92, s92, 2\n"
    "s_lshr_b32 s92, 17168, s92\n"
    "s_and_b32 s92, s92, 15\n"
    "L_kvt%=:\n"
    "s_cmp_eq_u32 s92, 0\n"
    "s_cbranch_scc1 L_kvtx%=\n"
    "s_mul_i32 s90, s92, 1\n"
    "s_add_u32 s90, s90, s93\n"
    "s_lshl_b32 s90, s90, 13\n"
    "s_add_u32 s90, s90, s86\n"
    "s_and_b32 s90, s90, 0x1ffff\n"
    "buffer_load_dword v254, v6, s[36:39], s90 offen\n"
    "s_sub_u32 s92, s92, 1\n"
    "s_branch L_kvt%=\n"
    "L_kvtx%=:\n"
    "v_lshrrev_b32 v6, 4, v240\n"
    "v_and_b32 v7, 15, v240\n"
    "v_lshlrev_b32 v7, 4, v7\n"
    "v_lshl_add_u32 v7, v6, 8, v7\n"
    "v_lshlrev_b32 v6, 6, v6\n"
    "v_xor_b32 v2, v6, v7\n"
    "v_xor_b32 v3, 16, v2\n"
    "v_xor_b32 v4, 32, v2\n"
    "v_xor_b32 v5, 48, v2\n"
    "s_lshr_b32 s92, %2, 1\n"
    "s_lshl_b32 s92, s92, 14\n"
    "s_and_b32 s90, %2, 1\n"
    "s_lshl_b32 s90, s90, 12\n"
    "s_add_u32 s92, s92, s90\n"
    "s_add_u32 s90, s90, s86\n"
    "s_add_u32 m0, s92, 131072\n"
    "s_nop 0\n"
    "buffer_load_dwordx4 v2, s[44:47], s90 offen offset:0 nt lds\n"
    "buffer_load_dwordx4 v3, s[44:47], s90 offen offset:1024 nt lds\n"
    "buffer_load_dwordx4 v4, s[44:47], s90 offen offset:2048 nt lds\n"
    "buffer_load_dwordx4 v5, s[44:47], s90 offen offset:3072 nt lds\n"
    "s_add_u32 m0, s92, 98304\n"
    "s_nop 0\n"
    "buffer_load_dwordx4 v242, s[48:51], s90 offen offset:0 nt lds\n"
    "buffer_load_dwordx4 v243, s[48:51], s90 offen offset:1024 nt lds\n"
    "buffer_load_dwordx4 v242, s[48:51], s90 offen offset:2048 nt lds\n"
    "buffer_load_dwordx4 v243, s[48:51], s90 offen offset:3072 nt lds\n"
    "s_add_u32 m0, s92, 139264\n"
    "s_add_u32 s90, s90, 8192\n"
    "s_and_b32 s90, s90, 0x1ffff\n"
    "buffer_load_dwordx4 v2, s[44:47], s90 offen offset:0 nt lds\n"
    "buffer_load_dwordx4 v3, s[44:47], s90 offen offset:1024 nt lds\n"
    "buffer_load_dwordx4 v4, s[44:47], s90 offen offset:2048 nt lds\n"
    "buffer_load_dwordx4 v5, s[44:47], s90 offen offset:3072 nt lds\n"
    "s_add_u32 m0, s92, 106496\n"
    "s_nop 0\n"
    "buffer_load_dwordx4 v242, s[48:51], s90 offen offset:0 nt lds\n"
    "buffer_load_dwordx4 v243, s[48:51], s90 offen offset:1024 nt lds\n"
    "buffer_load_dwordx4 v242, s[48:51], s90 offen offset:2048 nt lds\n"
    "buffer_load_dwordx4 v243, s[48:51], s90 offen offset:3072 nt lds\n"
    "s_waitcnt lgkmcnt(0)\n"
    "s_memtime s[90:91]\n"
    "s_memrealtime s[92:93]\n"
    "s_waitcnt lgkmcnt(0)\n"
    "s_sub_u32 s90, s90, s96\n"
    "s_sub_u32 s92, s92, s34\n"
    "s_mul_i32 s90, s90, 10\n"
    "s_mul_i32 s92, s92, 178\n"
    "s_cmp_lt_u32 s90, s92\n"
    "s_cselect_b32 s6, 1, 0\n"
    "L_notouch%=:\n"
    "buffer_load_dwordx4 v[160:163], v244, s[40:43], 0 offen\n"
    "buffer_load_dwordx4 v[164:167], v244, s[40:43], s68 offen\n"
    "buffer_load_dwordx4 v[168:171], v244, s[40:43], s69 offen\n"
    "buffer_load_dwordx4 v[172:175], v244, s[40:43], s70 offen\n"
    "buffer_load_dwordx4 v[176:179], v244, s[40:43], s71 offen\n"
    "buffer_load_dwordx4 v[180:183], v244, s[40:43], s72 offen\n"
    "buffer_load_dwordx4 v[184:187], v244, s[40:43], s73 offen\n"
    "buffer_load_dwordx4 v[188:191], v244, s[40:43], s74 offen\n"
    "v_readlane_b32 s84, v252, s95\n"
    "s_add_u32 s95, s95, 1\n"
    "s_nop 0\n"
    "v_add_u32 v244, s84, v244\n"
    "v_mov_b32 v0, 0\n"
    "v_mov_b32 v1, 0\n"
    "v_mov_b32 v2, 0\n"
    "v_mov_b32 v3, 0\n"
    "v_mov_b32 v4, 0\n"
    "v_mov_b32 v5, 0\n"
    "v_mov_b32 v6, 0\n"
    "v_mov_b32 v7, 0\n"
    "buffer_load_dwordx4 v[192:195], v244, s[40:43], 0 offen\n"
    "buffer_load_dwordx4 v[196:199], v244, s[40:43], s68 offen\n"
    "buffer_load_dwordx4 v[200:203], v244, s[40:43], s69 offen\n"
    "buffer_load_dwordx4 v[204:207], v244, s[40:43], s70 offen\n"
    "buffer_load_dwordx4 v[208:211], v244, s[40:43], s71 offen\n"
    "buffer_load_dwordx4 v[212:215], v244, s[40:43], s72 offen\n"
    "buffer_load_dwordx4 v[216:219], v244, s[40:43], s73 offen\n"
    "buffer_load_dwordx4 v[220:223], v244, s[40:43], s74 offen\n"
    "v_readlane_b32 s84, v252, s95\n"
    "s_add_u32 s95, s95, 1\n"
    "s_nop 0\n"
    "v_add_u32 v244, s84, v244\n"
    "buffer_load_dwordx4 v[128:131], v244, s[40:43], 0 offen\n"
    "buffer_load_dwordx4 v[132:135], v244, s[40:43], s68 offen\n"
    "s_waitcnt vmcnt(18)\n"
    "s_barrier\n"
    "ds_read_b128 a[0:3], v246 offset:0\n"
    "ds_read_b128 a[4:7], v246 offset:2048\n"
    "ds_read_b128 a[8:11], v246 offset:4096\n"
    "ds_read_b128 a[12:15], v246 offset:6144\n"
    "ds_read_b128 a[16:19], v246 offset:8192\n"
    "v_xor_b32 v246, 64, v246\n"
    "v_add_u32 v246, s83, v246\n"
    "s_xor_b32 s83, s83, 10240\n"
    "ds_read_b128 a[20:23], v246 offset:0\n"
    "ds_read_b128 a[24:27], v246 offset:2048\n"
    "ds_read_b128 a[28:31], v246 offset:4096\n"
    "ds_read_b128 a[32:35], v246 offset:6144\n"
    "ds_read_b128 a[36:39], v246 offset:8192\n"
    "v_xor_b32 v246, 64, v246\n"
    "v_add_u32 v246, s83, v246\n"
    "s_xor_b32 s83, s83, 10240\n"
    "v_readfirstlane_b32 s80, v250\n"
    "v_readfirstlane_b32 s81, v251\n"
    "s_add_u32 s80, s80, 0x9e3779b9\n"
    "s_addc_u32 s81, s81, 0x7f4a7c15\n"
    "s_and_b32 s92, s33, 3\n"
    "s_lshl_b32 s92, s92, 2\n"
    "s_lshr_b32 s92, 21520, s92\n"
    "s_and_b32 s92, s92, 15\n"
    "s_cmp_lg_u32 s6, 0\n"
    "s_cselect_b32 s92, 0, s92\n"
    "s_mov_b32 s35, 0\n"
    "s_waitcnt vmcnt(16)\n"
    "v_perm_b32 v224, v164, v160, s75\n"
    "s_waitcnt vmcnt(14)\n"
    "v_perm_b32 v225, v172, v168, s75\n"
    "s_waitcnt vmcnt(12)\n"
    "v_perm_b32 v226, v180, v176, s75\n"
    "s_waitcnt vmcnt(10)\n"
    "v_perm_b32 v227, v188, v184, s75\n"
    "v_perm_b32 v228, v164, v160, s76\n"
    "v_perm_b32 v229, v172, v168, s76\n"
    "v_perm_b32 v230, v180, v176, s76\n"
    "v_perm_b32 v231, v188, v184, s76\n"
    "s_waitcnt lgkmcnt(0)\n"
    "L_p1%=:\n"
    "v_mfma_f32_16x16x32_bf16 v[0:3], v[224:227], a[0:3], v[0:3]\n"
    "v_perm_b32 v232, v165, v161, s75\n"
    "v_perm_b32 v233, v173, v169, s75\n"
    "v_mfma_f32_16x16x32_bf16 v[32:35], v[224:227], a[4:7], v[32:35]\n"
    "v_perm_b32 v234, v181, v177, s75\n"
    "v_perm_b32 v235, v189, v185, s75\n"
    "v_mfma_f32_16x16x32_bf16 v[64:67], v[224:227], a[8:11], v[64:67]\n"
    "buffer_load_dwordx4 v[136:139], v244, s[40:43], s69 offen\n"
    "v_mfma_f32_16x16x32_bf16 v[96:99], v[224:227], a[12:15], v[96:99]\n"
    "v_perm_b32 v236, v165, v161, s76\n"
    "v_perm_b32 v237, v173, v169, s76\n"
    "v_mfma_f32_16x16x32_bf16 a[176:179], v[224:227], a[16:19], a[176:179]\n"
    "v_perm_b32 v238, v181, v177, s76\n"
    "v_perm_b32 v239, v189, v185, s76\n"
    "v_mfma_f32_16x16x32_bf16 v[4:7], v[228:231], a[0:3], v[4:7]\n"
    "v_mfma_f32_16x16x32_bf16 v[36:39], v[228:231], a[4:7], v[36:39]\n"
    "v_mfma_f32_16x16x32_bf16 v[68:71], v[228:231], a[8:11], v[68:71]\n"
    "buffer_load_dwordx4 v[140:143], v244, s[40:43], s70 offen\n"
    "v_mfma_f32_16x16x32_bf16 v[100:103], v[228:231], a[12:15], v[100:103]\n"
    "ds_read_b128 a[40:43], v246 offset:0\n"
    "v_mfma_f32_16x16x32_bf16 a[180:183], v[228:231], a[16:19], a[180:183]\n"
    "ds_read_b128 a[44:47], v246 offset:2048\n"
    "v_mfma_f32_16x16x32_bf16 v[8:11], v[232:235], a[0:3], v[8:11]\n"
    "v_perm_b32 v224, v166, v162, s75\n"
    "v_perm_b32 v225, v174, v170, s75\n"
    "v_mfma_f32_16x16x32_bf16 v[40:43], v[232:235], a[4:7], v[40:43]\n"
    "v_perm_b32 v226, v182, v178, s75\n"
    "v_perm_b32 v227, v190, v186, s75\n"
    "v_mfma_f32_16x16x32_bf16 v[72:75], v[232:235], a[8:11], v[72:75]\n"
    "buffer_load_dwordx4 v[144:147], v244, s[40:43], s71 offen\n"
    "v_mfma_f32_16x16x32_bf16 v[104:107], v[232:235], a[12:15], v[104:107]\n"
    "v_perm_b32 v228, v166, v162, s76\n"
    "v_perm_b32 v229, v174, v170, s76\n"
    "v_mfma_f32_16x16x32_bf16 a[184:187], v[232:235], a[16:19], a[184:187]\n"
    "v_perm_b32 v230, v182, v178, s76\n"
    "v_perm_b32 v231, v190, v186, s76\n"
    "v_mfma_f32_16x16x32_bf16 v[12:15], v[236:239], a[0:3], v[12:15]\n"
    "ds_read_b128 a[48:51], v246 offset:4096\n"
    "v_mfma_f32_16x16x32_bf16 v[44:47], v[236:239], a[4:7], v[44:47]\n"
    "ds_read_b128 a[52:55], v246 offset:6144\n"
    "v_mfma_f32_16x16x32_bf16 v[76:79], v[236:239], a[8:11], v[76:79]\n"
    "buffer_load_dwordx4 v[148:151], v244, s[40:43], s72 offen\n"
    "v_mfma_f32_16x16x32_bf16 v[108:111], v[236:239], a[12:15], v[108:111]\n"
    "ds_read_b128 a[56:59], v246 offset:8192\n"
    "v_mfma_f32_16x16x32_bf16 a[188:191], v[236:239], a[16:19], a[188:191]\n"
    "v_readlane_b32 s84, v252, s95\n"
    "s_add_u32 s95, s95, 1\n"
    "v_mfma_f32_16x16x32_bf16 v[16:19], v[224:227], a[0:3], v[16:19]\n"
    "v_perm_b32 v232, v167, v163, s75\n"
    "v_perm_b32 v233, v175, v171, s75\n"
    "v_mfma_f32_16x16x32_bf16 v[48:51], v[224:227], a[4:7], v[48:51]\n"
    "v_perm_b32 v234, v183, v179, s75\n"
    "v_perm_b32 v235, v191, v187, s75\n"
    "v_mfma_f32_16x16x32_bf16 v[80:83], v[224:227], a[8:11], v[80:83]\n"
    "buffer_load_dwordx4 v[152:155], v244, s[40:43], s73 offen\n"
    "v_mfma_f32_16x16x32_bf16 v[112:115], v[224:227], a[12:15], v[112:115]\n"
    "v_perm_b32 v236, v167, v163, s76\n"
    "v_perm_b32 v237, v175, v171, s76\n"
    "v_mfma_f32_16x16x32_bf16 a[192:195], v[224:227], a[16:19], a[192:195]\n"
    "v_perm_b32 v238, v183, v179, s76\n"
    "v_perm_b32 v239, v191, v187, s76\n"
    "v_mfma_f32_16x16x32_bf16 v[20:23], v[228:231], a[0:3], v[20:23]\n"
    "v_mfma_f32_16x16x32_bf16 v[52:55], v[228:231], a[4:7], v[52:55]\n"
    "v_mfma_f32_16x16x32_bf16 v[84:87], v[228:231], a[8:11], v[84:87]\n"
    "buffer_load_dwordx4 v[156:159], v244, s[40:43], s74 offen\n"
    "s_cmp_le_u32 s82, s92\n"
    "v_mfma_f32_16x16x32_bf16 v[116:119], v[228:231], a[12:15], v[116:119]\n"
    "s_cbranch_scc1 L_nq0_%=\n"
    "L_nq0r_%=:\n"
    "v_mfma_f32_16x16x32_bf16 a[196:199], v[228:231], a[16:19], a[196:199]\n"
    "s_waitcnt vmcnt(8)\n"
    "v_add_u32 v244, s84, v244\n"
    "v_mfma_f32_16x16x32_bf16 v[24:27], v[232:235], a[0:3], v[24:27]\n"
    "v_perm_b32 v224, v196, v192, s75\n"
    "v_perm_b32 v225, v204, v200, s75\n"
    "v_mfma_f32_16x16x32_bf16 v[56:59], v[232:235], a[4:7], v[56:59]\n"
    "v_perm_b32 v226, v212, v208, s75\n"
    "v_perm_b32 v227, v220, v216, s75\n"
    "v_mfma_f32_16x16x32_bf16 v[88:91], v[232:235], a[8:11], v[88:91]\n"
    "v_perm_b32 v228, v196, v192, s76\n"
    "v_perm_b32 v229, v204, v200, s76\n"
    "v_mfma_f32_16x16x32_bf16 v[120:123], v[232:235], a[12:15], v[120:123]\n"
    "buffer_load_dwordx4 v[160:163], v244, s[40:43], 0 offen\n"
    "v_mfma_f32_16x16x32_bf16 a[200:203], v[232:235], a[16:19], a[200:203]\n"
    "v_perm_b32 v230, v212, v208, s76\n"
    "v_perm_b32 v231, v220, v216, s76\n"
    "v_mfma_f32_16x16x32_bf16 v[28:31], v[236:239], a[0:3], v[28:31]\n"
    "v_xor_b32 v246, 64, v246\n"
    "v_add_u32 v246, s83, v246\n"
    "v_mfma_f32_16x16x32_bf16 v[60:63], v[236:239], a[4:7], v[60:63]\n"
    "v_mfma_f32_16x16x32_bf16 v[92:95], v[236:239], a[8:11], v[92:95]\n"
    "v_mfma_f32_16x16x32_bf16 v[124:127], v[236:239], a[12:15], v[124:127]\n"
    "buffer_load_dwordx4 v[164:167], v244, s[40:43], s68 offen\n"
    "s_xor_b32 s83, s83, 10240\n"
    "v_mfma_f32_16x16x32_bf16 a[204:207], v[236:239], a[16:19], a[204:207]\n"
    "s_waitcnt lgkmcnt(0)\n"
    "v_mfma_f32_16x16x32_bf16 v[0:3], v[224:227], a[20:23], v[0:3]\n"
    "v_perm_b32 v232, v197, v193, s75\n"
    "v_perm_b32 v233, v205, v201, s75\n"
    "v_mfma_f32_16x16x32_bf16 v[32:35], v[224:227], a[24:27], v[32:35]\n"
    "v_perm_b32 v234, v213, v209, s75\n"
    "v_perm_b32 v235, v221, v217, s75\n"
    "v_mfma_f32_16x16x32_bf16 v[64:67], v[224:227], a[28:31], v[64:67]\n"
    "buffer_load_dwordx4 v[168:171], v244, s[40:43], s69 offen\n"
    "v_mfma_f32_16x16x32_bf16 v[96:99], v[224:227], a[32:35], v[96:99]\n"
    "v_perm_b32 v236, v197, v193, s76\n"
    "v_perm_b32 v237, v205, v201, s76\n"
    "v_mfma_f32_16x16x32_bf16 a[176:179], v[224:227], a[36:39], a[176:179]\n"
    "v_perm_b32 v238, v213, v209, s76\n"
    "v_perm_b32 v239, v221, v217, s76\n"
    "v_mfma_f32_16x16x32_bf16 v[4:7], v[228:231], a[20:23], v[4:7]\n"
    "v_mfma_f32_16x16x32_bf16 v[36:39], v[228:231], a[24:27], v[36:39]\n"
    "v_mfma_f32_16x16x32_bf16 v[68:71], v[228:231], a[28:31], v[68:71]\n"
    "buffer_load_dwordx4 v[172:175], v244, s[40:43], s70 offen\n"
    "v_mfma_f32_16x16x32_bf16 v[100:103], v[228:231], a[32:35], v[100:103]\n"
    "ds_read_b128 a[0:3], v246 offset:0\n"
    "v_mfma_f32_16x16x32_bf16 a[180:183], v[228:231], a[36:39], a[180:183]\n"
    "ds_read_b128 a[4:7], v246 offset:2048\n"
    "v_mfma_f32_16x16x32_bf16 v[8:11], v[232:235], a[20:23], v[8:11]\n"
    "v_perm_b32 v224, v198, v194, s75\n"
    "v_perm_b32 v225, v206, v202, s75\n"
    "v_mfma_f32_16x16x32_bf16 v[40:43], v[232:235], a[24:27], v[40:43]\n"
    "v_perm_b32 v226, v214, v210, s75\n"
    "v_perm_b32 v227, v222, v218, s75\n"
    "v_mfma_f32_16x16x32_bf16 v[72:75], v[232:235], a[28:31], v[72:75]\n"
    "buffer_load_dwordx4 v[176:179], v244, s[40:43], s71 offen\n"
    "v_mfma_f32_16x16x32_bf16 v[104:107], v[232:235], a[32:35], v[104:107]\n"
    "v_perm_b32 v228, v198, v194, s76\n"
    "v_perm_b32 v229, v206, v202, s76\n"
    "v_mfma_f32_16x16x32_bf16 a[184:187], v[232:235], a[36:39], a[184:187]\n"
    "v_perm_b32 v230, v214, v210, s76\n"
    "v_perm_b32 v231, v222, v218, s76\n"
    "v_mfma_f32_16x16x32_bf16 v[12:15], v[236:239], a[20:23], v[12:15]\n"
    "ds_read_b128 a[8:11], v246 offset:4096\n"
    "v_mfma_f32_16x16x32_bf16 v[44:47], v[236:239], a[24:27], v[44:47]\n"
    "ds_read_b128 a[12:15], v246 offset:6144\n"
    "v_mfma_f32_16x16x32_bf16 v[76:79], v[236:239], a[28:31], v[76:79]\n"
    "buffer_load_dwordx4 v[180:183], v244, s[40:43], s72 offen\n"
    "v_mfma_f32_16x16x32_bf16 v[108:111], v[236:239], a[32:35], v[108:111]\n"
    "ds_read_b128 a[16:19], v246 offset:8192\n"
    "v_mfma_f32_16x16x32_bf16 a[188:191], v[236:239], a[36:39], a[188:191]\n"
    "v_readlane_b32 s84, v252, s95\n"
    "s_add_u32 s95, s95, 1\n"
    "v_mfma_f32_16x16x32_bf16 v[16:19], v[224:227], a[20:23], v[16:19]\n"
    "v_perm_b32 v232, v199, v195, s75\n"
    "v_perm_b32 v233, v207, v203, s75\n"
    "v_mfma_f32_16x16x32_bf16 v[48:51], v[224:227], a[24:27], v[48:51]\n"
    "v_perm_b32 v234, v215, v211, s75\n"
    "v_perm_b32 v235, v223, v219, s75\n"
    "v_mfma_f32_16x16x32_bf16 v[80:83], v[224:227], a[28:31], v[80:83]\n"
    "buffer_load_dwordx4 v[184:187], v244, s[40:43], s73 offen\n"
    "v_mfma_f32_16x16x32_bf16 v[112:115], v[224:227], a[32:35], v[112:115]\n"
    "v_perm_b32 v236, v199, v195, s76\n"
    "v_perm_b32 v237, v207, v203, s76\n"
    "v_mfma_f32_16x16x32_bf16 a[192:195], v[224:227], a[36:39], a[192:195]\n"
    "v_perm_b32 v238, v215, v211, s76\n"
    "v_perm_b32 v239, v223, v219, s76\n"
    "v_mfma_f32_16x16x32_bf16 v[20:23], v[228:231], a[20:23], v[20:23]\n"
    "v_mfma_f32_16x16x32_bf16 v[52:55], v[228:231], a[24:27], v[52:55]\n"
    "v_mfma_f32_16x16x32_bf16 v[84:87], v[228:231], a[28:31], v[84:87]\n"
    "buffer_load_dwordx4 v[188:191], v244, s[40:43], s74 offen\n"
    "s_cmp_le_u32 s82, s92\n"
    "v_mfma_f32_16x16x32_bf16 v[116:119], v[228:231], a[32:35], v[116:119]\n"
    "s_cbranch_scc1 L_nq1_%=\n"
    "L_nq1r_%=:\n"
    "v_mfma_f32_16x16x32_bf16 a[196:199], v[228:231], a[36:39], a[196:199]\n"
    "s_waitcnt vmcnt(8)\n"
    "v_add_u32 v244, s84, v244\n"
    "v_mfma_f32_16x16x32_bf16 v[24:27], v[232:235], a[20:23], v[24:27]\n"
    "v_perm_b32 v224, v132, v128, s75\n"
    "v_perm_b32 v225, v140, v136, s75\n"
    "v_mfma_f32_16x16x32_bf16 v[56:59], v[232:235], a[24:27], v[56:59]\n"
    "v_perm_b32 v226, v148, v144, s75\n"
    "v_perm_b32 v227, v156, v152, s75\n"
    "v_mfma_f32_16x16x32_bf16 v[88:91], v[232:235], a[28:31], v[88:91]\n"
    "v_perm_b32 v228, v132, v128, s76\n"
    "v_perm_b32 v229, v140, v136, s76\n"
    "v_mfma_f32_16x16x32_bf16 v[120:123], v[232:235], a[32:35], v[120:123]\n"
    "buffer_load_dwordx4 v[192:195], v244, s[40:43], 0 offen\n"
    "v_mfma_f32_16x16x32_bf16 a[200:203], v[232:235], a[36:39], a[200:203]\n"
    "v_perm_b32 v230, v148, v144, s76\n"
    "v_perm_b32 v231, v156, v152, s76\n"
    "v_mfma_f32_16x16x32_bf16 v[28:31], v[236:239], a[20:23], v[28:31]\n"
    "v_xor_b32 v246, 64, v246\n"
    "v_add_u32 v246, s83, v246\n"
    "v_mfma_f32_16x16x32_bf16 v[60:63], v[236:239], a[24:27], v[60:63]\n"
    "v_mfma_f32_16x16x32_bf16 v[92:95], v[236:239], a[28:31], v[92:95]\n"
    "v_mfma_f32_16x16x32_bf16 v[124:127], v[236:239], a[32:35], v[124:127]\n"
    "buffer_load_dwordx4 v[196:199], v244, s[40:43], s68 offen\n"
    "s_xor_b32 s83, s83, 10240\n"
    "v_mfma_f32_16x16x32_bf16 a[204:207], v[236:239], a[36:39], a[204:207]\n"
    "s_waitcnt lgkmcnt(0)\n"
    "v_mfma_f32_16x16x32_bf16 v[0:3], v[224:227], a[40:43], v[0:3]\n"
    "v_perm_b32 v232, v133, v129, s75\n"
    "v_perm_b32 v233, v141, v137, s75\n"
    "v_mfma_f32_16x16x32_bf16 v[32:35], v[224:227], a[44:47], v[32:35]\n"
    "v_perm_b32 v234, v149, v145, s75\n"
    "v_perm_b32 v235, v157, v153, s75\n"
    "v_mfma_f32_16x16x32_bf16 v[64:67], v[224:227], a[48:51], v[64:67]\n"
    "buffer_load_dwordx4 v[200:203], v244, s[40:43], s69 offen\n"
    "v_mfma_f32_16x16x32_bf16 v[96:99], v[224:227], a[52:55], v[96:99]\n"
    "v_perm_b32 v236, v133, v129, s76\n"
    "v_perm_b32 v237, v141, v137, s76\n"
    "v_mfma_f32_16x16x32_bf16 a[176:179], v[224:227], a[56:59], a[176:179]\n"
    "v_perm_b32 v238, v149, v145, s76\n"
    "v_perm_b32 v239, v157, v153, s76\n"
    "v_mfma_f32_16x16x32_bf16 v[4:7], v[228:231], a[40:43], v[4:7]\n"
    "v_mfma_f32_16x16x32_bf16 v[36:39], v[228:231], a[44:47], v[36:39]\n"
    "v_mfma_f32_16x16x32_bf16 v[68:71], v[228:231], a[48:51], v[68:71]\n"
    "buffer_load_dwordx4 v[204:207], v244, s[40:43], s70 offen\n"
    "v_mfma_f32_16x16x32_bf16 v[100:103], v[228:231], a[52:55], v[100:103]\n"
    "ds_read_b128 a[20:23], v246 offset:0\n"
    "v_mfma_f32_16x16x32_bf16 a[180:183], v[228:231], a[56:59], a[180:183]\n"
    "ds_read_b128 a[24:27], v246 offset:2048\n"
    "v_mfma_f32_16x16x32_bf16 v[8:11], v[232:235], a[40:43], v[8:11]\n"
    "v_perm_b32 v224, v134, v130, s75\n"
    "v_perm_b32 v225, v142, v138, s75\n"
    "v_mfma_f32_16x16x32_bf16 v[40:43], v[232:235], a[44:47], v[40:43]\n"
    "v_perm_b32 v226, v150, v146, s75\n"
    "v_perm_b32 v227, v158, v154, s75\n"
    "v_mfma_f32_16x16x32_bf16 v[72:75], v[232:235], a[48:51], v[72:75]\n"
    "buffer_load_dwordx4 v[208:211], v244, s[40:43], s71 offen\n"
    "v_mfma_f32_16x16x32_bf16 v[104:107], v[232:235], a[52:55], v[104:107]\n"
    "v_perm_b32 v228, v134, v130, s76\n"
    "v_perm_b32 v229, v142, v138, s76\n"
    "v_mfma_f32_16x16x32_bf16 a[184:187], v[232:235], a[56:59], a[184:187]\n"
    "v_perm_b32 v230, v150, v146, s76\n"
    "v_perm_b32 v231, v158, v154, s76\n"
    "v_mfma_f32_16x16x32_bf16 v[12:15], v[236:239], a[40:43], v[12:15]\n"
    "ds_read_b128 a[28:31], v246 offset:4096\n"
    "v_mfma_f32_16x16x32_bf16 v[44:47], v[236:239], a[44:47], v[44:47]\n"
    "ds_read_b128 a[32:35], v246 offset:6144\n"
    "v_mfma_f32_16x16x32_bf16 v[76:79], v[236:239], a[48:51], v[76:79]\n"
    "buffer_load_dwordx4 v[212:215], v244, s[40:43], s72 offen\n"
    "v_mfma_f32_16x16x32_bf16 v[108:111], v[236:239], a[52:55], v[108:111]\n"
    "ds_read_b128 a[36:39], v246 offset:8192\n"
    "v_mfma_f32_16x16x32_bf16 a[188:191], v[236:239], a[56:59], a[188:191]\n"
    "v_readlane_b32 s84, v252, s95\n"
    "s_add_u32 s95, s95, 1\n"
    "v_mfma_f32_16x16x32_bf16 v[16:19], v[224:227], a[40:43], v[16:19]\n"
    "v_perm_b32 v232, v135, v131, s75\n"
    "v_perm_b32 v233, v143, v139, s75\n"
    "v_mfma_f32_16x16x32_bf16 v[48:51], v[224:227], a[44:47], v[48:51]\n"
    "v_perm_b32 v234, v151, v147, s75\n"
    "v_perm_b32 v235, v159, v155, s75\n"
    "v_mfma_f32_16x16x32_bf16 v[80:83], v[224:227], a[48:51], v[80:83]\n"
    "buffer_load_dwordx4 v[216:219], v244, s[40:43], s73 offen\n"
    "v_mfma_f32_16x16x32_bf16 v[112:115], v[224:227], a[52:55], v[112:115]\n"
    "v_perm_b32 v236, v135, v131, s76\n"
    "v_perm_b32 v237, v143, v139, s76\n"
    "v_mfma_f32_16x16x32_bf16 a[192:195], v[224:227], a[56:59], a[192:195]\n"
    "v_perm_b32 v238, v151, v147, s76\n"
    "v_perm_b32 v239, v159, v155, s76\n"
    "v_mfma_f32_16x16x32_bf16 v[20:23], v[228:231], a[40:43], v[20:23]\n"
    "v_mfma_f32_16x16x32_bf16 v[52:55], v[228:231], a[44:47], v[52:55]\n"
    "v_mfma_f32_16x16x32_bf16 v[84:87], v[228:231], a[48:51], v[84:87]\n"
    "buffer_load_dwordx4 v[220:223], v244, s[40:43], s74 offen\n"
    "s_cmp_le_u32 s82, s92\n"
    "v_mfma_f32_16x16x32_bf16 v[116:119], v[228:231], a[52:55], v[116:119]\n"
    "s_cbranch_scc1 L_nq2_%=\n"
    "L_nq2r_%=:\n"
    "v_mfma_f32_16x16x32_bf16 a[196:199], v[228:231], a[56:59], a[196:199]\n"
    "s_waitcnt vmcnt(8)\n"
    "v_add_u32 v244, s84, v244\n"
    "v_mfma_f32_16x16x32_bf16 v[24:27], v[232:235], a[40:43], v[24:27]\n"
    "v_perm_b32 v224, v164, v160, s75\n"
    "v_perm_b32 v225, v172, v168, s75\n"
    "v_mfma_f32_16x16x32_bf16 v[56:59], v[232:235], a[44:47], v[56:59]\n"
    "v_perm_b32 v226, v180, v176, s75\n"
    "v_perm_b32 v227, v188, v184, s75\n"
    "v_mfma_f32_16x16x32_bf16 v[88:91], v[232:235], a[48:51], v[88:91]\n"
    "v_perm_b32 v228, v164, v160, s76\n"
    "v_perm_b32 v229, v172, v168, s76\n"
    "v_mfma_f32_16x16x32_bf16 v[120:123], v[232:235], a[52:55], v[120:123]\n"
    "buffer_load_dwordx4 v[128:131], v244, s[40:43], 0 offen\n"
    "s_sub_u32 s82, s82, 1\n"
    "v_mfma_f32_16x16x32_bf16 a[200:203], v[232:235], a[56:59], a[200:203]\n"
    "v_perm_b32 v230, v180, v176, s76\n"
    "v_perm_b32 v231, v188, v184, s76\n"
    "v_mfma_f32_16x16x32_bf16 v[28:31], v[236:239], a[40:43], v[28:31]\n"
    "v_xor_b32 v246, 64, v246\n"
    "v_add_u32 v246, s83, v246\n"
    "v_mfma_f32_16x16x32_bf16 v[60:63], v[236:239], a[44:47], v[60:63]\n"
    "v_mfma_f32_16x16x32_bf16 v[92:95], v[236:239], a[48:51], v[92:95]\n"
    "v_mfma_f32_16x16x32_bf16 v[124:127], v[236:239], a[52:55], v[124:127]\n"
    "buffer_load_dwordx4 v[132:135], v244, s[40:43], s68 offen\n"
    "s_xor_b32 s83, s83, 10240\n"
    "v_mfma_f32_16x16x32_bf16 a[204:207], v[236:239], a[56:59], a[204:207]\n"
    "s_waitcnt lgkmcnt(0)\n"
    "s_cmp_lg_u32 s82, 0\n"
    "s_cbranch_scc1 L_p1%=\n"
    "s_cmp_lg_u32 s7, 0\n"
    "s_cbranch_scc1 L_varB%=\n"
    "v_mfma_f32_16x16x32_bf16 v[0:3], v[224:227], a[0:3], v[0:3]\n"
    "v_perm_b32 v232, v165, v161, s75\n"
    "v_perm_b32 v233, v173, v169, s75\n"
    "v_mfma_f32_16x16x32_bf16 v[32:35], v[224:227], a[4:7], v[32:35]\n"
    "v_perm_b32 v234, v181, v177, s75\n"
    "v_perm_b32 v235, v189, v185, s75\n"
    "v_mfma_f32_16x16x32_bf16 v[64:67], v[224:227], a[8:11], v[64:67]\n"
    "buffer_load_dwordx4 v[136:139], v244, s[40:43], s69 offen\n"
    "v_mfma_f32_16x16x32_bf16 v[96:99], v[224:227], a[12:15], v[96:99]\n"
    "v_perm_b32 v236, v165, v161, s76\n"
    "v_perm_b32 v237, v173, v169, s76\n"
    "v_mfma_f32_16x16x32_bf16 a[176:179], v[224:227], a[16:19], a[176:179]\n"
    "v_perm_b32 v238, v181, v177, s76\n"
    "v_perm_b32 v239, v189, v185, s76\n"
    "v_mfma_f32_16x16x32_bf16 v[4:7], v[228:231], a[0:3], v[4:7]\n"
    "v_mfma_f32_16x16x32_bf16 v[36:39], v[228:231], a[4:7], v[36:39]\n"
    "v_mfma_f32_16x16x32_bf16 v[68:71], v[228:231], a[8:11], v[68:71]\n"
    "buffer_load_dwordx4 v[140:143], v244, s[40:43], s70 offen\n"
    "v_mfma_f32_16x16x32_bf16 v[100:103], v[228:231], a[12:15], v[100:103]\n"
    "ds_read_b128 a[40:43], v246 offset:0\n"
    "v_mfma_f32_16x16x32_bf16 a[180:183], v[228:231], a[16:19], a[180:183]\n"
    "ds_read_b128 a[44:47], v246 offset:2048\n"
    "v_mfma_f32_16x16x32_bf16 v[8:11], v[232:235], a[0:3], v[8:11]\n"
    "v_perm_b32 v224, v166, v162, s75\n"
    "v_perm_b32 v225, v174, v170, s75\n"
    "v_mfma_f32_16x16x32_bf16 v[40:43], v[232:235], a[4:7], v[40:43]\n"
    "v_perm_b32 v226, v182, v178, s75\n"
    "v_perm_b32 v227, v190, v186, s75\n"
    "v_mfma_f32_16x16x32_bf16 v[72:75], v[232:235], a[8:11], v[72:75]\n"
    "buffer_load_dwordx4 v[144:147], v244, s[40:43], s71 offen\n"
    "v_mfma_f32_16x16x32_bf16 v[104:107], v[232:235], a[12:15], v[104:107]\n"
    "v_perm_b32 v228, v166, v162, s76\n"
    "v_perm_b32 v229, v174, v170, s76\n"
    "v_mfma_f32_16x16x32_bf16 a[184:187], v[232:235], a[16:19], a[184:187]\n"
    "v_perm_b32 v230, v182, v178, s76\n"
    "v_perm_b32 v231, v190, v186, s76\n"
    "v_mfma_f32_16x16x32_bf16 v[12:15], v[236:239], a[0:3], v[12:15]\n"
    "ds_read_b128 a[48:51], v246 offset:4096\n"
    "v_mfma_f32_16x16x32_bf16 v[44:47], v[236:239], a[4:7], v[44:47]\n"
    "ds_read_b128 a[52:55], v246 offset:6144\n"
    "v_mfma_f32_16x16x32_bf16 v[76:79], v[236:239], a[8:11], v[76:79]\n"
    "buffer_load_dwordx4 v[148:151], v244, s[40:43], s72 offen\n"
    "v_mfma_f32_16x16x32_bf16 v[108:111], v[236:239], a[12:15], v[108:111]\n"
    "ds_read_b128 a[56:59], v246 offset:8192\n"
    "v_mfma_f32_16x16x32_bf16 a[188:191], v[236:239], a[16:19], a[188:191]\n"
    "v_mfma_f32_16x16x32_bf16 v[16:19], v[224:227], a[0:3], v[16:19]\n"
    "v_perm_b32 v232, v167, v163, s75\n"
    "v_perm_b32 v233, v175, v171, s75\n"
    "v_mfma_f32_16x16x32_bf16 v[48:51], v[224:227], a[4:7], v[48:51]\n"
    "v_perm_b32 v234, v183, v179, s75\n"
    "v_perm_b32 v235, v191, v187, s75\n"
    "v_mfma_f32_16x16x32_bf16 v[80:83], v[224:227], a[8:11], v[80:83]\n"
    "buffer_load_dwordx4 v[152:155], v244, s[40:43], s73 offen\n"
    "v_mfma_f32_16x16x32_bf16 v[112:115], v[224:227], a[12:15], v[112:115]\n"
    "v_perm_b32 v236, v167, v163, s76\n"
    "v_perm_b32 v237, v175, v171, s76\n"
    "v_mfma_f32_16x16x32_bf16 a[192:195], v[224:227], a[16:19], a[192:195]\n"
    "v_perm_b32 v238, v183, v179, s76\n"
    "v_perm_b32 v239, v191, v187, s76\n"
    "v_mfma_f32_16x16x32_bf16 v[20:23], v[228:231], a[0:3], v[20:23]\n"
    "v_mfma_f32_16x16x32_bf16 v[52:55], v[228:231], a[4:7], v[52:55]\n"
    "v_mfma_f32_16x16x32_bf16 v[84:87], v[228:231], a[8:11], v[84:87]\n"
    "buffer_load_dwordx4 v[156:159], v244, s[40:43], s74 offen\n"
    "v_mfma_f32_16x16x32_bf16 v[116:119], v[228:231], a[12:15], v[116:119]\n"
    "v_lshrrev_b32 v160, 4, v240\n"
    "v_and_b32 v161, 15, v240\n"
    "v_mfma_f32_16x16x32_bf16 a[196:199], v[228:231], a[16:19], a[196:199]\n"
    "s_waitcnt vmcnt(8)\n"
    "v_lshlrev_b32 v161, 4, v161\n"
    "v_lshl_add_u32 v161, v160, 8, v161\n"
    "v_mfma_f32_16x16x32_bf16 v[24:27], v[232:235], a[0:3], v[24:27]\n"
    "v_perm_b32 v224, v196, v192, s75\n"
    "v_perm_b32 v225, v204, v200, s75\n"
    "v_lshlrev_b32 v160, 6, v160\n"
    "v_xor_b32 v188, v160, v161\n"
    "v_mfma_f32_16x16x32_bf16 v[56:59], v[232:235], a[4:7], v[56:59]\n"
    "v_perm_b32 v226, v212, v208, s75\n"
    "v_perm_b32 v227, v220, v216, s75\n"
    "v_xor_b32 v189, 16, v188\n"
    "v_xor_b32 v190, 32, v188\n"
    "v_mfma_f32_16x16x32_bf16 v[88:91], v[232:235], a[8:11], v[88:91]\n"
    "v_perm_b32 v228, v196, v192, s76\n"
    "v_perm_b32 v229, v204, v200, s76\n"
    "v_xor_b32 v191, 48, v188\n"
    "s_lshr_b32 s91, %2, 1\n"
    "v_mfma_f32_16x16x32_bf16 v[120:123], v[232:235], a[12:15], v[120:123]\n"
    "v_perm_b32 v230, v212, v208, s76\n"
    "v_perm_b32 v231, v220, v216, s76\n"
    "s_lshl_b32 s91, s91, 14\n"
    "s_and_b32 s71, %2, 1\n"
    "v_mfma_f32_16x16x32_bf16 a[200:203], v[232:235], a[16:19], a[200:203]\n"
    "s_lshl_b32 s71, s71, 12\n"
    "s_add_u32 s92, s91, 131072\n"
    "v_mfma_f32_16x16x32_bf16 v[28:31], v[236:239], a[0:3], v[28:31]\n"
    "s_add_u32 s91, s91, 98304\n"
    "s_add_u32 s74, s92, s71\n"
    "v_mfma_f32_16x16x32_bf16 v[60:63], v[236:239], a[4:7], v[60:63]\n"
    "s_add_u32 s73, s91, s71\n"
    "s_add_u32 s71, s71, s86\n"
    "v_mfma_f32_16x16x32_bf16 v[92:95], v[236:239], a[8:11], v[92:95]\n"
    "s_add_u32 s85, s71, 8192\n"
    "s_add_u32 s71, s71, 8192\n"
    "v_mfma_f32_16x16x32_bf16 v[124:127], v[236:239], a[12:15], v[124:127]\n"
    "v_mfma_f32_16x16x32_bf16 a[204:207], v[236:239], a[16:19], a[204:207]\n"
    "s_waitcnt lgkmcnt(0)\n"
    "v_mfma_f32_16x16x32_bf16 v[0:3], v[224:227], a[20:23], v[0:3]\n"
    "s_barrier\n"
    "v_mfma_f32_16x16x32_bf16 v[32:35], v[224:227], a[24:27], v[32:35]\n"
    "v_perm_b32 v232, v197, v193, s75\n"
    "v_perm_b32 v233, v205, v201, s75\n"
    "s_mov_b32 m0, s89\n"
    "s_add_u32 s89, s89, 1024\n"
    "v_mfma_f32_16x16x32_bf16 v[64:67], v[224:227], a[28:31], v[64:67]\n"
    "buffer_load_dwordx4 v247, s[52:55], s88 offen offset:0 lds\n"
    "v_mfma_f32_16x16x32_bf16 v[96:99], v[224:227], a[32:35], v[96:99]\n"
    "v_perm_b32 v234, v213, v209, s75\n"
    "v_perm_b32 v235, v221, v217, s75\n"
    "v_mfma_f32_16x16x32_bf16 a[176:179], v[224:227], a[36:39], a[176:179]\n"
    "v_perm_b32 v236, v197, v193, s76\n"
    "v_perm_b32 v237, v205, v201, s76\n"
    "v_mfma_f32_16x16x32_bf16 v[4:7], v[228:231], a[20:23], v[4:7]\n"
    "v_perm_b32 v238, v213, v209, s76\n"
    "v_perm_b32 v239, v221, v217, s76\n"
    "s_add_u32 s88, s88, 9216\n"
    "s_mov_b32 m0, s89\n"
    "s_add_u32 s89, s89, 1024\n"
    "v_mfma_f32_16x16x32_bf16 v[36:39], v[228:231], a[24:27], v[36:39]\n"
    "buffer_load_dwordx4 v247, s[52:55], s88 offen offset:0 lds\n"
    "v_mfma_f32_16x16x32_bf16 v[68:71], v[228:231], a[28:31], v[68:71]\n"
    "v_mfma_f32_16x16x32_bf16 v[100:103], v[228:231], a[32:35], v[100:103]\n"
    "v_mfma_f32_16x16x32_bf16 a[180:183], v[228:231], a[36:39], a[180:183]\n"
    "s_add_u32 s88, s88, 9216\n"
    "s_mov_b32 m0, s89\n"
    "s_add_u32 s89, s89, 1024\n"
    "v_mfma_f32_16x16x32_bf16 v[8:11], v[232:235], a[20:23], v[8:11]\n"
    "buffer_load_dwordx4 v247, s[52:55], s88 offen offset:0 lds\n"
    "v_mfma_f32_16x16x32_bf16 v[40:43], v[232:235], a[24:27], v[40:43]\n"
    "v_perm_b32 v224, v198, v194, s75\n"
    "v_perm_b32 v225, v206, v202, s75\n"
    "v_mfma_f32_16x16x32_bf16 v[72:75], v[232:235], a[28:31], v[72:75]\n"
    "v_perm_b32 v226, v214, v210, s75\n"
    "v_perm_b32 v227, v222, v218, s75\n"
    "v_mfma_f32_16x16x32_bf16 v[104:107], v[232:235], a[32:35], v[104:107]\n"
    "v_perm_b32 v228, v198, v194, s76\n"
    "v_perm_b32 v229, v206, v202, s76\n"
    "s_add_u32 s88, s88, 9216\n"
    "s_mov_b32 m0, s89\n"
    "s_add_u32 s89, s89, 1024\n"
    "v_mfma_f32_16x16x32_bf16 a[184:187], v[232:235], a[36:39], a[184:187]\n"
    "buffer_load_dwordx4 v247, s[52:55], s88 offen offset:0 lds\n"
    "v_mfma_f32_16x16x32_bf16 v[12:15], v[236:239], a[20:23], v[12:15]\n"
    "v_perm_b32 v230, v214, v210, s76\n"
    "v_perm_b32 v231, v222, v218, s76\n"
    "v_mfma_f32_16x16x32_bf16 v[44:47], v[236:239], a[24:27], v[44:47]\n"
    "v_mfma_f32_16x16x32_bf16 v[76:79], v[236:239], a[28:31], v[76:79]\n"
    "s_add_u32 s88, s88, 9216\n"
    "s_mov_b32 m0, s89\n"
    "s_add_u32 s89, s89, 1024\n"
    "v_mfma_f32_16x16x32_bf16 v[108:111], v[236:239], a[32:35], v[108:111]\n"
    "buffer_load_dwordx4 v247, s[52:55], s88 offen offset:0 lds\n"
    "v_mfma_f32_16x16x32_bf16 a[188:191], v[236:239], a[36:39], a[188:191]\n"
    "v_mfma_f32_16x16x32_bf16 v[16:19], v[224:227], a[20:23], v[16:19]\n"
    "v_perm_b32 v232, v199, v195, s75\n"
    "v_perm_b32 v233, v207, v203, s75\n"
    "v_mfma_f32_16x16x32_bf16 v[48:51], v[224:227], a[24:27], v[48:51]\n"
    "v_perm_b32 v234, v215, v211, s75\n"
    "v_perm_b32 v235, v223, v219, s75\n"
    "s_add_u32 s88, s88, 9216\n"
    "s_mov_b32 m0, s89\n"
    "s_add_u32 s89, s89, 1024\n"
    "v_mfma_f32_16x16x32_bf16 v[80:83], v[224:227], a[28:31], v[80:83]\n"
    "buffer_load_dwordx4 v247, s[52:55], s88 offen offset:0 lds\n"
    "v_mfma_f32_16x16x32_bf16 v[112:115], v[224:227], a[32:35], v[112:115]\n"
    "v_perm_b32 v236, v199, v195, s76\n"
    "v_perm_b32 v237, v207, v203, s76\n"
    "v_mfma_f32_16x16x32_bf16 a[192:195], v[224:227], a[36:39], a[192:195]\n"
    "v_perm_b32 v238, v215, v211, s76\n"
    "v_perm_b32 v239, v223, v219, s76\n"
    "v_mfma_f32_16x16x32_bf16 v[20:23], v[228:231], a[20:23], v[20:23]\n"
    "s_add_u32 s88, s88, 9216\n"
    "s_mov_b32 m0, s89\n"
    "s_add_u32 s89, s89, 1024\n"
    "v_mfma_f32_16x16x32_bf16 v[52:55], v[228:231], a[24:27], v[52:55]\n"
    "buffer_load_dwordx4 v247, s[52:55], s88 offen offset:0 lds\n"
    "v_mfma_f32_16x16x32_bf16 v[84:87], v[228:231], a[28:31], v[84:87]\n"
    "v_mfma_f32_16x16x32_bf16 v[116:119], v[228:231], a[32:35], v[116:119]\n"
    "v_mfma_f32_16x16x32_bf16 a[196:199], v[228:231], a[36:39], a[196:199]\n"
    "s_waitcnt vmcnt(7)\n"
    "v_mfma_f32_16x16x32_bf16 v[24:27], v[232:235], a[20:23], v[24:27]\n"
    "v_perm_b32 v224, v132, v128, s75\n"
    "v_perm_b32 v225, v140, v136, s75\n"
    "v_mfma_f32_16x16x32_bf16 v[56:59], v[232:235], a[24:27], v[56:59]\n"
    "v_perm_b32 v226, v148, v144, s75\n"
    "v_perm_b32 v227, v156, v152, s75\n"
    "v_mfma_f32_16x16x32_bf16 v[88:91], v[232:235], a[28:31], v[88:91]\n"
    "v_perm_b32 v228, v132, v128, s76\n"
    "v_perm_b32 v229, v140, v136, s76\n"
    "s_add_u32 s88, s88, 9216\n"
    "s_mov_b32 m0, s89\n"
    "s_add_u32 s89, s89, 1024\n"
    "v_mfma_f32_16x16x32_bf16 v[120:123], v[232:235], a[32:35], v[120:123]\n"
    "buffer_load_dwordx4 v247, s[52:55], s88 offen offset:0 lds\n"
    "v_mfma_f32_16x16x32_bf16 a[200:203], v[232:235], a[36:39], a[200:203]\n"
    "v_perm_b32 v230, v148, v144, s76\n"
    "v_perm_b32 v231, v156, v152, s76\n"
    "v_mfma_f32_16x16x32_bf16 v[28:31], v[236:239], a[20:23], v[28:31]\n"
    "v_mfma_f32_16x16x32_bf16 v[60:63], v[236:239], a[24:27], v[60:63]\n"
    "v_mfma_f32_16x16x32_bf16 v[92:95], v[236:239], a[28:31], v[92:95]\n"
    "v_mfma_f32_16x16x32_bf16 v[124:127], v[236:239], a[32:35], v[124:127]\n"
    "v_mfma_f32_16x16x32_bf16 a[204:207], v[236:239], a[36:39], a[204:207]\n"
    "v_mfma_f32_16x16x32_bf16 v[0:3], v[224:227], a[40:43], v[0:3]\n"
    "v_perm_b32 v232, v133, v129, s75\n"
    "v_perm_b32 v233, v141, v137, s75\n"
    "v_mfma_f32_16x16x32_bf16 v[32:35], v[224:227], a[44:47], v[32:35]\n"
    "v_perm_b32 v234, v149, v145, s75\n"
    "v_perm_b32 v235, v157, v153, s75\n"
    "s_add_u32 s88, s88, 9216\n"
    "s_mov_b32 m0, s89\n"
    "s_add_u32 s89, s89, 1024\n"
    "v_mfma_f32_16x16x32_bf16 v[64:67], v[224:227], a[48:51], v[64:67]\n"
    "buffer_load_dwordx4 v247, s[52:55], s88 offen offset:0 lds\n"
    "v_mfma_f32_16x16x32_bf16 v[96:99], v[224:227], a[52:55], v[96:99]\n"
    "v_perm_b32 v236, v133, v129, s76\n"
    "v_perm_b32 v237, v141, v137, s76\n"
    "v_mfma_f32_16x16x32_bf16 a[176:179], v[224:227], a[56:59], a[176:179]\n"
    "v_perm_b32 v238, v149, v145, s76\n"
    "v_perm_b32 v239, v157, v153, s76\n"
    "v_mfma_f32_16x16x32_bf16 v[4:7], v[228:231], a[40:43], v[4:7]\n"
    "v_mfma_f32_16x16x32_bf16 v[36:39], v[228:231], a[44:47], v[36:39]\n"
    "s_add_u32 s88, s88, 9216\n"
    "s_mov_b32 m0, s89\n"
    "s_add_u32 s89, s89, 1024\n"
    "v_mfma_f32_16x16x32_bf16 v[68:71], v[228:231], a[48:51], v[68:71]\n"
    "buffer_load_dwordx4 v247, s[52:55], s88 offen offset:0 lds\n"
    "v_mfma_f32_16x16x32_bf16 v[100:103], v[228:231], a[52:55], v[100:103]\n"
    "v_mfma_f32_16x16x32_bf16 a[180:183], v[228:231], a[56:59], a[180:183]\n"
    "v_mfma_f32_16x16x32_bf16 v[8:11], v[232:235], a[40:43], v[8:11]\n"
    "v_perm_b32 v224, v134, v130, s75\n"
    "v_perm_b32 v225, v142, v138, s75\n"
    "v_mfma_f32_16x16x32_bf16 v[40:43], v[232:235], a[44:47], v[40:43]\n"
    "v_perm_b32 v226, v150, v146, s75\n"
    "v_perm_b32 v227, v158, v154, s75\n"
    "s_add_u32 s88, s88, 9216\n"
    "s_mov_b32 m0, s89\n"
    "s_add_u32 s89, s89, 1024\n"
    "v_mfma_f32_16x16x32_bf16 v[72:75], v[232:235], a[48:51], v[72:75]\n"
    "buffer_load_dwordx4 v247, s[52:55], s88 offen offset:0 lds\n"
    "v_mfma_f32_16x16x32_bf16 v[104:107], v[232:235], a[52:55], v[104:107]\n"
    "v_perm_b32 v228, v134, v130, s76\n"
    "v_perm_b32 v229, v142, v138, s76\n"
    "v_mfma_f32_16x16x32_bf16 a[184:187], v[232:235], a[56:59], a[184:187]\n"
    "v_perm_b32 v230, v150, v146, s76\n"
    "v_perm_b32 v231, v158, v154, s76\n"
    "v_mfma_f32_16x16x32_bf16 v[12:15], v[236:239], a[40:43], v[12:15]\n"
    "v_mfma_f32_16x16x32_bf16 v[44:47], v[236:239], a[44:47], v[44:47]\n"
    "s_add_u32 s88, s88, 9216\n"
    "s_mov_b32 m0, s89\n"
    "s_add_u32 s89, s89, 1024\n"
    "v_mfma_f32_16x16x32_bf16 v[76:79], v[236:239], a[48:51], v[76:79]\n"
    "buffer_load_dwordx4 v247, s[52:55], s88 offen offset:0 lds\n"
    "v_mfma_f32_16x16x32_bf16 v[108:111], v[236:239], a[52:55], v[108:111]\n"
    "v_mfma_f32_16x16x32_bf16 a[188:191], v[236:239], a[56:59], a[188:191]\n"
    "v_mfma_f32_16x16x32_bf16 v[16:19], v[224:227], a[40:43], v[16:19]\n"
    "v_perm_b32 v232, v135, v131, s75\n"
    "v_perm_b32 v233, v143, v139, s75\n"
    "v_mfma_f32_16x16x32_bf16 v[48:51], v[224:227], a[44:47], v[48:51]\n"
    "v_perm_b32 v234, v151, v147, s75\n"
    "v_perm_b32 v235, v159, v155, s75\n"
    "s_add_u32 s88, s88, 9216\n"
    "s_mov_b32 m0, s89\n"
    "s_add_u32 s89, s89, 1024\n"
    "v_mfma_f32_16x16x32_bf16 v[80:83], v[224:227], a[48:51], v[80:83]\n"
    "buffer_load_dwordx4 v247, s[52:55], s88 offen offset:0 lds\n"
    "v_mfma_f32_16x16x32_bf16 v[112:115], v[224:227], a[52:55], v[112:115]\n"
    "v_perm_b32 v236, v135, v131, s76\n"
    "v_perm_b32 v237, v143, v139, s76\n"
    "v_mfma_f32_16x16x32_bf16 a[192:195], v[224:227], a[56:59], a[192:195]\n"
    "v_perm_b32 v238, v151, v147, s76\n"
    "v_perm_b32 v239, v159, v155, s76\n"
    "v_mfma_f32_16x16x32_bf16 v[20:23], v[228:231], a[40:43], v[20:23]\n"
    "v_mfma_f32_16x16x32_bf16 v[52:55], v[228:231], a[44:47], v[52:55]\n"
    "s_add_u32 s88, s88, 9216\n"
    "s_mov_b32 m0, s89\n"
    "s_add_u32 s89, s89, 1024\n"
    "v_mfma_f32_16x16x32_bf16 v[84:87], v[228:231], a[48:51], v[84:87]\n"
    "buffer_load_dwordx4 v247, s[52:55], s88 offen offset:0 lds\n"
    "v_mfma_f32_16x16x32_bf16 v[116:119], v[228:231], a[52:55], v[116:119]\n"
    "v_mfma_f32_16x16x32_bf16 a[196:199], v[228:231], a[56:59], a[196:199]\n"
    "v_mfma_f32_16x16x32_bf16 v[24:27], v[232:235], a[40:43], v[24:27]\n"
    "v_mfma_f32_16x16x32_bf16 v[56:59], v[232:235], a[44:47], v[56:59]\n"
    "s_add_u32 s88, s88, 9216\n"
    "s_mov_b32 m0, s89\n"
    "s_add_u32 s89, s89, 1024\n"
    "v_mfma_f32_16x16x32_bf16 v[88:91], v[232:235], a[48:51], v[88:91]\n"
    "buffer_load_dwordx4 v247, s[52:55], s88 offen offset:0 lds\n"
    "v_mfma_f32_16x16x32_bf16 v[120:123], v[232:235], a[52:55], v[120:123]\n"
    "v_mfma_f32_16x16x32_bf16 a[200:203], v[232:235], a[56:59], a[200:203]\n"
    "v_mfma_f32_16x16x32_bf16 v[28:31], v[236:239], a[40:43], v[28:31]\n"
    "v_mfma_f32_16x16x32_bf16 v[60:63], v[236:239], a[44:47], v[60:63]\n"
    "s_add_u32 s88, s88, 9216\n"
    "s_mov_b32 m0, s89\n"
    "s_add_u32 s89, s89, 1024\n"
    "v_mfma_f32_16x16x32_bf16 v[92:95], v[236:239], a[48:51], v[92:95]\n"
    "buffer_load_dwordx4 v247, s[52:55], s88 offen offset:0 lds\n"
    "v_mfma_f32_16x16x32_bf16 v[124:127], v[236:239], a[52:55], v[124:127]\n"
    "v_mfma_f32_16x16x32_bf16 a[204:207], v[236:239], a[56:59], a[204:207]\n"
    "s_add_u32 m0, s73, 4294934528\n"
    "s_add_u32 s85, s85, 8192\n"
    "s_and_b32 s85, s85, 0x1ffff\n"
    "buffer_load_dwordx4 v242, s[48:51], s85 offen offset:0 nt lds\n"
    "v_pk_mul_f32 v[0:1], v[0:1], s[78:79]\n"
    "v_pk_mul_f32 v[2:3], v[2:3], s[78:79]\n"
    "v_pk_mul_f32 v[4:5], v[4:5], s[78:79]\n"
    "v_pk_mul_f32 v[6:7], v[6:7], s[78:79]\n"
    "v_pk_mul_f32 v[8:9], v[8:9], s[78:79]\n"
    "v_pk_mul_f32 v[10:11], v[10:11], s[78:79]\n"
    "v_pk_mul_f32 v[12:13], v[12:13], s[78:79]\n"
    "v_pk_mul_f32 v[14:15], v[14:15], s[78:79]\n"
    "v_pk_mul_f32 v[16:17], v[16:17], s[78:79]\n"
    "v_pk_mul_f32 v[18:19], v[18:19], s[78:79]\n"
    "v_pk_mul_f32 v[20:21], v[20:21], s[78:79]\n"
    "v_pk_mul_f32 v[22:23], v[22:23], s[78:79]\n"
    "v_pk_mul_f32 v[24:25], v[24:25], s[78:79]\n"
    "v_pk_mul_f32 v[26:27], v[26:27], s[78:79]\n"
    "v_pk_mul_f32 v[28:29], v[28:29], s[78:79]\n"
    "v_pk_mul_f32 v[30:31], v[30:31], s[78:79]\n"
    "v_cvt_pk_bf16_f32 v160, v0, v4\n"
    "v_cvt_pk_bf16_f32 v161, v8, v12\n"
    "v_cvt_pk_bf16_f32 v162, v16, v20\n"
    "v_cvt_pk_bf16_f32 v163, v24, v28\n"
    "v_accvgpr_write_b32 a0, v160\n"
    "v_accvgpr_write_b32 a1, v161\n"
    "v_accvgpr_write_b32 a2, v162\n"
    "v_accvgpr_write_b32 a3, v163\n"
    "v_cvt_pk_bf16_f32 v164, v1, v5\n"
    "v_cvt_pk_bf16_f32 v165, v9, v13\n"
    "v_cvt_pk_bf16_f32 v166, v17, v21\n"
    "v_cvt_pk_bf16_f32 v167, v25, v29\n"
    "v_accvgpr_write_b32 a4, v164\n"
    "v_accvgpr_write_b32 a5, v165\n"
    "v_accvgpr_write_b32 a6, v166\n"
    "v_accvgpr_write_b32 a7, v167\n"
    "v_cvt_pk_bf16_f32 v160, v2, v6\n"
    "v_cvt_pk_bf16_f32 v161, v10, v14\n"
    "v_cvt_pk_bf16_f32 v162, v18, v22\n"
    "v_cvt_pk_bf16_f32 v163, v26, v30\n"
    "v_accvgpr_write_b32 a8, v160\n"
    "v_accvgpr_write_b32 a9, v161\n"
    "v_accvgpr_write_b32 a10, v162\n"
    "v_accvgpr_write_b32 a11, v163\n"
    "v_cvt_pk_bf16_f32 v164, v3, v7\n"
    "v_cvt_pk_bf16_f32 v165, v11, v15\n"
    "v_cvt_pk_bf16_f32 v166, v19, v23\n"
    "v_cvt_pk_bf16_f32 v167, v27, v31\n"
    "v_accvgpr_write_b32 a12, v164\n"
    "v_accvgpr_write_b32 a13, v165\n"
    "v_accvgpr_write_b32 a14, v166\n"
    "v_accvgpr_write_b32 a15, v167\n"
    "v_pk_mul_f32 v[32:33], v[32:33], s[78:79]\n"
    "v_pk_mul_f32 v[34:35], v[34:35], s[78:79]\n"
    "v_pk_mul_f32 v[36:37], v[36:37], s[78:79]\n"
    "v_pk_mul_f32 v[38:39], v[38:39], s[78:79]\n"
    "v_pk_mul_f32 v[40:41], v[40:41], s[78:79]\n"
    "v_pk_mul_f32 v[42:43], v[42:43], s[78:79]\n"
    "buffer_load_dwordx4 v243, s[48:51], s85 offen offset:1024 nt lds\n"
    "v_pk_mul_f32 v[44:45], v[44:45], s[78:79]\n"
    "v_pk_mul_f32 v[46:47], v[46:47], s[78:79]\n"
    "v_pk_mul_f32 v[48:49], v[48:49], s[78:79]\n"
    "v_pk_mul_f32 v[50:51], v[50:51], s[78:79]\n"
    "v_pk_mul_f32 v[52:53], v[52:53], s[78:79]\n"
    "v_pk_mul_f32 v[54:55], v[54:55], s[78:79]\n"
    "v_pk_mul_f32 v[56:57], v[56:57], s[78:79]\n"
    "v_pk_mul_f32 v[58:59], v[58:59], s[78:79]\n"
    "v_pk_mul_f32 v[60:61], v[60:61], s[78:79]\n"
    "v_pk_mul_f32 v[62:63], v[62:63], s[78:79]\n"
    "v_cvt_pk_bf16_f32 v160, v32, v36\n"
    "v_cvt_pk_bf16_f32 v161, v40, v44\n"
    "v_cvt_pk_bf16_f32 v162, v48, v52\n"
    "v_cvt_pk_bf16_f32 v163, v56, v60\n"
    "v_accvgpr_write_b32 a16, v160\n"
    "v_accvgpr_write_b32 a17, v161\n"
    "v_accvgpr_write_b32 a18, v162\n"
    "v_accvgpr_write_b32 a19, v163\n"
    "v_cvt_pk_bf16_f32 v164, v33, v37\n"
    "v_cvt_pk_bf16_f32 v165, v41, v45\n"
    "v_cvt_pk_bf16_f32 v166, v49, v53\n"
    "v_cvt_pk_bf16_f32 v167, v57, v61\n"
    "v_accvgpr_write_b32 a20, v164\n"
    "v_accvgpr_write_b32 a21, v165\n"
    "v_accvgpr_write_b32 a22, v166\n"
    "v_accvgpr_write_b32 a23, v167\n"
    "v_cvt_pk_bf16_f32 v160, v34, v38\n"
    "v_cvt_pk_bf16_f32 v161, v42, v46\n"
    "v_cvt_pk_bf16_f32 v162, v50, v54\n"
    "v_cvt_pk_bf16_f32 v163, v58, v62\n"
    "v_accvgpr_write_b32 a24, v160\n"
    "v_accvgpr_write_b32 a25, v161\n"
    "v_accvgpr_write_b32 a26, v162\n"
    "v_accvgpr_write_b32 a27, v163\n"
    "v_cvt_pk_bf16_f32 v164, v35, v39\n"
    "v_cvt_pk_bf16_f32 v165, v43, v47\n"
    "v_cvt_pk_bf16_f32 v166, v51, v55\n"
    "v_cvt_pk_bf16_f32 v167, v59, v63\n"
    "v_accvgpr_write_b32 a28, v164\n"
    "v_accvgpr_write_b32 a29, v165\n"
    "v_accvgpr_write_b32 a30, v166\n"
    "v_accvgpr_write_b32 a31, v167\n"
    "v_pk_mul_f32 v[64:65], v[64:65], s[78:79]\n"
    "v_pk_mul_f32 v[66:67], v[66:67], s[78:79]\n"
    "v_pk_mul_f32 v[68:69], v[68:69], s[78:79]\n"
    "v_pk_mul_f32 v[70:71], v[70:71], s[78:79]\n"
    "v_pk_mul_f32 v[72:73], v[72:73], s[78:79]\n"
    "v_pk_mul_f32 v[74:75], v[74:75], s[78:79]\n"
    "v_pk_mul_f32 v[76:77], v[76:77], s[78:79]\n"
    "v_pk_mul_f32 v[78:79], v[78:79], s[78:79]\n"
    "v_pk_mul_f32 v[80:81], v[80:81], s[78:79]\n"
    "v_pk_mul_f32 v[82:83], v[82:83], s[78:79]\n"
    "v_pk_mul_f32 v[84:85], v[84:85], s[78:79]\n"
    "v_pk_mul_f32 v[86:87], v[86:87], s[78:79]\n"
    "buffer_load_dwordx4 v242, s[48:51], s85 offen offset:2048 nt lds\n"
    "v_pk_mul_f32 v[88:89], v[88:89], s[78:79]\n"
    "v_pk_mul_f32 v[90:91], v[90:91], s[78:79]\n"
    "v_pk_mul_f32 v[92:93], v[92:93], s[78:79]\n"
    "v_pk_mul_f32 v[94:95], v[94:95], s[78:79]\n"
    "v_cvt_pk_bf16_f32 v160, v64, v68\n"
    "v_cvt_pk_bf16_f32 v161, v72, v76\n"
    "v_cvt_pk_bf16_f32 v162, v80, v84\n"
    "v_cvt_pk_bf16_f32 v163, v88, v92\n"
    "v_accvgpr_write_b32 a32, v160\n"
    "v_accvgpr_write_b32 a33, v161\n"
    "v_accvgpr_write_b32 a34, v162\n"
    "v_accvgpr_write_b32 a35, v163\n"
    "v_cvt_pk_bf16_f32 v164, v65, v69\n"
    "v_cvt_pk_bf16_f32 v165, v73, v77\n"
    "v_cvt_pk_bf16_f32 v166, v81, v85\n"
    "v_cvt_pk_bf16_f32 v167, v89, v93\n"
    "v_accvgpr_write_b32 a36, v164\n"
    "v_accvgpr_write_b32 a37, v165\n"
    "v_accvgpr_write_b32 a38, v166\n"
    "v_accvgpr_write_b32 a39, v167\n"
    "v_cvt_pk_bf16_f32 v160, v66, v70\n"
    "v_cvt_pk_bf16_f32 v161, v74, v78\n"
    "v_cvt_pk_bf16_f32 v162, v82, v86\n"
    "v_cvt_pk_bf16_f32 v163, v90, v94\n"
    "v_accvgpr_write_b32 a40, v160\n"
    "v_accvgpr_write_b32 a41, v161\n"
    "v_accvgpr_write_b32 a42, v162\n"
    "v_accvgpr_write_b32 a43, v163\n"
    "v_cvt_pk_bf16_f32 v164, v67, v71\n"
    "v_cvt_pk_bf16_f32 v165, v75, v79\n"
    "v_cvt_pk_bf16_f32 v166, v83, v87\n"
    "v_cvt_pk_bf16_f32 v167, v91, v95\n"
    "v_accvgpr_write_b32 a44, v164\n"
    "v_accvgpr_write_b32 a45, v165\n"
    "v_accvgpr_write_b32 a46, v166\n"
    "v_accvgpr_write_b32 a47, v167\n"
    "v_pk_mul_f32 v[96:97], v[96:97], s[78:79]\n"
    "v_pk_mul_f32 v[98:99], v[98:99], s[78:79]\n"
    "v_pk_mul_f32 v[100:101], v[100:101], s[78:79]\n"
    "v_pk_mul_f32 v[102:103], v[102:103], s[78:79]\n"
    "v_pk_mul_f32 v[104:105], v[104:105], s[78:79]\n"
    "v_pk_mul_f32 v[106:107], v[106:107], s[78:79]\n"
    "v_pk_mul_f32 v[108:109], v[108:109], s[78:79]\n"
    "v_pk_mul_f32 v[110:111], v[110:111], s[78:79]\n"
    "v_pk_mul_f32 v[112:113], v[112:113], s[78:79]\n"
    "v_pk_mul_f32 v[114:115], v[114:115], s[78:79]\n"
    "v_pk_mul_f32 v[116:117], v[116:117], s[78:79]\n"
    "v_pk_mul_f32 v[118:119], v[118:119], s[78:79]\n"
    "v_pk_mul_f32 v[120:121], v[120:121], s[78:79]\n"
    "v_pk_mul_f32 v[122:123], v[122:123], s[78:79]\n"
    "v_pk_mul_f32 v[124:125], v[124:125], s[78:79]\n"
    "v_pk_mul_f32 v[126:127], v[126:127], s[78:79]\n"
    "v_cvt_pk_bf16_f32 v160, v96, v100\n"
    "v_cvt_pk_bf16_f32 v161, v104, v108\n"
    "buffer_load_dwordx4 v243, s[48:51], s85 offen offset:3072 nt lds\n"
    "v_cvt_pk_bf16_f32 v162, v112, v116\n"
    "v_cvt_pk_bf16_f32 v163, v120, v124\n"
    "v_accvgpr_write_b32 a48, v160\n"
    "v_accvgpr_write_b32 a49, v161\n"
    "v_accvgpr_write_b32 a50, v162\n"
    "v_accvgpr_write_b32 a51, v163\n"
    "v_cvt_pk_bf16_f32 v164, v97, v101\n"
    "v_cvt_pk_bf16_f32 v165, v105, v109\n"
    "v_cvt_pk_bf16_f32 v166, v113, v117\n"
    "v_cvt_pk_bf16_f32 v167, v121, v125\n"
    "v_accvgpr_write_b32 a52, v164\n"
    "v_accvgpr_write_b32 a53, v165\n"
    "v_accvgpr_write_b32 a54, v166\n"
    "v_accvgpr_write_b32 a55, v167\n"
    "v_cvt_pk_bf16_f32 v160, v98, v102\n"
    "v_cvt_pk_bf16_f32 v161, v106, v110\n"
    "v_cvt_pk_bf16_f32 v162, v114, v118\n"
    "v_cvt_pk_bf16_f32 v163, v122, v126\n"
    "v_accvgpr_write_b32 a56, v160\n"
    "v_accvgpr_write_b32 a57, v161\n"
    "v_accvgpr_write_b32 a58, v162\n"
    "v_accvgpr_write_b32 a59, v163\n"
    "v_cvt_pk_bf16_f32 v164, v99, v103\n"
    "v_cvt_pk_bf16_f32 v165, v107, v111\n"
    "v_cvt_pk_bf16_f32 v166, v115, v119\n"
    "v_cvt_pk_bf16_f32 v167, v123, v127\n"
    "v_accvgpr_write_b32 a60, v164\n"
    "v_accvgpr_write_b32 a61, v165\n"
    "v_accvgpr_write_b32 a62, v166\n"
    "v_accvgpr_write_b32 a63, v167\n"
    "v_accvgpr_read_b32 v128, a176\n"
    "v_accvgpr_read_b32 v129, a177\n"
    "v_accvgpr_read_b32 v130, a178\n"
    "v_accvgpr_read_b32 v131, a179\n"
    "v_accvgpr_read_b32 v132, a180\n"
    "v_accvgpr_read_b32 v133, a181\n"
    "v_accvgpr_read_b32 v134, a182\n"
    "v_accvgpr_read_b32 v135, a183\n"
    "v_accvgpr_read_b32 v136, a184\n"
    "v_accvgpr_read_b32 v137, a185\n"
    "v_accvgpr_read_b32 v138, a186\n"
    "v_accvgpr_read_b32 v139, a187\n"
    "v_accvgpr_read_b32 v140, a188\n"
    "v_accvgpr_read_b32 v141, a189\n"
    "v_accvgpr_read_b32 v142, a190\n"
    "v_accvgpr_read_b32 v143, a191\n"
    "v_accvgpr_read_b32 v144, a192\n"
    "v_accvgpr_read_b32 v145, a193\n"
    "v_accvgpr_read_b32 v146, a194\n"
    "v_accvgpr_read_b32 v147, a195\n"
    "v_accvgpr_read_b32 v148, a196\n"
    "v_accvgpr_read_b32 v149, a197\n"
    "v_accvgpr_read_b32 v150, a198\n"
    "v_accvgpr_read_b32 v151, a199\n"
    "v_accvgpr_read_b32 v152, a200\n"
    "v_accvgpr_read_b32 v153, a201\n"
    "v_accvgpr_read_b32 v154, a202\n"
    "v_accvgpr_read_b32 v155, a203\n"
    "v_accvgpr_read_b32 v156, a204\n"
    "v_accvgpr_read_b32 v157, a205\n"
    "v_accvgpr_read_b32 v158, a206\n"
    "v_accvgpr_read_b32 v159, a207\n"
    "v_pk_mul_f32 v[128:129], v[128:129], s[78:79]\n"
    "v_pk_mul_f32 v[130:131], v[130:131], s[78:79]\n"
    "v_pk_mul_f32 v[132:133], v[132:133], s[78:79]\n"
    "v_pk_mul_f32 v[134:135], v[134:135], s[78:79]\n"
    "v_pk_mul_f32 v[136:137], v[136:137], s[78:79]\n"
    "v_pk_mul_f32 v[138:139], v[138:139], s[78:79]\n"
    "v_pk_mul_f32 v[140:141], v[140:141], s[78:79]\n"
    "v_pk_mul_f32 v[142:143], v[142:143], s[78:79]\n"
    "v_pk_mul_f32 v[144:145], v[144:145], s[78:79]\n"
    "v_pk_mul_f32 v[146:147], v[146:147], s[78:79]\n"
    "v_pk_mul_f32 v[148:149], v[148:149], s[78:79]\n"
    "v_pk_mul_f32 v[150:151], v[150:151], s[78:79]\n"
    "v_pk_mul_f32 v[152:153], v[152:153], s[78:79]\n"
    "v_pk_mul_f32 v[154:155], v[154:155], s[78:79]\n"
    "v_pk_mul_f32 v[156:157], v[156:157], s[78:79]\n"
    "v_pk_mul_f32 v[158:159], v[158:159], s[78:79]\n"
    "v_cvt_pk_bf16_f32 v160, v128, v132\n"
    "v_cvt_pk_bf16_f32 v161, v136, v140\n"
    "v_cvt_pk_bf16_f32 v162, v144, v148\n"
    "v_cvt_pk_bf16_f32 v163, v152, v156\n"
    "v_accvgpr_write_b32 a64, v160\n"
    "v_accvgpr_write_b32 a65, v161\n"
    "v_accvgpr_write_b32 a66, v162\n"
    "v_accvgpr_write_b32 a67, v163\n"
    "v_cvt_pk_bf16_f32 v164, v129, v133\n"
    "v_cvt_pk_bf16_f32 v165, v137, v141\n"
    "v_cvt_pk_bf16_f32 v166, v145, v149\n"
    "v_cvt_pk_bf16_f32 v167, v153, v157\n"
    "v_accvgpr_write_b32 a68, v164\n"
    "v_accvgpr_write_b32 a69, v165\n"
    "v_accvgpr_write_b32 a70, v166\n"
    "v_accvgpr_write_b32 a71, v167\n"
    "v_cvt_pk_bf16_f32 v160, v130, v134\n"
    "v_cvt_pk_bf16_f32 v161, v138, v142\n"
    "v_cvt_pk_bf16_f32 v162, v146, v150\n"
    "v_cvt_pk_bf16_f32 v163, v154, v158\n"
    "v_accvgpr_write_b32 a72, v160\n"
    "v_accvgpr_write_b32 a73, v161\n"
    "v_accvgpr_write_b32 a74, v162\n"
    "v_accvgpr_write_b32 a75, v163\n"
    "v_cvt_pk_bf16_f32 v164, v131, v135\n"
    "v_cvt_pk_bf16_f32 v165, v139, v143\n"
    "v_cvt_pk_bf16_f32 v166, v147, v151\n"
    "v_cvt_pk_bf16_f32 v167, v155, v159\n"
    "v_accvgpr_write_b32 a76, v164\n"
    "v_accvgpr_write_b32 a77, v165\n"
    "v_accvgpr_write_b32 a78, v166\n"
    "v_accvgpr_write_b32 a79, v167\n"
    "v_lshrrev_b32 v237, 4, v240\n"
    "v_xor_b32 v237, v237, v240\n"
    "v_and_b32 v237, 3, v237\n"
    "v_lshlrev_b32 v237, 6, v237\n"
    "v_bfe_u32 v238, v240, 2, 2\n"
    "v_lshl_add_u32 v237, v238, 4, v237\n"
    "v_and_b32 v238, 15, v240\n"
    "v_lshl_add_u32 v237, v238, 8, v237\n"
    "v_add_u32 v220, s92, v237\n"
    "v_xor_b32 v221, 16, v220\n"
    "v_xor_b32 v222, 32, v220\n"
    "v_xor_b32 v223, 48, v220\n"
    "v_lshrrev_b32 v237, 2, v240\n"
    "v_and_b32 v238, 7, v237\n"
    "v_lshlrev_b32 v238, 5, v238\n"
    "v_lshl_add_u32 v238, v237, 8, v238\n"
    "v_and_b32 v237, 3, v240\n"
    "v_lshl_add_u32 v238, v237, 3, v238\n"
    "v_add_u32 v212, s91, v238\n"
    "v_xor_b32 v213, 32, v212\n"
    "v_xor_b32 v214, 64, v212\n"
    "v_xor_b32 v215, 96, v212\n"
    "v_xor_b32 v216, 128, v212\n"
    "v_xor_b32 v217, 160, v212\n"
    "v_xor_b32 v218, 192, v212\n"
    "v_xor_b32 v219, 224, v212\n"
    "ds_read_b128 a[112:115], v220 offset:0\n"
    "ds_read_b128 a[128:131], v220 offset:4096\n"
    "ds_read_b128 a[116:119], v221 offset:0\n"
    "ds_read_b128 a[132:135], v221 offset:4096\n"
    "ds_read_b128 a[120:123], v222 offset:0\n"
    "ds_read_b128 a[136:139], v222 offset:4096\n"
    "ds_read_b128 a[124:127], v223 offset:0\n"
    "ds_read_b128 a[140:143], v223 offset:4096\n"
    "s_waitcnt lgkmcnt(0)\n"
    "s_barrier\n"
    "s_add_u32 m0, s74, 0\n"
    "s_add_u32 s71, s71, 8192\n"
    "s_and_b32 s71, s71, 0x1ffff\n"
    "v_mfma_f32_16x16x32_bf16 v[160:163], a[112:115], a[0:3], 0\n"
    "buffer_load_dwordx4 v188, s[44:47], s71 offen offset:0 nt lds\n"
    "v_mfma_f32_16x16x32_bf16 v[164:167], a[128:131], a[0:3], 0\n"
    "buffer_load_dwordx4 v189, s[44:47], s71 offen offset:1024 nt lds\n"
    "v_mfma_f32_16x16x32_bf16 v[160:163], a[116:119], a[4:7], v[160:163]\n"
    "buffer_load_dwordx4 v190, s[44:47], s71 offen offset:2048 nt lds\n"
    "v_mfma_f32_16x16x32_bf16 v[164:167], a[132:135], a[4:7], v[164:167]\n"
    "buffer_load_dwordx4 v191, s[44:47], s71 offen offset:3072 nt lds\n"
    "v_mfma_f32_16x16x32_bf16 v[160:163], a[120:123], a[8:11], v[160:163]\n"
    "v_mfma_f32_16x16x32_bf16 v[164:167], a[136:139], a[8:11], v[164:167]\n"
    "v_mfma_f32_16x16x32_bf16 v[160:163], a[124:127], a[12:15], v[160:163]\n"
    "v_mfma_f32_16x16x32_bf16 v[164:167], a[140:143], a[12:15], v[164:167]\n"
    "v_mfma_f32_16x16x32_bf16 v[168:171], a[112:115], a[16:19], 0\n"
    "ds_read_b64_tr_b16 a[80:81], v212 offset:0\n"
    "s_nop 4\n"
    "v_max3_f32 v237, v160, v161, v162\n"
    "v_max3_f32 v238, v163, v164, v165\n"
    "v_max3_f32 v237, v237, v166, v167\n"
    "v_max_f32 v237, v237, v238\n"
    "v_mov_b32 v238, v237\n"
    "s_nop 1\n"
    "v_permlane16_swap_b32 v237, v238\n"
    "v_mfma_f32_16x16x32_bf16 v[172:175], a[128:131], a[16:19], 0\n"
    "ds_read_b64_tr_b16 a[82:83], v212 offset:4096\n"
    "v_max_f32 v237, v237, v238\n"
    "v_mov_b32 v238, v237\n"
    "s_nop 1\n"
    "v_permlane32_swap_b32 v237, v238\n"
    "v_max_f32 v237, v237, v238\n"
    "v_sub_f32 v160, v160, v237\n"
    "v_sub_f32 v161, v161, v237\n"
    "v_mfma_f32_16x16x32_bf16 v[168:171], a[116:119], a[20:23], v[168:171]\n"
    "ds_read_b64_tr_b16 a[84:85], v213 offset:0\n"
    "v_sub_f32 v162, v162, v237\n"
    "v_sub_f32 v163, v163, v237\n"
    "v_sub_f32 v164, v164, v237\n"
    "v_sub_f32 v165, v165, v237\n"
    "v_sub_f32 v166, v166, v237\n"
    "v_sub_f32 v167, v167, v237\n"
    "v_mfma_f32_16x16x32_bf16 v[172:175], a[132:135], a[20:23], v[172:175]\n"
    "ds_read_b64_tr_b16 a[86:87], v213 offset:4096\n"
    "v_exp_f32 v176, v160\n"
    "v_exp_f32 v177, v161\n"
    "v_exp_f32 v178, v162\n"
    "v_exp_f32 v179, v163\n"
    "v_exp_f32 v180, v164\n"
    "v_exp_f32 v181, v165\n"
    "v_mfma_f32_16x16x32_bf16 v[168:171], a[120:123], a[24:27], v[168:171]\n"
    "ds_read_b64_tr_b16 a[88:89], v214 offset:0\n"
    "v_exp_f32 v182, v166\n"
    "v_exp_f32 v183, v167\n"
    "v_sub_f32 v192, 0, v237\n"
    "v_sub_f32 v193, 0, v237\n"
    "v_sub_f32 v194, 0, v237\n"
    "v_sub_f32 v195, 0, v237\n"
    "v_mfma_f32_16x16x32_bf16 v[172:175], a[136:139], a[24:27], v[172:175]\n"
    "ds_read_b64_tr_b16 a[90:91], v214 offset:4096\n"
    "v_add_f32 v184, v176, v177\n"
    "v_add_f32 v185, v178, v179\n"
    "v_add_f32 v186, v180, v181\n"
    "v_add_f32 v187, v182, v183\n"
    "v_add_f32 v184, v184, v185\n"
    "v_add_f32 v186, v186, v187\n"
    "v_mfma_f32_16x16x32_bf16 v[168:171], a[124:127], a[28:31], v[168:171]\n"
    "ds_read_b64_tr_b16 a[92:93], v215 offset:0\n"
    "v_add_f32 v184, v184, v186\n"
    "v_cvt_pk_bf16_f32 v224, v176, v177\n"
    "v_cvt_pk_bf16_f32 v225, v178, v179\n"
    "v_cvt_pk_bf16_f32 v226, v180, v181\n"
    "v_cvt_pk_bf16_f32 v227, v182, v183\n"
    "v_mov_b32 v232, v184\n"
    "v_mfma_f32_16x16x32_bf16 v[172:175], a[140:143], a[28:31], v[172:175]\n"
    "ds_read_b64_tr_b16 a[94:95], v215 offset:4096\n"
    "v_mfma_f32_16x16x32_bf16 v[160:163], a[112:115], a[32:35], 0\n"
    "ds_read_b64_tr_b16 a[96:97], v216 offset:0\n"
    "v_max3_f32 v237, v168, v169, v170\n"
    "s_nop 3\n"
    "v_max3_f32 v238, v171, v172, v173\n"
    "v_max3_f32 v237, v237, v174, v175\n"
    "v_mfma_f32_16x16x32_bf16 v[164:167], a[128:131], a[32:35], 0\n"
    "ds_read_b64_tr_b16 a[98:99], v216 offset:4096\n"
    "v_max_f32 v237, v237, v238\n"
    "v_mov_b32 v238, v237\n"
    "s_nop 1\n"
    "v_permlane16_swap_b32 v237, v238\n"
    "v_mfma_f32_16x16x32_bf16 v[160:163], a[116:119], a[36:39], v[160:163]\n"
    "ds_read_b64_tr_b16 a[100:101], v217 offset:0\n"
    "v_max_f32 v237, v237, v238\n"
    "v_mov_b32 v238, v237\n"
    "s_nop 1\n"
    "v_permlane32_swap_b32 v237, v238\n"
    "v_mfma_f32_16x16x32_bf16 v[164:167], a[132:135], a[36:39], v[164:167]\n"
    "ds_read_b64_tr_b16 a[102:103], v217 offset:4096\n"
    "v_max_f32 v237, v237, v238\n"
    "v_sub_f32 v168, v168, v237\n"
    "v_sub_f32 v169, v169, v237\n"
    "v_mfma_f32_16x16x32_bf16 v[160:163], a[120:123], a[40:43], v[160:163]\n"
    "ds_read_b64_tr_b16 a[104:105], v218 offset:0\n"
    "v_sub_f32 v170, v170, v237\n"
    "v_sub_f32 v171, v171, v237\n"
    "v_sub_f32 v172, v172, v237\n"
    "v_mfma_f32_16x16x32_bf16 v[164:167], a[136:139], a[40:43], v[164:167]\n"
    "ds_read_b64_tr_b16 a[106:107], v218 offset:4096\n"
    "v_sub_f32 v173, v173, v237\n"
    "v_sub_f32 v174, v174, v237\n"
    "v_sub_f32 v175, v175, v237\n"
    "v_mfma_f32_16x16x32_bf16 v[160:163], a[124:127], a[44:47], v[160:163]\n"
    "ds_read_b64_tr_b16 a[108:109], v219 offset:0\n"
    "v_exp_f32 v176, v168\n"
    "v_exp_f32 v177, v169\n"
    "v_exp_f32 v178, v170\n"
    "v_mfma_f32_16x16x32_bf16 v[164:167], a[140:143], a[44:47], v[164:167]\n"
    "ds_read_b64_tr_b16 a[110:111], v219 offset:4096\n"
    "v_exp_f32 v179, v171\n"
    "v_exp_f32 v180, v172\n"
    "v_exp_f32 v181, v173\n"
    "s_waitcnt lgkmcnt(4)\n"
    "v_mfma_f32_16x16x32_bf16 v[0:3], a[80:83], v[224:227], 0\n"
    "v_exp_f32 v182, v174\n"
    "v_exp_f32 v183, v175\n"
    "v_sub_f32 v196, 0, v237\n"
    "v_mfma_f32_16x16x32_bf16 v[4:7], a[84:87], v[224:227], 0\n"
    "v_sub_f32 v197, 0, v237\n"
    "v_sub_f32 v198, 0, v237\n"
    "v_sub_f32 v199, 0, v237\n"
    "v_mfma_f32_16x16x32_bf16 v[8:11], a[88:91], v[224:227], 0\n"
    "v_add_f32 v184, v176, v177\n"
    "v_add_f32 v185, v178, v179\n"
    "v_add_f32 v186, v180, v181\n"
    "v_mfma_f32_16x16x32_bf16 v[12:15], a[92:95], v[224:227], 0\n"
    "v_add_f32 v187, v182, v183\n"
    "v_add_f32 v184, v184, v185\n"
    "v_add_f32 v186, v186, v187\n"
    "v_mfma_f32_16x16x32_bf16 v[16:19], a[96:99], v[224:227], 0\n"
    "v_add_f32 v184, v184, v186\n"
    "v_cvt_pk_bf16_f32 v228, v176, v177\n"
    "v_cvt_pk_bf16_f32 v229, v178, v179\n"
    "v_mfma_f32_16x16x32_bf16 v[20:23], a[100:103], v[224:227], 0\n"
    "v_cvt_pk_bf16_f32 v230, v180, v181\n"
    "v_cvt_pk_bf16_f32 v231, v182, v183\n"
    "v_mov_b32 v233, v184\n"
    "s_waitcnt lgkmcnt(2)\n"
    "v_mfma_f32_16x16x32_bf16 v[24:27], a[104:107], v[224:227], 0\n"
    "s_waitcnt lgkmcnt(0)\n"
    "v_mfma_f32_16x16x32_bf16 v[28:31], a[108:111], v[224:227], 0\n"
    "s_barrier\n"
    "v_mfma_f32_16x16x32_bf16 v[168:171], a[112:115], a[48:51], 0\n"
    "ds_read_b128 a[144:147], v220 offset:8192\n"
    "v_max3_f32 v237, v160, v161, v162\n"
    "v_max3_f32 v238, v163, v164, v165\n"
    "v_max3_f32 v237, v237, v166, v167\n"
    "v_mfma_f32_16x16x32_bf16 v[172:175], a[128:131], a[48:51], 0\n"
    "ds_read_b128 a[160:163], v220 offset:12288\n"
    "v_max_f32 v237, v237, v238\n"
    "v_mov_b32 v238, v237\n"
    "s_nop 1\n"
    "v_permlane16_swap_b32 v237, v238\n"
    "v_mfma_f32_16x16x32_bf16 v[168:171], a[116:119], a[52:55], v[168:171]\n"
    "ds_read_b128 a[148:151], v221 offset:8192\n"
    "v_max_f32 v237, v237, v238\n"
    "v_mov_b32 v238, v237\n"
    "s_nop 1\n"
    "v_permlane32_swap_b32 v237, v238\n"
    "v_mfma_f32_16x16x32_bf16 v[172:175], a[132:135], a[52:55], v[172:175]\n"
    "ds_read_b128 a[164:167], v221 offset:12288\n"
    "v_max_f32 v237, v237, v238\n"
    "v_sub_f32 v160, v160, v237\n"
    "v_sub_f32 v161, v161, v237\n"
    "v_mfma_f32_16x16x32_bf16 v[168:171], a[120:123], a[56:59], v[168:171]\n"
    "ds_read_b128 a[152:155], v222 offset:8192\n"
    "v_sub_f32 v162, v162, v237\n"
    "v_sub_f32 v163, v163, v237\n"
    "v_sub_f32 v164, v164, v237\n"
    "v_mfma_f32_16x16x32_bf16 v[172:175], a[136:139], a[56:59], v[172:175]\n"
    "ds_read_b128 a[168:171], v222 offset:12288\n"
    "v_sub_f32 v165, v165, v237\n"
    "v_sub_f32 v166, v166, v237\n"
    "v_sub_f32 v167, v167, v237\n"
    "v_mfma_f32_16x16x32_bf16 v[168:171], a[124:127], a[60:63], v[168:171]\n"
    "ds_read_b128 a[156:159], v223 offset:8192\n"
    "v_exp_f32 v176, v160\n"
    "v_exp_f32 v177, v161\n"
    "v_exp_f32 v178, v162\n"
    "v_mfma_f32_16x16x32_bf16 v[172:175], a[140:143], a[60:63], v[172:175]\n"
    "ds_read_b128 a[172:175], v223 offset:12288\n"
    "v_exp_f32 v179, v163\n"
    "v_exp_f32 v180, v164\n"
    "v_exp_f32 v181, v165\n"
    "v_mfma_f32_16x16x32_bf16 v[32:35], a[80:83], v[228:231], 0\n"
    "v_exp_f32 v182, v166\n"
    "v_exp_f32 v183, v167\n"
    "v_sub_f32 v200, 0, v237\n"
    "s_add_u32 m0, s73, 4294942720\n"
    "s_add_u32 s85, s85, 8192\n"
    "s_and_b32 s85, s85, 0x1ffff\n"
    "v_mfma_f32_16x16x32_bf16 v[36:39], a[84:87], v[228:231], 0\n"
    "buffer_load_dwordx4 v242, s[48:51], s85 offen offset:0 nt lds\n"
    "v_sub_f32 v201, 0, v237\n"
    "v_sub_f32 v202, 0, v237\n"
    "v_sub_f32 v203, 0, v237\n"
    "v_mfma_f32_16x16x32_bf16 v[40:43], a[88:91], v[228:231], 0\n"
    "v_add_f32 v184, v176, v177\n"
    "v_add_f32 v185, v178, v179\n"
    "v_add_f32 v186, v180, v181\n"
    "v_mfma_f32_16x16x32_bf16 v[44:47], a[92:95], v[228:231], 0\n"
    "buffer_load_dwordx4 v243, s[48:51], s85 offen offset:1024 nt lds\n"
    "v_add_f32 v187, v182, v183\n"
    "v_add_f32 v184, v184, v185\n"
    "v_add_f32 v186, v186, v187\n"
    "v_mfma_f32_16x16x32_bf16 v[48:51], a[96:99], v[228:231], 0\n"
    "v_add_f32 v184, v184, v186\n"
    "v_cvt_pk_bf16_f32 v224, v176, v177\n"
    "v_cvt_pk_bf16_f32 v225, v178, v179\n"
    "v_mfma_f32_16x16x32_bf16 v[52:55], a[100:103], v[228:231], 0\n"
    "buffer_load_dwordx4 v242, s[48:51], s85 offen offset:2048 nt lds\n"
    "v_cvt_pk_bf16_f32 v226, v180, v181\n"
    "v_cvt_pk_bf16_f32 v227, v182, v183\n"
    "v_mov_b32 v234, v184\n"
    "v_mfma_f32_16x16x32_bf16 v[56:59], a[104:107], v[228:231], 0\n"
    "v_mfma_f32_16x16x32_bf16 v[60:63], a[108:111], v[228:231], 0\n"
    "buffer_load_dwordx4 v243, s[48:51], s85 offen offset:3072 nt lds\n"
    "s_waitcnt lgkmcnt(0)\n"
    "s_barrier\n"
    "v_mfma_f32_16x16x32_bf16 v[160:163], a[112:115], a[64:67], 0\n"
    "v_max3_f32 v237, v168, v169, v170\n"
    "v_max3_f32 v238, v171, v172, v173\n"
    "v_max3_f32 v237, v237, v174, v175\n"
    "s_add_u32 m0, s74, 8192\n"
    "s_add_u32 s71, s71, 8192\n"
    "s_and_b32 s71, s71, 0x1ffff\n"
    "v_mfma_f32_16x16x32_bf16 v[164:167], a[128:131], a[64:67], 0\n"
    "buffer_load_dwordx4 v188, s[44:47], s71 offen offset:0 nt lds\n"
    "v_max_f32 v237, v237, v238\n"
    "v_mov_b32 v238, v237\n"
    "s_nop 1\n"
    "v_permlane16_swap_b32 v237, v238\n"
    "v_mfma_f32_16x16x32_bf16 v[160:163], a[116:119], a[68:71], v[160:163]\n"
    "v_max_f32 v237, v237, v238\n"
    "v_mov_b32 v238, v237\n"
    "s_nop 1\n"
    "v_permlane32_swap_b32 v237, v238\n"
    "v_mfma_f32_16x16x32_bf16 v[164:167], a[132:135], a[68:71], v[164:167]\n"
    "v_max_f32 v237, v237, v238\n"
    "v_sub_f32 v168, v168, v237\n"
    "v_sub_f32 v169, v169, v237\n"
    "v_mfma_f32_16x16x32_bf16 v[160:163], a[120:123], a[72:75], v[160:163]\n"
    "buffer_load_dwordx4 v189, s[44:47], s71 offen offset:1024 nt lds\n"
    "v_sub_f32 v170, v170, v237\n"
    "v_sub_f32 v171, v171, v237\n"
    "v_sub_f32 v172, v172, v237\n"
    "v_mfma_f32_16x16x32_bf16 v[164:167], a[136:139], a[72:75], v[164:167]\n"
    "v_sub_f32 v173, v173, v237\n"
    "v_sub_f32 v174, v174, v237\n"
    "v_sub_f32 v175, v175, v237\n"
    "v_mfma_f32_16x16x32_bf16 v[160:163], a[124:127], a[76:79], v[160:163]\n"
    "v_exp_f32 v176, v168\n"
    "v_exp_f32 v177, v169\n"
    "v_exp_f32 v178, v170\n"
    "v_mfma_f32_16x16x32_bf16 v[164:167], a[140:143], a[76:79], v[164:167]\n"
    "buffer_load_dwordx4 v190, s[44:47], s71 offen offset:2048 nt lds\n"
    "v_exp_f32 v179, v171\n"
    "v_exp_f32 v180, v172\n"
    "v_exp_f32 v181, v173\n"
    "v_mfma_f32_16x16x32_bf16 v[64:67], a[80:83], v[224:227], 0\n"
    "v_exp_f32 v182, v174\n"
    "v_exp_f32 v183, v175\n"
    "v_sub_f32 v204, 0, v237\n"
    "v_mfma_f32_16x16x32_bf16 v[68:71], a[84:87], v[224:227], 0\n"
    "v_sub_f32 v205, 0, v237\n"
    "v_sub_f32 v206, 0, v237\n"
    "v_sub_f32 v207, 0, v237\n"
    "v_mfma_f32_16x16x32_bf16 v[72:75], a[88:91], v[224:227], 0\n"
    "buffer_load_dwordx4 v191, s[44:47], s71 offen offset:3072 nt lds\n"
    "v_add_f32 v184, v176, v177\n"
    "v_add_f32 v185, v178, v179\n"
    "v_add_f32 v186, v180, v181\n"
    "v_mfma_f32_16x16x32_bf16 v[76:79], a[92:95], v[224:227], 0\n"
    "v_add_f32 v187, v182, v183\n"
    "v_add_f32 v184, v184, v185\n"
    "v_add_f32 v186, v186, v187\n"
    "v_mfma_f32_16x16x32_bf16 v[80:83], a[96:99], v[224:227], 0\n"
    "v_add_f32 v184, v184, v186\n"
    "v_cvt_pk_bf16_f32 v228, v176, v177\n"
    "v_cvt_pk_bf16_f32 v229, v178, v179\n"
    "v_mfma_f32_16x16x32_bf16 v[84:87], a[100:103], v[224:227], 0\n"
    "v_cvt_pk_bf16_f32 v230, v180, v181\n"
    "v_cvt_pk_bf16_f32 v231, v182, v183\n"
    "v_mov_b32 v235, v184\n"
    "v_mfma_f32_16x16x32_bf16 v[88:91], a[104:107], v[224:227], 0\n"
    "v_mfma_f32_16x16x32_bf16 v[92:95], a[108:111], v[224:227], 0\n"
    "v_mfma_f32_16x16x32_bf16 v[168:171], a[144:147], a[0:3], v[192:195]\n"
    "v_max3_f32 v237, v160, v161, v162\n"
    "v_max3_f32 v238, v163, v164, v165\n"
    "v_max3_f32 v237, v237, v166, v167\n"
    "v_mfma_f32_16x16x32_bf16 v[172:175], a[160:163], a[0:3], v[192:195]\n"
    "v_max_f32 v237, v237, v238\n"
    "v_mov_b32 v238, v237\n"
    "s_nop 1\n"
    "v_permlane16_swap_b32 v237, v238\n"
    "v_mfma_f32_16x16x32_bf16 v[168:171], a[148:151], a[4:7], v[168:171]\n"
    "v_max_f32 v237, v237, v238\n"
    "v_mov_b32 v238, v237\n"
    "s_nop 1\n"
    "v_permlane32_swap_b32 v237, v238\n"
    "v_mfma_f32_16x16x32_bf16 v[172:175], a[164:167], a[4:7], v[172:175]\n"
    "v_max_f32 v237, v237, v238\n"
    "v_sub_f32 v160, v160, v237\n"
    "v_sub_f32 v161, v161, v237\n"
    "v_mfma_f32_16x16x32_bf16 v[168:171], a[152:155], a[8:11], v[168:171]\n"
    "v_sub_f32 v162, v162, v237\n"
    "v_sub_f32 v163, v163, v237\n"
    "v_sub_f32 v164, v164, v237\n"
    "v_mfma_f32_16x16x32_bf16 v[172:175], a[168:171], a[8:11], v[172:175]\n"
    "v_sub_f32 v165, v165, v237\n"
    "v_sub_f32 v166, v166, v237\n"
    "v_sub_f32 v167, v167, v237\n"
    "v_mfma_f32_16x16x32_bf16 v[168:171], a[156:159], a[12:15], v[168:171]\n"
    "v_exp_f32 v176, v160\n"
    "v_exp_f32 v177, v161\n"
    "v_exp_f32 v178, v162\n"
    "v_mfma_f32_16x16x32_bf16 v[172:175], a[172:175], a[12:15], v[172:175]\n"
    "v_exp_f32 v179, v163\n"
    "v_exp_f32 v180, v164\n"
    "v_exp_f32 v181, v165\n"
    "v_mfma_f32_16x16x32_bf16 v[96:99], a[80:83], v[228:231], 0\n"
    "v_exp_f32 v182, v166\n"
    "v_exp_f32 v183, v167\n"
    "v_sub_f32 v208, 0, v237\n"
    "v_mfma_f32_16x16x32_bf16 v[100:103], a[84:87], v[228:231], 0\n"
    "v_sub_f32 v209, 0, v237\n"
    "v_sub_f32 v210, 0, v237\n"
    "v_sub_f32 v211, 0, v237\n"
    "v_mfma_f32_16x16x32_bf16 v[104:107], a[88:91], v[228:231], 0\n"
    "v_add_f32 v184, v176, v177\n"
    "v_add_f32 v185, v178, v179\n"
    "v_add_f32 v186, v180, v181\n"
    "v_mfma_f32_16x16x32_bf16 v[108:111], a[92:95], v[228:231], 0\n"
    "v_add_f32 v187, v182, v183\n"
    "v_add_f32 v184, v184, v185\n"
    "v_add_f32 v186, v186, v187\n"
    "v_mfma_f32_16x16x32_bf16 v[112:115], a[96:99], v[228:231], 0\n"
    "v_add_f32 v184, v184, v186\n"
    "v_cvt_pk_bf16_f32 v224, v176, v177\n"
    "v_cvt_pk_bf16_f32 v225, v178, v179\n"
    "v_mfma_f32_16x16x32_bf16 v[116:119], a[100:103], v[228:231], 0\n"
    "v_cvt_pk_bf16_f32 v226, v180, v181\n"
    "v_cvt_pk_bf16_f32 v227, v182, v183\n"
    "v_mov_b32 v236, v184\n"
    "v_mfma_f32_16x16x32_bf16 v[120:123], a[104:107], v[228:231], 0\n"
    "v_mfma_f32_16x16x32_bf16 v[124:127], a[108:111], v[228:231], 0\n"
    "v_mfma_f32_16x16x32_bf16 v[160:163], a[144:147], a[16:19], v[196:199]\n"
    "v_exp_f32 v176, v168\n"
    "v_mfma_f32_16x16x32_bf16 v[164:167], a[160:163], a[16:19], v[196:199]\n"
    "v_exp_f32 v177, v169\n"
    "v_mfma_f32_16x16x32_bf16 v[160:163], a[148:151], a[20:23], v[160:163]\n"
    "v_exp_f32 v178, v170\n"
    "v_mfma_f32_16x16x32_bf16 v[164:167], a[164:167], a[20:23], v[164:167]\n"
    "v_exp_f32 v179, v171\n"
    "v_mfma_f32_16x16x32_bf16 v[160:163], a[152:155], a[24:27], v[160:163]\n"
    "v_exp_f32 v180, v172\n"
    "v_mfma_f32_16x16x32_bf16 v[164:167], a[168:171], a[24:27], v[164:167]\n"
    "v_exp_f32 v181, v173\n"
    "v_mfma_f32_16x16x32_bf16 v[160:163], a[156:159], a[28:31], v[160:163]\n"
    "v_exp_f32 v182, v174\n"
    "v_mfma_f32_16x16x32_bf16 v[164:167], a[172:175], a[28:31], v[164:167]\n"
    "v_exp_f32 v183, v175\n"
    "v_mfma_f32_16x16x32_bf16 v[128:131], a[80:83], v[224:227], 0\n"
    "ds_read_b64_tr_b16 a[80:81], v212 offset:8192\n"
    "v_add_f32 v184, v176, v177\n"
    "v_add_f32 v185, v178, v179\n"
    "v_mfma_f32_16x16x32_bf16 v[132:135], a[84:87], v[224:227], 0\n"
    "ds_read_b64_tr_b16 a[82:83], v212 offset:12288\n"
    "v_add_f32 v186, v180, v181\n"
    "v_add_f32 v187, v182, v183\n"
    "v_mfma_f32_16x16x32_bf16 v[136:139], a[88:91], v[224:227], 0\n"
    "ds_read_b64_tr_b16 a[84:85], v213 offset:8192\n"
    "v_add_f32 v184, v184, v185\n"
    "v_add_f32 v186, v186, v187\n"
    "v_mfma_f32_16x16x32_bf16 v[140:143], a[92:95], v[224:227], 0\n"
    "ds_read_b64_tr_b16 a[86:87], v213 offset:12288\n"
    "v_add_f32 v184, v184, v186\n"
    "v_cvt_pk_bf16_f32 v228, v176, v177\n"
    "v_mfma_f32_16x16x32_bf16 v[144:147], a[96:99], v[224:227], 0\n"
    "ds_read_b64_tr_b16 a[88:89], v214 offset:8192\n"
    "v_cvt_pk_bf16_f32 v229, v178, v179\n"
    "v_cmp_lt_f32 vcc, s77, v184\n"
    "v_mfma_f32_16x16x32_bf16 v[148:151], a[100:103], v[224:227], 0\n"
    "ds_read_b64_tr_b16 a[90:91], v214 offset:12288\n"
    "v_cvt_pk_bf16_f32 v230, v180, v181\n"
    "v_cvt_pk_bf16_f32 v231, v182, v183\n"
    "v_mfma_f32_16x16x32_bf16 v[152:155], a[104:107], v[224:227], 0\n"
    "ds_read_b64_tr_b16 a[92:93], v215 offset:8192\n"
    "s_cbranch_vccnz L_rareA_h5%=\n"
    "L_rareA_h5_r%=:\n"
    "v_add_f32 v232, v232, v184\n"
    "v_mfma_f32_16x16x32_bf16 v[156:159], a[108:111], v[224:227], 0\n"
    "ds_read_b64_tr_b16 a[94:95], v215 offset:12288\n"
    "s_mov_b32 s82, 6\n"
    "L_attA%=:\n"
    "v_mfma_f32_16x16x32_bf16 v[168:171], a[144:147], a[32:35], v[200:203]\n"
    "ds_read_b64_tr_b16 a[96:97], v216 offset:8192\n"
    "v_exp_f32 v176, v160\n"
    "v_mfma_f32_16x16x32_bf16 v[172:175], a[160:163], a[32:35], v[200:203]\n"
    "ds_read_b64_tr_b16 a[98:99], v216 offset:12288\n"
    "v_exp_f32 v177, v161\n"
    "v_mfma_f32_16x16x32_bf16 v[168:171], a[148:151], a[36:39], v[168:171]\n"
    "ds_read_b64_tr_b16 a[100:101], v217 offset:8192\n"
    "v_exp_f32 v178, v162\n"
    "v_mfma_f32_16x16x32_bf16 v[172:175], a[164:167], a[36:39], v[172:175]\n"
    "ds_read_b64_tr_b16 a[102:103], v217 offset:12288\n"
    "v_exp_f32 v179, v163\n"
    "v_mfma_f32_16x16x32_bf16 v[168:171], a[152:155], a[40:43], v[168:171]\n"
    "ds_read_b64_tr_b16 a[104:105], v218 offset:8192\n"
    "v_exp_f32 v180, v164\n"
    "v_mfma_f32_16x16x32_bf16 v[172:175], a[168:171], a[40:43], v[172:175]\n"
    "ds_read_b64_tr_b16 a[106:107], v218 offset:12288\n"
    "v_exp_f32 v181, v165\n"
    "v_mfma_f32_16x16x32_bf16 v[168:171], a[156:159], a[44:47], v[168:171]\n"
    "ds_read_b64_tr_b16 a[108:109], v219 offset:8192\n"
    "v_exp_f32 v182, v166\n"
    "v_mfma_f32_16x16x32_bf16 v[172:175], a[172:175], a[44:47], v[172:175]\n"
    "ds_read_b64_tr_b16 a[110:111], v219 offset:12288\n"
    "v_exp_f32 v183, v167\n"
    "s_waitcnt lgkmcnt(4)\n"
    "v_mfma_f32_16x16x32_bf16 v[0:3], a[80:83], v[228:231], v[0:3]\n"
    "v_add_f32 v184, v176, v177\n"
    "v_add_f32 v185, v178, v179\n"
    "v_mfma_f32_16x16x32_bf16 v[4:7], a[84:87], v[228:231], v[4:7]\n"
    "v_add_f32 v186, v180, v181\n"
    "v_add_f32 v187, v182, v183\n"
    "v_mfma_f32_16x16x32_bf16 v[8:11], a[88:91], v[228:231], v[8:11]\n"
    "v_add_f32 v184, v184, v185\n"
    "v_add_f32 v186, v186, v187\n"
    "v_mfma_f32_16x16x32_bf16 v[12:15], a[92:95], v[228:231], v[12:15]\n"
    "v_add_f32 v184, v184, v186\n"
    "v_cvt_pk_bf16_f32 v224, v176, v177\n"
    "v_mfma_f32_16x16x32_bf16 v[16:19], a[96:99], v[228:231], v[16:19]\n"
    "v_cvt_pk_bf16_f32 v225, v178, v179\n"
    "v_cmp_lt_f32 vcc, s77, v184\n"
    "v_mfma_f32_16x16x32_bf16 v[20:23], a[100:103], v[228:231], v[20:23]\n"
    "v_cvt_pk_bf16_f32 v226, v180, v181\n"
    "v_cvt_pk_bf16_f32 v227, v182, v183\n"
    "s_waitcnt lgkmcnt(2)\n"
    "v_mfma_f32_16x16x32_bf16 v[24:27], a[104:107], v[228:231], v[24:27]\n"
    "s_cbranch_vccnz L_rareA_l0%=\n"
    "L_rareA_l0_r%=:\n"
    "v_add_f32 v233, v233, v184\n"
    "s_waitcnt lgkmcnt(0)\n"
    "v_mfma_f32_16x16x32_bf16 v[28:31], a[108:111], v[228:231], v[28:31]\n"
    "s_waitcnt vmcnt(8)\n"
    "s_barrier\n"
    "v_mfma_f32_16x16x32_bf16 v[160:163], a[144:147], a[48:51], v[204:207]\n"
    "ds_read_b128 a[112:115], v220 offset:0\n"
    "v_exp_f32 v176, v168\n"
    "v_mfma_f32_16x16x32_bf16 v[164:167], a[160:163], a[48:51], v[204:207]\n"
    "ds_read_b128 a[128:131], v220 offset:4096\n"
    "v_exp_f32 v177, v169\n"
    "v_mfma_f32_16x16x32_bf16 v[160:163], a[148:151], a[52:55], v[160:163]\n"
    "ds_read_b128 a[116:119], v221 offset:0\n"
    "v_exp_f32 v178, v170\n"
    "v_mfma_f32_16x16x32_bf16 v[164:167], a[164:167], a[52:55], v[164:167]\n"
    "ds_read_b128 a[132:135], v221 offset:4096\n"
    "v_exp_f32 v179, v171\n"
    "v_mfma_f32_16x16x32_bf16 v[160:163], a[152:155], a[56:59], v[160:163]\n"
    "ds_read_b128 a[120:123], v222 offset:0\n"
    "v_exp_f32 v180, v172\n"
    "v_mfma_f32_16x16x32_bf16 v[164:167], a[168:171], a[56:59], v[164:167]\n"
    "ds_read_b128 a[136:139], v222 offset:4096\n"
    "v_exp_f32 v181, v173\n"
    "v_mfma_f32_16x16x32_bf16 v[160:163], a[156:159], a[60:63], v[160:163]\n"
    "ds_read_b128 a[124:127], v223 offset:0\n"
    "v_exp_f32 v182, v174\n"
    "v_mfma_f32_16x16x32_bf16 v[164:167], a[172:175], a[60:63], v[164:167]\n"
    "ds_read_b128 a[140:143], v223 offset:4096\n"
    "v_exp_f32 v183, v175\n"
    "v_mfma_f32_16x16x32_bf16 v[32:35], a[80:83], v[224:227], v[32:35]\n"
    "v_add_f32 v184, v176, v177\n"
    "v_add_f32 v185, v178, v179\n"
    "s_add_u32 m0, s73, 0\n"
    "s_add_u32 s85, s85, 8192\n"
    "s_and_b32 s85, s85, 0x1ffff\n"
    "v_mfma_f32_16x16x32_bf16 v[36:39], a[84:87], v[224:227], v[36:39]\n"
    "buffer_load_dwordx4 v242, s[48:51], s85 offen offset:0 nt lds\n"
    "v_add_f32 v186, v180, v181\n"
    "v_add_f32 v187, v182, v183\n"
    "v_mfma_f32_16x16x32_bf16 v[40:43], a[88:91], v[224:227], v[40:43]\n"
    "v_add_f32 v184, v184, v185\n"
    "v_add_f32 v186, v186, v187\n"
    "v_mfma_f32_16x16x32_bf16 v[44:47], a[92:95], v[224:227], v[44:47]\n"
    "buffer_load_dwordx4 v243, s[48:51], s85 offen offset:1024 nt lds\n"
    "v_add_f32 v184, v184, v186\n"
    "v_cvt_pk_bf16_f32 v228, v176, v177\n"
    "v_mfma_f32_16x16x32_bf16 v[48:51], a[96:99], v[224:227], v[48:51]\n"
    "v_cvt_pk_bf16_f32 v229, v178, v179\n"
    "v_cmp_lt_f32 vcc, s77, v184\n"
    "v_mfma_f32_16x16x32_bf16 v[52:55], a[100:103], v[224:227], v[52:55]\n"
    "buffer_load_dwordx4 v242, s[48:51], s85 offen offset:2048 nt lds\n"
    "v_cvt_pk_bf16_f32 v230, v180, v181\n"
    "v_cvt_pk_bf16_f32 v231, v182, v183\n"
    "v_mfma_f32_16x16x32_bf16 v[56:59], a[104:107], v[224:227], v[56:59]\n"
    "s_cbranch_vccnz L_rareA_l1%=\n"
    "L_rareA_l1_r%=:\n"
    "v_add_f32 v234, v234, v184\n"
    "v_mfma_f32_16x16x32_bf16 v[60:63], a[108:111], v[224:227], v[60:63]\n"
    "buffer_load_dwordx4 v243, s[48:51], s85 offen offset:3072 nt lds\n"
    "s_waitcnt lgkmcnt(0)\n"
    "s_barrier\n"
    "v_mfma_f32_16x16x32_bf16 v[168:171], a[144:147], a[64:67], v[208:211]\n"
    "v_exp_f32 v176, v160\n"
    "s_add_u32 m0, s74, 0\n"
    "s_add_u32 s71, s71, 8192\n"
    "s_and_b32 s71, s71, 0x1ffff\n"
    "v_mfma_f32_16x16x32_bf16 v[172:175], a[160:163], a[64:67], v[208:211]\n"
    "buffer_load_dwordx4 v188, s[44:47], s71 offen offset:0 nt lds\n"
    "v_exp_f32 v177, v161\n"
    "v_mfma_f32_16x16x32_bf16 v[168:171], a[148:151], a[68:71], v[168:171]\n"
    "v_exp_f32 v178, v162\n"
    "v_mfma_f32_16x16x32_bf16 v[172:175], a[164:167], a[68:71], v[172:175]\n"
    "v_exp_f32 v179, v163\n"
    "v_mfma_f32_16x16x32_bf16 v[168:171], a[152:155], a[72:75], v[168:171]\n"
    "buffer_load_dwordx4 v189, s[44:47], s71 offen offset:1024 nt lds\n"
    "v_exp_f32 v180, v164\n"
    "v_mfma_f32_16x16x32_bf16 v[172:175], a[168:171], a[72:75], v[172:175]\n"
    "v_exp_f32 v181, v165\n"
    "v_mfma_f32_16x16x32_bf16 v[168:171], a[156:159], a[76:79], v[168:171]\n"
    "v_exp_f32 v182, v166\n"
    "v_mfma_f32_16x16x32_bf16 v[172:175], a[172:175], a[76:79], v[172:175]\n"
    "buffer_load_dwordx4 v190, s[44:47], s71 offen offset:2048 nt lds\n"
    "v_exp_f32 v183, v167\n"
    "v_mfma_f32_16x16x32_bf16 v[64:67], a[80:83], v[228:231], v[64:67]\n"
    "v_add_f32 v184, v176, v177\n"
    "v_add_f32 v185, v178, v179\n"
    "v_mfma_f32_16x16x32_bf16 v[68:71], a[84:87], v[228:231], v[68:71]\n"
    "v_add_f32 v186, v180, v181\n"
    "v_add_f32 v187, v182, v183\n"
    "v_mfma_f32_16x16x32_bf16 v[72:75], a[88:91], v[228:231], v[72:75]\n"
    "buffer_load_dwordx4 v191, s[44:47], s71 offen offset:3072 nt lds\n"
    "v_add_f32 v184, v184, v185\n"
    "v_add_f32 v186, v186, v187\n"
    "v_mfma_f32_16x16x32_bf16 v[76:79], a[92:95], v[228:231], v[76:79]\n"
    "v_add_f32 v184, v184, v186\n"
    "v_cvt_pk_bf16_f32 v224, v176, v177\n"
    "v_mfma_f32_16x16x32_bf16 v[80:83], a[96:99], v[228:231], v[80:83]\n"
    "v_cvt_pk_bf16_f32 v225, v178, v179\n"
    "v_cmp_lt_f32 vcc, s77, v184\n"
    "v_mfma_f32_16x16x32_bf16 v[84:87], a[100:103], v[228:231], v[84:87]\n"
    "v_cvt_pk_bf16_f32 v226, v180, v181\n"
    "v_cvt_pk_bf16_f32 v227, v182, v183\n"
    "v_mfma_f32_16x16x32_bf16 v[88:91], a[104:107], v[228:231], v[88:91]\n"
    "s_cbranch_vccnz L_rareA_l2%=\n"
    "L_rareA_l2_r%=:\n"
    "v_add_f32 v235, v235, v184\n"
    "v_mfma_f32_16x16x32_bf16 v[92:95], a[108:111], v[228:231], v[92:95]\n"
    "v_mfma_f32_16x16x32_bf16 v[160:163], a[112:115], a[0:3], v[192:195]\n"
    "v_exp_f32 v176, v168\n"
    "v_mfma_f32_16x16x32_bf16 v[164:167], a[128:131], a[0:3], v[192:195]\n"
    "v_exp_f32 v177, v169\n"
    "v_mfma_f32_16x16x32_bf16 v[160:163], a[116:119], a[4:7], v[160:163]\n"
    "v_exp_f32 v178, v170\n"
    "v_mfma_f32_16x16x32_bf16 v[164:167], a[132:135], a[4:7], v[164:167]\n"
    "v_exp_f32 v179, v171\n"
    "v_mfma_f32_16x16x32_bf16 v[160:163], a[120:123], a[8:11], v[160:163]\n"
    "v_exp_f32 v180, v172\n"
    "v_mfma_f32_16x16x32_bf16 v[164:167], a[136:139], a[8:11], v[164:167]\n"
    "v_exp_f32 v181, v173\n"
    "v_mfma_f32_16x16x32_bf16 v[160:163], a[124:127], a[12:15], v[160:163]\n"
    "v_exp_f32 v182, v174\n"
    "v_mfma_f32_16x16x32_bf16 v[164:167], a[140:143], a[12:15], v[164:167]\n"
    "v_exp_f32 v183, v175\n"
    "v_mfma_f32_16x16x32_bf16 v[96:99], a[80:83], v[224:227], v[96:99]\n"
    "v_add_f32 v184, v176, v177\n"
    "v_add_f32 v185, v178, v179\n"
    "v_xor_b32 v212, 32768, v212\n"
    "v_mfma_f32_16x16x32_bf16 v[100:103], a[84:87], v[224:227], v[100:103]\n"
    "v_add_f32 v186, v180, v181\n"
    "v_add_f32 v187, v182, v183\n"
    "v_xor_b32 v213, 32768, v213\n"
    "v_mfma_f32_16x16x32_bf16 v[104:107], a[88:91], v[224:227], v[104:107]\n"
    "v_add_f32 v184, v184, v185\n"
    "v_add_f32 v186, v186, v187\n"
    "v_xor_b32 v214, 32768, v214\n"
    "v_mfma_f32_16x16x32_bf16 v[108:111], a[92:95], v[224:227], v[108:111]\n"
    "v_add_f32 v184, v184, v186\n"
    "v_cvt_pk_bf16_f32 v228, v176, v177\n"
    "v_xor_b32 v215, 32768, v215\n"
    "v_mfma_f32_16x16x32_bf16 v[112:115], a[96:99], v[224:227], v[112:115]\n"
    "v_cvt_pk_bf16_f32 v229, v178, v179\n"
    "v_cmp_lt_f32 vcc, s77, v184\n"
    "v_xor_b32 v216, 32768, v216\n"
    "v_mfma_f32_16x16x32_bf16 v[116:119], a[100:103], v[224:227], v[116:119]\n"
    "v_cvt_pk_bf16_f32 v230, v180, v181\n"
    "v_cvt_pk_bf16_f32 v231, v182, v183\n"
    "v_xor_b32 v217, 32768, v217\n"
    "v_mfma_f32_16x16x32_bf16 v[120:123], a[104:107], v[224:227], v[120:123]\n"
    "s_cbranch_vccnz L_rareA_l3%=\n"
    "L_rareA_l3_r%=:\n"
    "v_add_f32 v236, v236, v184\n"
    "v_xor_b32 v218, 32768, v218\n"
    "v_mfma_f32_16x16x32_bf16 v[124:127], a[108:111], v[224:227], v[124:127]\n"
    "v_xor_b32 v219, 32768, v219\n"
    "v_mfma_f32_16x16x32_bf16 v[168:171], a[112:115], a[16:19], v[196:199]\n"
    "v_exp_f32 v176, v160\n"
    "v_mfma_f32_16x16x32_bf16 v[172:175], a[128:131], a[16:19], v[196:199]\n"
    "v_exp_f32 v177, v161\n"
    "v_mfma_f32_16x16x32_bf16 v[168:171], a[116:119], a[20:23], v[168:171]\n"
    "v_exp_f32 v178, v162\n"
    "v_mfma_f32_16x16x32_bf16 v[172:175], a[132:135], a[20:23], v[172:175]\n"
    "v_exp_f32 v179, v163\n"
    "v_mfma_f32_16x16x32_bf16 v[168:171], a[120:123], a[24:27], v[168:171]\n"
    "v_exp_f32 v180, v164\n"
    "v_mfma_f32_16x16x32_bf16 v[172:175], a[136:139], a[24:27], v[172:175]\n"
    "v_exp_f32 v181, v165\n"
    "v_mfma_f32_16x16x32_bf16 v[168:171], a[124:127], a[28:31], v[168:171]\n"
    "v_exp_f32 v182, v166\n"
    "v_mfma_f32_16x16x32_bf16 v[172:175], a[140:143], a[28:31], v[172:175]\n"
    "v_exp_f32 v183, v167\n"
    "v_mfma_f32_16x16x32_bf16 v[128:131], a[80:83], v[228:231], v[128:131]\n"
    "ds_read_b64_tr_b16 a[80:81], v212 offset:0\n"
    "v_add_f32 v184, v176, v177\n"
    "v_add_f32 v185, v178, v179\n"
    "v_mfma_f32_16x16x32_bf16 v[132:135], a[84:87], v[228:231], v[132:135]\n"
    "ds_read_b64_tr_b16 a[82:83], v212 offset:4096\n"
    "v_add_f32 v186, v180, v181\n"
    "v_add_f32 v187, v182, v183\n"
    "v_mfma_f32_16x16x32_bf16 v[136:139], a[88:91], v[228:231], v[136:139]\n"
    "ds_read_b64_tr_b16 a[84:85], v213 offset:0\n"
    "v_add_f32 v184, v184, v185\n"
    "v_add_f32 v186, v186, v187\n"
    "v_mfma_f32_16x16x32_bf16 v[140:143], a[92:95], v[228:231], v[140:143]\n"
    "ds_read_b64_tr_b16 a[86:87], v213 offset:4096\n"
    "v_add_f32 v184, v184, v186\n"
    "v_cvt_pk_bf16_f32 v224, v176, v177\n"
    "v_mfma_f32_16x16x32_bf16 v[144:147], a[96:99], v[228:231], v[144:147]\n"
    "ds_read_b64_tr_b16 a[88:89], v214 offset:0\n"
    "v_cvt_pk_bf16_f32 v225, v178, v179\n"
    "v_cmp_lt_f32 vcc, s77, v184\n"
    "v_mfma_f32_16x16x32_bf16 v[148:151], a[100:103], v[228:231], v[148:151]\n"
    "ds_read_b64_tr_b16 a[90:91], v214 offset:4096\n"
    "v_cvt_pk_bf16_f32 v226, v180, v181\n"
    "v_cvt_pk_bf16_f32 v227, v182, v183\n"
    "v_mfma_f32_16x16x32_bf16 v[152:155], a[104:107], v[228:231], v[152:155]\n"
    "ds_read_b64_tr_b16 a[92:93], v215 offset:0\n"
    "s_cbranch_vccnz L_rareA_l4%=\n"
    "L_rareA_l4_r%=:\n"
    "v_add_f32 v232, v232, v184\n"
    "v_mfma_f32_16x16x32_bf16 v[156:159], a[108:111], v[228:231], v[156:159]\n"
    "ds_read_b64_tr_b16 a[94:95], v215 offset:4096\n"
    "v_mfma_f32_16x16x32_bf16 v[160:163], a[112:115], a[32:35], v[200:203]\n"
    "ds_read_b64_tr_b16 a[96:97], v216 offset:0\n"
    "v_exp_f32 v176, v168\n"
    "v_mfma_f32_16x16x32_bf16 v[164:167], a[128:131], a[32:35], v[200:203]\n"
    "ds_read_b64_tr_b16 a[98:99], v216 offset:4096\n"
    "v_exp_f32 v177, v169\n"
    "v_mfma_f32_16x16x32_bf16 v[160:163], a[116:119], a[36:39], v[160:163]\n"
    "ds_read_b64_tr_b16 a[100:101], v217 offset:0\n"
    "v_exp_f32 v178, v170\n"
    "v_mfma_f32_16x16x32_bf16 v[164:167], a[132:135], a[36:39], v[164:167]\n"
    "ds_read_b64_tr_b16 a[102:103], v217 offset:4096\n"
    "v_exp_f32 v179, v171\n"
    "v_mfma_f32_16x16x32_bf16 v[160:163], a[120:123], a[40:43], v[160:163]\n"
    "ds_read_b64_tr_b16 a[104:105], v218 offset:0\n"
    "v_exp_f32 v180, v172\n"
    "v_mfma_f32_16x16x32_bf16 v[164:167], a[136:139], a[40:43], v[164:167]\n"
    "ds_read_b64_tr_b16 a[106:107], v218 offset:4096\n"
    "v_exp_f32 v181, v173\n"
    "v_mfma_f32_16x16x32_bf16 v[160:163], a[124:127], a[44:47], v[160:163]\n"
    "ds_read_b64_tr_b16 a[108:109], v219 offset:0\n"
    "v_exp_f32 v182, v174\n"
    "v_mfma_f32_16x16x32_bf16 v[164:167], a[140:143], a[44:47], v[164:167]\n"
    "ds_read_b64_tr_b16 a[110:111], v219 offset:4096\n"
    "v_exp_f32 v183, v175\n"
    "s_waitcnt lgkmcnt(4)\n"
    "v_mfma_f32_16x16x32_bf16 v[0:3], a[80:83], v[224:227], v[0:3]\n"
    "v_add_f32 v184, v176, v177\n"
    "v_add_f32 v185, v178, v179\n"
    "v_mfma_f32_16x16x32_bf16 v[4:7], a[84:87], v[224:227], v[4:7]\n"
    "v_add_f32 v186, v180, v181\n"
    "v_add_f32 v187, v182, v183\n"
    "v_mfma_f32_16x16x32_bf16 v[8:11], a[88:91], v[224:227], v[8:11]\n"
    "v_add_f32 v184, v184, v185\n"
    "v_add_f32 v186, v186, v187\n"
    "v_mfma_f32_16x16x32_bf16 v[12:15], a[92:95], v[224:227], v[12:15]\n"
    "v_add_f32 v184, v184, v186\n"
    "v_cvt_pk_bf16_f32 v228, v176, v177\n"
    "v_mfma_f32_16x16x32_bf16 v[16:19], a[96:99], v[224:227], v[16:19]\n"
    "v_cvt_pk_bf16_f32 v229, v178, v179\n"
    "v_cmp_lt_f32 vcc, s77, v184\n"
    "v_mfma_f32_16x16x32_bf16 v[20:23], a[100:103], v[224:227], v[20:23]\n"
    "v_cvt_pk_bf16_f32 v230, v180, v181\n"
    "v_cvt_pk_bf16_f32 v231, v182, v183\n"
    "s_waitcnt lgkmcnt(2)\n"
    "v_mfma_f32_16x16x32_bf16 v[24:27], a[104:107], v[224:227], v[24:27]\n"
    "s_cbranch_vccnz L_rareA_l5%=\n"
    "L_rareA_l5_r%=:\n"
    "v_add_f32 v233, v233, v184\n"
    "s_waitcnt lgkmcnt(0)\n"
    "v_mfma_f32_16x16x32_bf16 v[28:31], a[108:111], v[224:227], v[28:31]\n"
    "s_waitcnt vmcnt(8)\n"
    "s_barrier\n"
    "v_mfma_f32_16x16x32_bf16 v[168:171], a[112:115], a[48:51], v[204:207]\n"
    "ds_read_b128 a[144:147], v220 offset:8192\n"
    "v_exp_f32 v176, v160\n"
    "v_mfma_f32_16x16x32_bf16 v[172:175], a[128:131], a[48:51], v[204:207]\n"
    "ds_read_b128 a[160:163], v220 offset:12288\n"
    "v_exp_f32 v177, v161\n"
    "v_mfma_f32_16x16x32_bf16 v[168:171], a[116:119], a[52:55], v[168:171]\n"
    "ds_read_b128 a[148:151], v221 offset:8192\n"
    "v_exp_f32 v178, v162\n"
    "v_mfma_f32_16x16x32_bf16 v[172:175], a[132:135], a[52:55], v[172:175]\n"
    "ds_read_b128 a[164:167], v221 offset:12288\n"
    "v_exp_f32 v179, v163\n"
    "v_mfma_f32_16x16x32_bf16 v[168:171], a[120:123], a[56:59], v[168:171]\n"
    "ds_read_b128 a[152:155], v222 offset:8192\n"
    "v_exp_f32 v180, v164\n"
    "v_mfma_f32_16x16x32_bf16 v[172:175], a[136:139], a[56:59], v[172:175]\n"
    "ds_read_b128 a[168:171], v222 offset:12288\n"
    "v_exp_f32 v181, v165\n"
    "v_mfma_f32_16x16x32_bf16 v[168:171], a[124:127], a[60:63], v[168:171]\n"
    "ds_read_b128 a[156:159], v223 offset:8192\n"
    "v_exp_f32 v182, v166\n"
    "v_mfma_f32_16x16x32_bf16 v[172:175], a[140:143], a[60:63], v[172:175]\n"
    "ds_read_b128 a[172:175], v223 offset:12288\n"
    "v_exp_f32 v183, v167\n"
    "v_mfma_f32_16x16x32_bf16 v[32:35], a[80:83], v[228:231], v[32:35]\n"
    "v_add_f32 v184, v176, v177\n"
    "v_add_f32 v185, v178, v179\n"
    "s_add_u32 m0, s73, 8192\n"
    "s_add_u32 s85, s85, 8192\n"
    "s_and_b32 s85, s85, 0x1ffff\n"
    "v_mfma_f32_16x16x32_bf16 v[36:39], a[84:87], v[228:231], v[36:39]\n"
    "buffer_load_dwordx4 v242, s[48:51], s85 offen offset:0 nt lds\n"
    "v_add_f32 v186, v180, v181\n"
    "v_add_f32 v187, v182, v183\n"
    "v_mfma_f32_16x16x32_bf16 v[40:43], a[88:91], v[228:231], v[40:43]\n"
    "v_add_f32 v184, v184, v185\n"
    "v_add_f32 v186, v186, v187\n"
    "v_mfma_f32_16x16x32_bf16 v[44:47], a[92:95], v[228:231], v[44:47]\n"
    "buffer_load_dwordx4 v243, s[48:51], s85 offen offset:1024 nt lds\n"
    "v_add_f32 v184, v184, v186\n"
    "v_cvt_pk_bf16_f32 v224, v176, v177\n"
    "v_mfma_f32_16x16x32_bf16 v[48:51], a[96:99], v[228:231], v[48:51]\n"
    "v_cvt_pk_bf16_f32 v225, v178, v179\n"
    "v_cmp_lt_f32 vcc, s77, v184\n"
    "v_mfma_f32_16x16x32_bf16 v[52:55], a[100:103], v[228:231], v[52:55]\n"
    "buffer_load_dwordx4 v242, s[48:51], s85 offen offset:2048 nt lds\n"
    "v_cvt_pk_bf16_f32 v226, v180, v181\n"
    "v_cvt_pk_bf16_f32 v227, v182, v183\n"
    "v_mfma_f32_16x16x32_bf16 v[56:59], a[104:107], v[228:231], v[56:59]\n"
    "s_cbranch_vccnz L_rareA_l6%=\n"
    "L_rareA_l6_r%=:\n"
    "v_add_f32 v234, v234, v184\n"
    "v_mfma_f32_16x16x32_bf16 v[60:63], a[108:111], v[228:231], v[60:63]\n"
    "buffer_load_dwordx4 v243, s[48:51], s85 offen offset:3072 nt lds\n"
    "s_waitcnt lgkmcnt(0)\n"
    "s_barrier\n"
    "v_mfma_f32_16x16x32_bf16 v[160:163], a[112:115], a[64:67], v[208:211]\n"
    "v_exp_f32 v176, v168\n"
    "s_add_u32 m0, s74, 8192\n"
    "s_add_u32 s71, s71, 8192\n"
    "s_and_b32 s71, s71, 0x1ffff\n"
    "v_mfma_f32_16x16x32_bf16 v[164:167], a[128:131], a[64:67], v[208:211]\n"
    "buffer_load_dwordx4 v188, s[44:47], s71 offen offset:0 nt lds\n"
    "v_exp_f32 v177, v169\n"
    "v_mfma_f32_16x16x32_bf16 v[160:163], a[116:119], a[68:71], v[160:163]\n"
    "v_exp_f32 v178, v170\n"
    "v_mfma_f32_16x16x32_bf16 v[164:167], a[132:135], a[68:71], v[164:167]\n"
    "v_exp_f32 v179, v171\n"
    "v_mfma_f32_16x16x32_bf16 v[160:163], a[120:123], a[72:75], v[160:163]\n"
    "buffer_load_dwordx4 v189, s[44:47], s71 offen offset:1024 nt lds\n"
    "v_exp_f32 v180, v172\n"
    "v_mfma_f32_16x16x32_bf16 v[164:167], a[136:139], a[72:75], v[164:167]\n"
    "v_exp_f32 v181, v173\n"
    "v_mfma_f32_16x16x32_bf16 v[160:163], a[124:127], a[76:79], v[160:163]\n"
    "v_exp_f32 v182, v174\n"
    "v_mfma_f32_16x16x32_bf16 v[164:167], a[140:143], a[76:79], v[164:167]\n"
    "buffer_load_dwordx4 v190, s[44:47], s71 offen offset:2048 nt lds\n"
    "v_exp_f32 v183, v175\n"
    "v_mfma_f32_16x16x32_bf16 v[64:67], a[80:83], v[224:227], v[64:67]\n"
    "v_add_f32 v184, v176, v177\n"
    "v_add_f32 v185, v178, v179\n"
    "v_mfma_f32_16x16x32_bf16 v[68:71], a[84:87], v[224:227], v[68:71]\n"
    "v_add_f32 v186, v180, v181\n"
    "v_add_f32 v187, v182, v183\n"
    "v_mfma_f32_16x16x32_bf16 v[72:75], a[88:91], v[224:227], v[72:75]\n"
    "buffer_load_dwordx4 v191, s[44:47], s71 offen offset:3072 nt lds\n"
    "v_add_f32 v184, v184, v185\n"
    "v_add_f32 v186, v186, v187\n"
    "v_mfma_f32_16x16x32_bf16 v[76:79], a[92:95], v[224:227], v[76:79]\n"
    "v_add_f32 v184, v184, v186\n"
    "v_cvt_pk_bf16_f32 v228, v176, v177\n"
    "v_mfma_f32_16x16x32_bf16 v[80:83], a[96:99], v[224:227], v[80:83]\n"
    "v_cvt_pk_bf16_f32 v229, v178, v179\n"
    "v_cmp_lt_f32 vcc, s77, v184\n"
    "v_mfma_f32_16x16x32_bf16 v[84:87], a[100:103], v[224:227], v[84:87]\n"
    "v_cvt_pk_bf16_f32 v230, v180, v181\n"
    "v_cvt_pk_bf16_f32 v231, v182, v183\n"
    "v_mfma_f32_16x16x32_bf16 v[88:91], a[104:107], v[224:227], v[88:91]\n"
    "s_cbranch_vccnz L_rareA_l7%=\n"
    "L_rareA_l7_r%=:\n"
    "v_add_f32 v235, v235, v184\n"
    "v_mfma_f32_16x16x32_bf16 v[92:95], a[108:111], v[224:227], v[92:95]\n"
    "s_xor_b32 s73, s73, 32768\n"
    "v_mfma_f32_16x16x32_bf16 v[168:171], a[144:147], a[0:3], v[192:195]\n"
    "v_exp_f32 v176, v160\n"
    "v_mfma_f32_16x16x32_bf16 v[172:175], a[160:163], a[0:3], v[192:195]\n"
    "v_exp_f32 v177, v161\n"
    "v_mfma_f32_16x16x32_bf16 v[168:171], a[148:151], a[4:7], v[168:171]\n"
    "v_exp_f32 v178, v162\n"
    "v_mfma_f32_16x16x32_bf16 v[172:175], a[164:167], a[4:7], v[172:175]\n"
    "v_exp_f32 v179, v163\n"
    "v_mfma_f32_16x16x32_bf16 v[168:171], a[152:155], a[8:11], v[168:171]\n"
    "v_exp_f32 v180, v164\n"
    "v_mfma_f32_16x16x32_bf16 v[172:175], a[168:171], a[8:11], v[172:175]\n"
    "v_exp_f32 v181, v165\n"
    "v_mfma_f32_16x16x32_bf16 v[168:171], a[156:159], a[12:15], v[168:171]\n"
    "v_exp_f32 v182, v166\n"
    "v_mfma_f32_16x16x32_bf16 v[172:175], a[172:175], a[12:15], v[172:175]\n"
    "v_exp_f32 v183, v167\n"
    "v_mfma_f32_16x16x32_bf16 v[96:99], a[80:83], v[228:231], v[96:99]\n"
    "v_add_f32 v184, v176, v177\n"
    "v_add_f32 v185, v178, v179\n"
    "v_mfma_f32_16x16x32_bf16 v[100:103], a[84:87], v[228:231], v[100:103]\n"
    "v_add_f32 v186, v180, v181\n"
    "v_add_f32 v187, v182, v183\n"
    "v_mfma_f32_16x16x32_bf16 v[104:107], a[88:91], v[228:231], v[104:107]\n"
    "v_add_f32 v184, v184, v185\n"
    "v_add_f32 v186, v186, v187\n"
    "v_mfma_f32_16x16x32_bf16 v[108:111], a[92:95], v[228:231], v[108:111]\n"
    "v_add_f32 v184, v184, v186\n"
    "v_cvt_pk_bf16_f32 v224, v176, v177\n"
    "v_mfma_f32_16x16x32_bf16 v[112:115], a[96:99], v[228:231], v[112:115]\n"
    "v_cvt_pk_bf16_f32 v225, v178, v179\n"
    "v_cmp_lt_f32 vcc, s77, v184\n"
    "v_mfma_f32_16x16x32_bf16 v[116:119], a[100:103], v[228:231], v[116:119]\n"
    "v_cvt_pk_bf16_f32 v226, v180, v181\n"
    "v_cvt_pk_bf16_f32 v227, v182, v183\n"
    "v_mfma_f32_16x16x32_bf16 v[120:123], a[104:107], v[228:231], v[120:123]\n"
    "s_cbranch_vccnz L_rareA_l8%=\n"
    "L_rareA_l8_r%=:\n"
    "v_add_f32 v236, v236, v184\n"
    "v_mfma_f32_16x16x32_bf16 v[124:127], a[108:111], v[228:231], v[124:127]\n"
    "v_mfma_f32_16x16x32_bf16 v[160:163], a[144:147], a[16:19], v[196:199]\n"
    "v_exp_f32 v176, v168\n"
    "v_mfma_f32_16x16x32_bf16 v[164:167], a[160:163], a[16:19], v[196:199]\n"
    "v_exp_f32 v177, v169\n"
    "v_mfma_f32_16x16x32_bf16 v[160:163], a[148:151], a[20:23], v[160:163]\n"
    "v_exp_f32 v178, v170\n"
    "v_mfma_f32_16x16x32_bf16 v[164:167], a[164:167], a[20:23], v[164:167]\n"
    "v_exp_f32 v179, v171\n"
    "v_mfma_f32_16x16x32_bf16 v[160:163], a[152:155], a[24:27], v[160:163]\n"
    "v_exp_f32 v180, v172\n"
    "v_mfma_f32_16x16x32_bf16 v[164:167], a[168:171], a[24:27], v[164:167]\n"
    "v_exp_f32 v181, v173\n"
    "v_mfma_f32_16x16x32_bf16 v[160:163], a[156:159], a[28:31], v[160:163]\n"
    "v_exp_f32 v182, v174\n"
    "v_mfma_f32_16x16x32_bf16 v[164:167], a[172:175], a[28:31], v[164:167]\n"
    "v_exp_f32 v183, v175\n"
    "v_mfma_f32_16x16x32_bf16 v[128:131], a[80:83], v[224:227], v[128:131]\n"
    "ds_read_b64_tr_b16 a[80:81], v212 offset:8192\n"
    "v_add_f32 v184, v176, v177\n"
    "v_add_f32 v185, v178, v179\n"
    "v_mfma_f32_16x16x32_bf16 v[132:135], a[84:87], v[224:227], v[132:135]\n"
    "ds_read_b64_tr_b16 a[82:83], v212 offset:12288\n"
    "v_add_f32 v186, v180, v181\n"
    "v_add_f32 v187, v182, v183\n"
    "v_mfma_f32_16x16x32_bf16 v[136:139], a[88:91], v[224:227], v[136:139]\n"
    "ds_read_b64_tr_b16 a[84:85], v213 offset:8192\n"
    "v_add_f32 v184, v184, v185\n"
    "v_add_f32 v186, v186, v187\n"
    "v_mfma_f32_16x16x32_bf16 v[140:143], a[92:95], v[224:227], v[140:143]\n"
    "ds_read_b64_tr_b16 a[86:87], v213 offset:12288\n"
    "v_add_f32 v184, v184, v186\n"
    "v_cvt_pk_bf16_f32 v228, v176, v177\n"
    "v_mfma_f32_16x16x32_bf16 v[144:147], a[96:99], v[224:227], v[144:147]\n"
    "ds_read_b64_tr_b16 a[88:89], v214 offset:8192\n"
    "v_cvt_pk_bf16_f32 v229, v178, v179\n"
    "v_cmp_lt_f32 vcc, s77, v184\n"
    "v_mfma_f32_16x16x32_bf16 v[148:151], a[100:103], v[224:227], v[148:151]\n"
    "ds_read_b64_tr_b16 a[90:91], v214 offset:12288\n"
    "v_cvt_pk_bf16_f32 v230, v180, v181\n"
    "v_cvt_pk_bf16_f32 v231, v182, v183\n"
    "v_mfma_f32_16x16x32_bf16 v[152:155], a[104:107], v[224:227], v[152:155]\n"
    "ds_read_b64_tr_b16 a[92:93], v215 offset:8192\n"
    "s_cbranch_vccnz L_rareA_l9%=\n"
    "L_rareA_l9_r%=:\n"
    "v_add_f32 v232, v232, v184\n"
    "s_sub_u32 s82, s82, 1\n"
    "v_mfma_f32_16x16x32_bf16 v[156:159], a[108:111], v[224:227], v[156:159]\n"
    "ds_read_b64_tr_b16 a[94:95], v215 offset:12288\n"
    "s_cmp_lg_u32 s82, 0\n"
    "s_cbranch_scc1 L_attA%=\n"
    "v_mfma_f32_16x16x32_bf16 v[168:171], a[144:147], a[32:35], v[200:203]\n"
    "ds_read_b64_tr_b16 a[96:97], v216 offset:8192\n"
    "v_exp_f32 v176, v160\n"
    "v_mfma_f32_16x16x32_bf16 v[172:175], a[160:163], a[32:35], v[200:203]\n"
    "ds_read_b64_tr_b16 a[98:99], v216 offset:12288\n"
    "v_exp_f32 v177, v161\n"
    "v_mfma_f32_16x16x32_bf16 v[168:171], a[148:151], a[36:39], v[168:171]\n"
    "ds_read_b64_tr_b16 a[100:101], v217 offset:8192\n"
    "v_exp_f32 v178, v162\n"
    "v_mfma_f32_16x16x32_bf16 v[172:175], a[164:167], a[36:39], v[172:175]\n"
    "ds_read_b64_tr_b16 a[102:103], v217 offset:12288\n"
    "v_exp_f32 v179, v163\n"
    "v_mfma_f32_16x16x32_bf16 v[168:171], a[152:155], a[40:43], v[168:171]\n"
    "ds_read_b64_tr_b16 a[104:105], v218 offset:8192\n"
    "v_exp_f32 v180, v164\n"
    "v_mfma_f32_16x16x32_bf16 v[172:175], a[168:171], a[40:43], v[172:175]\n"
    "ds_read_b64_tr_b16 a[106:107], v218 offset:12288\n"
    "v_exp_f32 v181, v165\n"
    "v_mfma_f32_16x16x32_bf16 v[168:171], a[156:159], a[44:47], v[168:171]\n"
    "ds_read_b64_tr_b16 a[108:109], v219 offset:8192\n"
    "v_exp_f32 v182, v166\n"
    "v_mfma_f32_16x16x32_bf16 v[172:175], a[172:175], a[44:47], v[172:175]\n"
    "ds_read_b64_tr_b16 a[110:111], v219 offset:12288\n"
    "v_exp_f32 v183, v167\n"
    "s_waitcnt lgkmcnt(4)\n"
    "v_mfma_f32_16x16x32_bf16 v[0:3], a[80:83], v[228:231], v[0:3]\n"
    "v_add_f32 v184, v176, v177\n"
    "v_add_f32 v185, v178, v179\n"
    "v_mfma_f32_16x16x32_bf16 v[4:7], a[84:87], v[228:231], v[4:7]\n"
    "v_add_f32 v186, v180, v181\n"
    "v_add_f32 v187, v182, v183\n"
    "v_mfma_f32_16x16x32_bf16 v[8:11], a[88:91], v[228:231], v[8:11]\n"
    "v_add_f32 v184, v184, v185\n"
    "v_add_f32 v186, v186, v187\n"
    "v_mfma_f32_16x16x32_bf16 v[12:15], a[92:95], v[228:231], v[12:15]\n"
    "v_add_f32 v184, v184, v186\n"
    "v_cvt_pk_bf16_f32 v224, v176, v177\n"
    "v_mfma_f32_16x16x32_bf16 v[16:19], a[96:99], v[228:231], v[16:19]\n"
    "v_cvt_pk_bf16_f32 v225, v178, v179\n"
    "v_cmp_lt_f32 vcc, s77, v184\n"
    "v_mfma_f32_16x16x32_bf16 v[20:23], a[100:103], v[228:231], v[20:23]\n"
    "v_cvt_pk_bf16_f32 v226, v180, v181\n"
    "v_cvt_pk_bf16_f32 v227, v182, v183\n"
    "s_waitcnt lgkmcnt(2)\n"
    "v_mfma_f32_16x16x32_bf16 v[24:27], a[104:107], v[228:231], v[24:27]\n"
    "s_cbranch_vccnz L_rareA_e66%=\n"
    "L_rareA_e66_r%=:\n"
    "v_add_f32 v233, v233, v184\n"
    "s_waitcnt lgkmcnt(0)\n"
    "v_mfma_f32_16x16x32_bf16 v[28:31], a[108:111], v[228:231], v[28:31]\n"
    "s_waitcnt vmcnt(8)\n"
    "s_barrier\n"
    "v_mfma_f32_16x16x32_bf16 v[160:163], a[144:147], a[48:51], v[204:207]\n"
    "ds_read_b128 a[112:115], v220 offset:0\n"
    "v_exp_f32 v176, v168\n"
    "v_mfma_f32_16x16x32_bf16 v[164:167], a[160:163], a[48:51], v[204:207]\n"
    "ds_read_b128 a[128:131], v220 offset:4096\n"
    "v_exp_f32 v177, v169\n"
    "v_mfma_f32_16x16x32_bf16 v[160:163], a[148:151], a[52:55], v[160:163]\n"
    "ds_read_b128 a[116:119], v221 offset:0\n"
    "v_exp_f32 v178, v170\n"
    "v_mfma_f32_16x16x32_bf16 v[164:167], a[164:167], a[52:55], v[164:167]\n"
    "ds_read_b128 a[132:135], v221 offset:4096\n"
    "v_exp_f32 v179, v171\n"
    "v_mfma_f32_16x16x32_bf16 v[160:163], a[152:155], a[56:59], v[160:163]\n"
    "ds_read_b128 a[120:123], v222 offset:0\n"
    "v_exp_f32 v180, v172\n"
    "v_mfma_f32_16x16x32_bf16 v[164:167], a[168:171], a[56:59], v[164:167]\n"
    "ds_read_b128 a[136:139], v222 offset:4096\n"
    "v_exp_f32 v181, v173\n"
    "v_mfma_f32_16x16x32_bf16 v[160:163], a[156:159], a[60:63], v[160:163]\n"
    "ds_read_b128 a[124:127], v223 offset:0\n"
    "v_exp_f32 v182, v174\n"
    "v_mfma_f32_16x16x32_bf16 v[164:167], a[172:175], a[60:63], v[164:167]\n"
    "ds_read_b128 a[140:143], v223 offset:4096\n"
    "v_exp_f32 v183, v175\n"
    "v_mfma_f32_16x16x32_bf16 v[32:35], a[80:83], v[224:227], v[32:35]\n"
    "v_add_f32 v184, v176, v177\n"
    "v_add_f32 v185, v178, v179\n"
    "v_mfma_f32_16x16x32_bf16 v[36:39], a[84:87], v[224:227], v[36:39]\n"
    "v_add_f32 v186, v180, v181\n"
    "v_add_f32 v187, v182, v183\n"
    "v_mfma_f32_16x16x32_bf16 v[40:43], a[88:91], v[224:227], v[40:43]\n"
    "v_add_f32 v184, v184, v185\n"
    "v_add_f32 v186, v186, v187\n"
    "v_mfma_f32_16x16x32_bf16 v[44:47], a[92:95], v[224:227], v[44:47]\n"
    "v_add_f32 v184, v184, v186\n"
    "v_cvt_pk_bf16_f32 v228, v176, v177\n"
    "v_mfma_f32_16x16x32_bf16 v[48:51], a[96:99], v[224:227], v[48:51]\n"
    "v_cvt_pk_bf16_f32 v229, v178, v179\n"
    "v_cmp_lt_f32 vcc, s77, v184\n"
    "v_mfma_f32_16x16x32_bf16 v[52:55], a[100:103], v[224:227], v[52:55]\n"
    "v_cvt_pk_bf16_f32 v230, v180, v181\n"
    "v_cvt_pk_bf16_f32 v231, v182, v183\n"
    "v_mfma_f32_16x16x32_bf16 v[56:59], a[104:107], v[224:227], v[56:59]\n"
    "s_cbranch_vccnz L_rareA_e67%=\n"
    "L_rareA_e67_r%=:\n"
    "v_add_f32 v234, v234, v184\n"
    "v_mfma_f32_16x16x32_bf16 v[60:63], a[108:111], v[224:227], v[60:63]\n"
    "s_waitcnt lgkmcnt(0)\n"
    "v_mfma_f32_16x16x32_bf16 v[168:171], a[144:147], a[64:67], v[208:211]\n"
    "v_exp_f32 v176, v160\n"
    "v_mfma_f32_16x16x32_bf16 v[172:175], a[160:163], a[64:67], v[208:211]\n"
    "v_exp_f32 v177, v161\n"
    "v_mfma_f32_16x16x32_bf16 v[168:171], a[148:151], a[68:71], v[168:171]\n"
    "v_exp_f32 v178, v162\n"
    "v_mfma_f32_16x16x32_bf16 v[172:175], a[164:167], a[68:71], v[172:175]\n"
    "v_exp_f32 v179, v163\n"
    "v_mfma_f32_16x16x32_bf16 v[168:171], a[152:155], a[72:75], v[168:171]\n"
    "v_exp_f32 v180, v164\n"
    "v_mfma_f32_16x16x32_bf16 v[172:175], a[168:171], a[72:75], v[172:175]\n"
    "v_exp_f32 v181, v165\n"
    "v_mfma_f32_16x16x32_bf16 v[168:171], a[156:159], a[76:79], v[168:171]\n"
    "v_exp_f32 v182, v166\n"
    "v_mfma_f32_16x16x32_bf16 v[172:175], a[172:175], a[76:79], v[172:175]\n"
    "v_exp_f32 v183, v167\n"
    "v_mfma_f32_16x16x32_bf16 v[64:67], a[80:83], v[228:231], v[64:67]\n"
    "v_add_f32 v184, v176, v177\n"
    "v_add_f32 v185, v178, v179\n"
    "v_mfma_f32_16x16x32_bf16 v[68:71], a[84:87], v[228:231], v[68:71]\n"
    "v_add_f32 v186, v180, v181\n"
    "v_add_f32 v187, v182, v183\n"
    "v_mfma_f32_16x16x32_bf16 v[72:75], a[88:91], v[228:231], v[72:75]\n"
    "v_add_f32 v184, v184, v185\n"
    "v_add_f32 v186, v186, v187\n"
    "v_mfma_f32_16x16x32_bf16 v[76:79], a[92:95], v[228:231], v[76:79]\n"
    "v_add_f32 v184, v184, v186\n"
    "v_cvt_pk_bf16_f32 v224, v176, v177\n"
    "v_mfma_f32_16x16x32_bf16 v[80:83], a[96:99], v[228:231], v[80:83]\n"
    "v_cvt_pk_bf16_f32 v225, v178, v179\n"
    "v_cmp_lt_f32 vcc, s77, v184\n"
    "v_mfma_f32_16x16x32_bf16 v[84:87], a[100:103], v[228:231], v[84:87]\n"
    "v_cvt_pk_bf16_f32 v226, v180, v181\n"
    "v_cvt_pk_bf16_f32 v227, v182, v183\n"
    "v_mfma_f32_16x16x32_bf16 v[88:91], a[104:107], v[228:231], v[88:91]\n"
    "s_cbranch_vccnz L_rareA_e68%=\n"
    "L_rareA_e68_r%=:\n"
    "v_add_f32 v235, v235, v184\n"
    "v_mfma_f32_16x16x32_bf16 v[92:95], a[108:111], v[228:231], v[92:95]\n"
    "v_mfma_f32_16x16x32_bf16 v[160:163], a[112:115], a[0:3], v[192:195]\n"
    "v_exp_f32 v176, v168\n"
    "v_mfma_f32_16x16x32_bf16 v[164:167], a[128:131], a[0:3], v[192:195]\n"
    "v_exp_f32 v177, v169\n"
    "v_mfma_f32_16x16x32_bf16 v[160:163], a[116:119], a[4:7], v[160:163]\n"
    "v_exp_f32 v178, v170\n"
    "v_mfma_f32_16x16x32_bf16 v[164:167], a[132:135], a[4:7], v[164:167]\n"
    "v_exp_f32 v179, v171\n"
    "v_mfma_f32_16x16x32_bf16 v[160:163], a[120:123], a[8:11], v[160:163]\n"
    "v_exp_f32 v180, v172\n"
    "v_mfma_f32_16x16x32_bf16 v[164:167], a[136:139], a[8:11], v[164:167]\n"
    "v_exp_f32 v181, v173\n"
    "v_mfma_f32_16x16x32_bf16 v[160:163], a[124:127], a[12:15], v[160:163]\n"
    "v_exp_f32 v182, v174\n"
    "v_mfma_f32_16x16x32_bf16 v[164:167], a[140:143], a[12:15], v[164:167]\n"
    "v_exp_f32 v183, v175\n"
    "v_mfma_f32_16x16x32_bf16 v[96:99], a[80:83], v[224:227], v[96:99]\n"
    "v_add_f32 v184, v176, v177\n"
    "v_add_f32 v185, v178, v179\n"
    "v_xor_b32 v212, 32768, v212\n"
    "v_mfma_f32_16x16x32_bf16 v[100:103], a[84:87], v[224:227], v[100:103]\n"
    "v_add_f32 v186, v180, v181\n"
    "v_add_f32 v187, v182, v183\n"
    "v_xor_b32 v213, 32768, v213\n"
    "v_mfma_f32_16x16x32_bf16 v[104:107], a[88:91], v[224:227], v[104:107]\n"
    "v_add_f32 v184, v184, v185\n"
    "v_add_f32 v186, v186, v187\n"
    "v_xor_b32 v214, 32768, v214\n"
    "v_mfma_f32_16x16x32_bf16 v[108:111], a[92:95], v[224:227], v[108:111]\n"
    "v_add_f32 v184, v184, v186\n"
    "v_cvt_pk_bf16_f32 v228, v176, v177\n"
    "v_xor_b32 v215, 32768, v215\n"
    "v_mfma_f32_16x16x32_bf16 v[112:115], a[96:99], v[224:227], v[112:115]\n"
    "v_cvt_pk_bf16_f32 v229, v178, v179\n"
    "v_cmp_lt_f32 vcc, s77, v184\n"
    "v_xor_b32 v216, 32768, v216\n"
    "v_mfma_f32_16x16x32_bf16 v[116:119], a[100:103], v[224:227], v[116:119]\n"
    "v_cvt_pk_bf16_f32 v230, v180, v181\n"
    "v_cvt_pk_bf16_f32 v231, v182, v183\n"
    "v_xor_b32 v217, 32768, v217\n"
    "v_mfma_f32_16x16x32_bf16 v[120:123], a[104:107], v[224:227], v[120:123]\n"
    "s_cbranch_vccnz L_rareA_e69%=\n"
    "L_rareA_e69_r%=:\n"
    "v_add_f32 v236, v236, v184\n"
    "v_xor_b32 v218, 32768, v218\n"
    "v_mfma_f32_16x16x32_bf16 v[124:127], a[108:111], v[224:227], v[124:127]\n"
    "v_xor_b32 v219, 32768, v219\n"
    "v_mfma_f32_16x16x32_bf16 v[168:171], a[112:115], a[16:19], v[196:199]\n"
    "v_exp_f32 v176, v160\n"
    "v_mfma_f32_16x16x32_bf16 v[172:175], a[128:131], a[16:19], v[196:199]\n"
    "v_exp_f32 v177, v161\n"
    "v_mfma_f32_16x16x32_bf16 v[168:171], a[116:119], a[20:23], v[168:171]\n"
    "v_exp_f32 v178, v162\n"
    "v_mfma_f32_16x16x32_bf16 v[172:175], a[132:135], a[20:23], v[172:175]\n"
    "v_exp_f32 v179, v163\n"
    "v_mfma_f32_16x16x32_bf16 v[168:171], a[120:123], a[24:27], v[168:171]\n"
    "v_exp_f32 v180, v164\n"
    "v_mfma_f32_16x16x32_bf16 v[172:175], a[136:139], a[24:27], v[172:175]\n"
    "v_exp_f32 v181, v165\n"
    "v_mfma_f32_16x16x32_bf16 v[168:171], a[124:127], a[28:31], v[168:171]\n"
    "v_exp_f32 v182, v166\n"
    "v_mfma_f32_16x16x32_bf16 v[172:175], a[140:143], a[28:31], v[172:175]\n"
    "v_exp_f32 v183, v167\n"
    "v_mfma_f32_16x16x32_bf16 v[128:131], a[80:83], v[228:231], v[128:131]\n"
    "ds_read_b64_tr_b16 a[80:81], v212 offset:0\n"
    "v_add_f32 v184, v176, v177\n"
    "v_add_f32 v185, v178, v179\n"
    "v_mfma_f32_16x16x32_bf16 v[132:135], a[84:87], v[228:231], v[132:135]\n"
    "ds_read_b64_tr_b16 a[82:83], v212 offset:4096\n"
    "v_add_f32 v186, v180, v181\n"
    "v_add_f32 v187, v182, v183\n"
    "v_mfma_f32_16x16x32_bf16 v[136:139], a[88:91], v[228:231], v[136:139]\n"
    "ds_read_b64_tr_b16 a[84:85], v213 offset:0\n"
    "v_add_f32 v184, v184, v185\n"
    "v_add_f32 v186, v186, v187\n"
    "v_mfma_f32_16x16x32_bf16 v[140:143], a[92:95], v[228:231], v[140:143]\n"
    "ds_read_b64_tr_b16 a[86:87], v213 offset:4096\n"
    "v_add_f32 v184, v184, v186\n"
    "v_cvt_pk_bf16_f32 v224, v176, v177\n"
    "v_mfma_f32_16x16x32_bf16 v[144:147], a[96:99], v[228:231], v[144:147]\n"
    "ds_read_b64_tr_b16 a[88:89], v214 offset:0\n"
    "v_cvt_pk_bf16_f32 v225, v178, v179\n"
    "v_cmp_lt_f32 vcc, s77, v184\n"
    "v_mfma_f32_16x16x32_bf16 v[148:151], a[100:103], v[228:231], v[148:151]\n"
    "ds_read_b64_tr_b16 a[90:91], v214 offset:4096\n"
    "v_cvt_pk_bf16_f32 v226, v180, v181\n"
    "v_cvt_pk_bf16_f32 v227, v182, v183\n"
    "v_mfma_f32_16x16x32_bf16 v[152:155], a[104:107], v[228:231], v[152:155]\n"
    "ds_read_b64_tr_b16 a[92:93], v215 offset:0\n"
    "s_cbranch_vccnz L_rareA_e70%=\n"
    "L_rareA_e70_r%=:\n"
    "v_add_f32 v232, v232, v184\n"
    "v_mfma_f32_16x16x32_bf16 v[156:159], a[108:111], v[228:231], v[156:159]\n"
    "ds_read_b64_tr_b16 a[94:95], v215 offset:4096\n"
    "v_mfma_f32_16x16x32_bf16 v[160:163], a[112:115], a[32:35], v[200:203]\n"
    "ds_read_b64_tr_b16 a[96:97], v216 offset:0\n"
    "v_exp_f32 v176, v168\n"
    "v_mfma_f32_16x16x32_bf16 v[164:167], a[128:131], a[32:35], v[200:203]\n"
    "ds_read_b64_tr_b16 a[98:99], v216 offset:4096\n"
    "v_exp_f32 v177, v169\n"
    "v_mfma_f32_16x16x32_bf16 v[160:163], a[116:119], a[36:39], v[160:163]\n"
    "ds_read_b64_tr_b16 a[100:101], v217 offset:0\n"
    "v_exp_f32 v178, v170\n"
    "v_mfma_f32_16x16x32_bf16 v[164:167], a[132:135], a[36:39], v[164:167]\n"
    "ds_read_b64_tr_b16 a[102:103], v217 offset:4096\n"
    "v_exp_f32 v179, v171\n"
    "v_mfma_f32_16x16x32_bf16 v[160:163], a[120:123], a[40:43], v[160:163]\n"
    "ds_read_b64_tr_b16 a[104:105], v218 offset:0\n"
    "v_exp_f32 v180, v172\n"
    "v_mfma_f32_16x16x32_bf16 v[164:167], a[136:139], a[40:43], v[164:167]\n"
    "ds_read_b64_tr_b16 a[106:107], v218 offset:4096\n"
    "v_exp_f32 v181, v173\n"
    "v_mfma_f32_16x16x32_bf16 v[160:163], a[124:127], a[44:47], v[160:163]\n"
    "ds_read_b64_tr_b16 a[108:109], v219 offset:0\n"
    "v_exp_f32 v182, v174\n"
    "v_mfma_f32_16x16x32_bf16 v[164:167], a[140:143], a[44:47], v[164:167]\n"
    "ds_read_b64_tr_b16 a[110:111], v219 offset:4096\n"
    "v_exp_f32 v183, v175\n"
    "s_waitcnt lgkmcnt(4)\n"
    "v_mfma_f32_16x16x32_bf16 v[0:3], a[80:83], v[224:227], v[0:3]\n"
    "v_add_f32 v184, v176, v177\n"
    "v_add_f32 v185, v178, v179\n"
    "v_mfma_f32_16x16x32_bf16 v[4:7], a[84:87], v[224:227], v[4:7]\n"
    "v_add_f32 v186, v180, v181\n"
    "v_add_f32 v187, v182, v183\n"
    "v_mfma_f32_16x16x32_bf16 v[8:11], a[88:91], v[224:227], v[8:11]\n"
    "v_add_f32 v184, v184, v185\n"
    "v_add_f32 v186, v186, v187\n"
    "v_mfma_f32_16x16x32_bf16 v[12:15], a[92:95], v[224:227], v[12:15]\n"
    "v_add_f32 v184, v184, v186\n"
    "v_cvt_pk_bf16_f32 v228, v176, v177\n"
    "v_mfma_f32_16x16x32_bf16 v[16:19], a[96:99], v[224:227], v[16:19]\n"
    "v_cvt_pk_bf16_f32 v229, v178, v179\n"
    "v_cmp_lt_f32 vcc, s77, v184\n"
    "v_mfma_f32_16x16x32_bf16 v[20:23], a[100:103], v[224:227], v[20:23]\n"
    "v_cvt_pk_bf16_f32 v230, v180, v181\n"
    "v_cvt_pk_bf16_f32 v231, v182, v183\n"
    "s_waitcnt lgkmcnt(2)\n"
    "v_mfma_f32_16x16x32_bf16 v[24:27], a[104:107], v[224:227], v[24:27]\n"
    "s_cbranch_vccnz L_rareA_e71%=\n"
    "L_rareA_e71_r%=:\n"
    "v_add_f32 v233, v233, v184\n"
    "s_waitcnt lgkmcnt(0)\n"
    "v_mfma_f32_16x16x32_bf16 v[28:31], a[108:111], v[224:227], v[28:31]\n"
    "s_waitcnt vmcnt(0)\n"
    "s_barrier\n"
    "v_mfma_f32_16x16x32_bf16 v[168:171], a[112:115], a[48:51], v[204:207]\n"
    "ds_read_b128 a[144:147], v220 offset:8192\n"
    "v_exp_f32 v176, v160\n"
    "v_mfma_f32_16x16x32_bf16 v[172:175], a[128:131], a[48:51], v[204:207]\n"
    "ds_read_b128 a[160:163], v220 offset:12288\n"
    "v_exp_f32 v177, v161\n"
    "v_mfma_f32_16x16x32_bf16 v[168:171], a[116:119], a[52:55], v[168:171]\n"
    "ds_read_b128 a[148:151], v221 offset:8192\n"
    "v_exp_f32 v178, v162\n"
    "v_mfma_f32_16x16x32_bf16 v[172:175], a[132:135], a[52:55], v[172:175]\n"
    "ds_read_b128 a[164:167], v221 offset:12288\n"
    "v_exp_f32 v179, v163\n"
    "v_mfma_f32_16x16x32_bf16 v[168:171], a[120:123], a[56:59], v[168:171]\n"
    "ds_read_b128 a[152:155], v222 offset:8192\n"
    "v_exp_f32 v180, v164\n"
    "v_mfma_f32_16x16x32_bf16 v[172:175], a[136:139], a[56:59], v[172:175]\n"
    "ds_read_b128 a[168:171], v222 offset:12288\n"
    "v_exp_f32 v181, v165\n"
    "v_mfma_f32_16x16x32_bf16 v[168:171], a[124:127], a[60:63], v[168:171]\n"
    "ds_read_b128 a[156:159], v223 offset:8192\n"
    "v_exp_f32 v182, v166\n"
    "v_mfma_f32_16x16x32_bf16 v[172:175], a[140:143], a[60:63], v[172:175]\n"
    "ds_read_b128 a[172:175], v223 offset:12288\n"
    "v_exp_f32 v183, v167\n"
    "v_mfma_f32_16x16x32_bf16 v[32:35], a[80:83], v[228:231], v[32:35]\n"
    "v_add_f32 v184, v176, v177\n"
    "v_add_f32 v185, v178, v179\n"
    "v_mfma_f32_16x16x32_bf16 v[36:39], a[84:87], v[228:231], v[36:39]\n"
    "v_add_f32 v186, v180, v181\n"
    "v_add_f32 v187, v182, v183\n"
    "v_mfma_f32_16x16x32_bf16 v[40:43], a[88:91], v[228:231], v[40:43]\n"
    "v_add_f32 v184, v184, v185\n"
    "v_add_f32 v186, v186, v187\n"
    "v_mfma_f32_16x16x32_bf16 v[44:47], a[92:95], v[228:231], v[44:47]\n"
    "v_add_f32 v184, v184, v186\n"
    "v_cvt_pk_bf16_f32 v224, v176, v177\n"
    "v_mfma_f32_16x16x32_bf16 v[48:51], a[96:99], v[228:231], v[48:51]\n"
    "v_cvt_pk_bf16_f32 v225, v178, v179\n"
    "v_cmp_lt_f32 vcc, s77, v184\n"
    "v_mfma_f32_16x16x32_bf16 v[52:55], a[100:103], v[228:231], v[52:55]\n"
    "v_cvt_pk_bf16_f32 v226, v180, v181\n"
    "v_cvt_pk_bf16_f32 v227, v182, v183\n"
    "v_mfma_f32_16x16x32_bf16 v[56:59], a[104:107], v[228:231], v[56:59]\n"
    "s_cbranch_vccnz L_rareA_e72%=\n"
    "L_rareA_e72_r%=:\n"
    "v_add_f32 v234, v234, v184\n"
    "v_mfma_f32_16x16x32_bf16 v[60:63], a[108:111], v[228:231], v[60:63]\n"
    "s_waitcnt lgkmcnt(0)\n"
    "v_mfma_f32_16x16x32_bf16 v[160:163], a[112:115], a[64:67], v[208:211]\n"
    "v_exp_f32 v176, v168\n"
    "v_mfma_f32_16x16x32_bf16 v[164:167], a[128:131], a[64:67], v[208:211]\n"
    "v_exp_f32 v177, v169\n"
    "v_mfma_f32_16x16x32_bf16 v[160:163], a[116:119], a[68:71], v[160:163]\n"
    "v_exp_f32 v178, v170\n"
    "v_mfma_f32_16x16x32_bf16 v[164:167], a[132:135], a[68:71], v[164:167]\n"
    "v_exp_f32 v179, v171\n"
    "v_mfma_f32_16x16x32_bf16 v[160:163], a[120:123], a[72:75], v[160:163]\n"
    "v_exp_f32 v180, v172\n"
    "v_mfma_f32_16x16x32_bf16 v[164:167], a[136:139], a[72:75], v[164:167]\n"
    "v_exp_f32 v181, v173\n"
    "v_mfma_f32_16x16x32_bf16 v[160:163], a[124:127], a[76:79], v[160:163]\n"
    "v_exp_f32 v182, v174\n"
    "v_mfma_f32_16x16x32_bf16 v[164:167], a[140:143], a[76:79], v[164:167]\n"
    "v_exp_f32 v183, v175\n"
    "v_mfma_f32_16x16x32_bf16 v[64:67], a[80:83], v[224:227], v[64:67]\n"
    "v_add_f32 v184, v176, v177\n"
    "v_add_f32 v185, v178, v179\n"
    "v_mfma_f32_16x16x32_bf16 v[68:71], a[84:87], v[224:227], v[68:71]\n"
    "v_add_f32 v186, v180, v181\n"
    "v_add_f32 v187, v182, v183\n"
    "v_mfma_f32_16x16x32_bf16 v[72:75], a[88:91], v[224:227], v[72:75]\n"
    "v_add_f32 v184, v184, v185\n"
    "v_add_f32 v186, v186, v187\n"
    "v_mfma_f32_16x16x32_bf16 v[76:79], a[92:95], v[224:227], v[76:79]\n"
    "v_add_f32 v184, v184, v186\n"
    "v_cvt_pk_bf16_f32 v228, v176, v177\n"
    "v_mfma_f32_16x16x32_bf16 v[80:83], a[96:99], v[224:227], v[80:83]\n"
    "v_cvt_pk_bf16_f32 v229, v178, v179\n"
    "v_cmp_lt_f32 vcc, s77, v184\n"
    "v_mfma_f32_16x16x32_bf16 v[84:87], a[100:103], v[224:227], v[84:87]\n"
    "v_cvt_pk_bf16_f32 v230, v180, v181\n"
    "v_cvt_pk_bf16_f32 v231, v182, v183\n"
    "v_mfma_f32_16x16x32_bf16 v[88:91], a[104:107], v[224:227], v[88:91]\n"
    "s_cbranch_vccnz L_rareA_e73%=\n"
    "L_rareA_e73_r%=:\n"
    "v_add_f32 v235, v235, v184\n"
    "v_mfma_f32_16x16x32_bf16 v[92:95], a[108:111], v[224:227], v[92:95]\n"
    "v_mfma_f32_16x16x32_bf16 v[168:171], a[144:147], a[0:3], v[192:195]\n"
    "v_exp_f32 v176, v160\n"
    "v_mfma_f32_16x16x32_bf16 v[172:175], a[160:163], a[0:3], v[192:195]\n"
    "v_exp_f32 v177, v161\n"
    "v_mfma_f32_16x16x32_bf16 v[168:171], a[148:151], a[4:7], v[168:171]\n"
    "v_exp_f32 v178, v162\n"
    "v_mfma_f32_16x16x32_bf16 v[172:175], a[164:167], a[4:7], v[172:175]\n"
    "v_exp_f32 v179, v163\n"
    "v_mfma_f32_16x16x32_bf16 v[168:171], a[152:155], a[8:11], v[168:171]\n"
    "v_exp_f32 v180, v164\n"
    "v_mfma_f32_16x16x32_bf16 v[172:175], a[168:171], a[8:11], v[172:175]\n"
    "v_exp_f32 v181, v165\n"
    "v_mfma_f32_16x16x32_bf16 v[168:171], a[156:159], a[12:15], v[168:171]\n"
    "v_exp_f32 v182, v166\n"
    "v_mfma_f32_16x16x32_bf16 v[172:175], a[172:175], a[12:15], v[172:175]\n"
    "v_exp_f32 v183, v167\n"
    "v_mfma_f32_16x16x32_bf16 v[96:99], a[80:83], v[228:231], v[96:99]\n"
    "v_add_f32 v184, v176, v177\n"
    "v_add_f32 v185, v178, v179\n"
    "v_mfma_f32_16x16x32_bf16 v[100:103], a[84:87], v[228:231], v[100:103]\n"
    "v_add_f32 v186, v180, v181\n"
    "v_add_f32 v187, v182, v183\n"
    "v_mfma_f32_16x16x32_bf16 v[104:107], a[88:91], v[228:231], v[104:107]\n"
    "v_add_f32 v184, v184, v185\n"
    "v_add_f32 v186, v186, v187\n"
    "v_mfma_f32_16x16x32_bf16 v[108:111], a[92:95], v[228:231], v[108:111]\n"
    "v_add_f32 v184, v184, v186\n"
    "v_cvt_pk_bf16_f32 v224, v176, v177\n"
    "v_mfma_f32_16x16x32_bf16 v[112:115], a[96:99], v[228:231], v[112:115]\n"
    "v_cvt_pk_bf16_f32 v225, v178, v179\n"
    "v_cmp_lt_f32 vcc, s77, v184\n"
    "v_mfma_f32_16x16x32_bf16 v[116:119], a[100:103], v[228:231], v[116:119]\n"
    "v_cvt_pk_bf16_f32 v226, v180, v181\n"
    "v_cvt_pk_bf16_f32 v227, v182, v183\n"
    "v_mfma_f32_16x16x32_bf16 v[120:123], a[104:107], v[228:231], v[120:123]\n"
    "s_cbranch_vccnz L_rareA_e74%=\n"
    "L_rareA_e74_r%=:\n"
    "v_add_f32 v236, v236, v184\n"
    "v_mfma_f32_16x16x32_bf16 v[124:127], a[108:111], v[228:231], v[124:127]\n"
    "v_mfma_f32_16x16x32_bf16 v[160:163], a[144:147], a[16:19], v[196:199]\n"
    "v_exp_f32 v176, v168\n"
    "v_add_u32 v212, 8192, v212\n"
    "v_mfma_f32_16x16x32_bf16 v[164:167], a[160:163], a[16:19], v[196:199]\n"
    "v_exp_f32 v177, v169\n"
    "v_add_u32 v213, 8192, v213\n"
    "v_mfma_f32_16x16x32_bf16 v[160:163], a[148:151], a[20:23], v[160:163]\n"
    "v_exp_f32 v178, v170\n"
    "v_add_u32 v214, 8192, v214\n"
    "v_mfma_f32_16x16x32_bf16 v[164:167], a[164:167], a[20:23], v[164:167]\n"
    "v_exp_f32 v179, v171\n"
    "v_add_u32 v215, 8192, v215\n"
    "v_mfma_f32_16x16x32_bf16 v[160:163], a[152:155], a[24:27], v[160:163]\n"
    "v_exp_f32 v180, v172\n"
    "v_add_u32 v216, 8192, v216\n"
    "v_mfma_f32_16x16x32_bf16 v[164:167], a[168:171], a[24:27], v[164:167]\n"
    "v_exp_f32 v181, v173\n"
    "v_add_u32 v217, 8192, v217\n"
    "v_mfma_f32_16x16x32_bf16 v[160:163], a[156:159], a[28:31], v[160:163]\n"
    "v_exp_f32 v182, v174\n"
    "v_add_u32 v218, 8192, v218\n"
    "v_mfma_f32_16x16x32_bf16 v[164:167], a[172:175], a[28:31], v[164:167]\n"
    "v_exp_f32 v183, v175\n"
    "v_add_u32 v219, 8192, v219\n"
    "v_mfma_f32_16x16x32_bf16 v[128:131], a[80:83], v[224:227], v[128:131]\n"
    "v_add_f32 v184, v176, v177\n"
    "v_add_f32 v185, v178, v179\n"
    "v_mfma_f32_16x16x32_bf16 v[132:135], a[84:87], v[224:227], v[132:135]\n"
    "v_add_f32 v186, v180, v181\n"
    "v_add_f32 v187, v182, v183\n"
    "v_mfma_f32_16x16x32_bf16 v[136:139], a[88:91], v[224:227], v[136:139]\n"
    "v_add_f32 v184, v184, v185\n"
    "v_add_f32 v186, v186, v187\n"
    "v_mfma_f32_16x16x32_bf16 v[140:143], a[92:95], v[224:227], v[140:143]\n"
    "v_add_f32 v184, v184, v186\n"
    "v_cvt_pk_bf16_f32 v228, v176, v177\n"
    "v_mfma_f32_16x16x32_bf16 v[144:147], a[96:99], v[224:227], v[144:147]\n"
    "v_cvt_pk_bf16_f32 v229, v178, v179\n"
    "v_cmp_lt_f32 vcc, s77, v184\n"
    "v_mfma_f32_16x16x32_bf16 v[148:151], a[100:103], v[224:227], v[148:151]\n"
    "v_cvt_pk_bf16_f32 v230, v180, v181\n"
    "v_cvt_pk_bf16_f32 v231, v182, v183\n"
    "v_mfma_f32_16x16x32_bf16 v[152:155], a[104:107], v[224:227], v[152:155]\n"
    "s_cbranch_vccnz L_rareA_e75%=\n"
    "L_rareA_e75_r%=:\n"
    "v_add_f32 v232, v232, v184\n"
    "v_mfma_f32_16x16x32_bf16 v[156:159], a[108:111], v[224:227], v[156:159]\n"
    "s_branch L_tail%=\n"
    "L_varB%=:\n"
    "v_mfma_f32_16x16x32_bf16 v[0:3], v[224:227], a[0:3], v[0:3]\n"
    "v_perm_b32 v232, v165, v161, s75\n"
    "v_perm_b32 v233, v173, v169, s75\n"
    "v_mfma_f32_16x16x32_bf16 v[32:35], v[224:227], a[4:7], v[32:35]\n"
    "v_perm_b32 v234, v181, v177, s75\n"
    "v_perm_b32 v235, v189, v185, s75\n"
    "v_mfma_f32_16x16x32_bf16 v[64:67], v[224:227], a[8:11], v[64:67]\n"
    "buffer_load_dwordx4 v[136:139], v244, s[40:43], s69 offen\n"
    "v_mfma_f32_16x16x32_bf16 v[96:99], v[224:227], a[12:15], v[96:99]\n"
    "v_perm_b32 v236, v165, v161, s76\n"
    "v_perm_b32 v237, v173, v169, s76\n"
    "v_mfma_f32_16x16x32_bf16 a[176:179], v[224:227], a[16:19], a[176:179]\n"
    "v_perm_b32 v238, v181, v177, s76\n"
    "v_perm_b32 v239, v189, v185, s76\n"
    "v_mfma_f32_16x16x32_bf16 v[4:7], v[228:231], a[0:3], v[4:7]\n"
    "v_mfma_f32_16x16x32_bf16 v[36:39], v[228:231], a[4:7], v[36:39]\n"
    "v_mfma_f32_16x16x32_bf16 v[68:71], v[228:231], a[8:11], v[68:71]\n"
    "buffer_load_dwordx4 v[140:143], v244, s[40:43], s70 offen\n"
    "v_mfma_f32_16x16x32_bf16 v[100:103], v[228:231], a[12:15], v[100:103]\n"
    "ds_read_b128 a[40:43], v246 offset:0\n"
    "v_mfma_f32_16x16x32_bf16 a[180:183], v[228:231], a[16:19], a[180:183]\n"
    "ds_read_b128 a[44:47], v246 offset:2048\n"
    "v_mfma_f32_16x16x32_bf16 v[8:11], v[232:235], a[0:3], v[8:11]\n"
    "v_perm_b32 v224, v166, v162, s75\n"
    "v_perm_b32 v225, v174, v170, s75\n"
    "v_mfma_f32_16x16x32_bf16 v[40:43], v[232:235], a[4:7], v[40:43]\n"
    "v_perm_b32 v226, v182, v178, s75\n"
    "v_perm_b32 v227, v190, v186, s75\n"
    "v_mfma_f32_16x16x32_bf16 v[72:75], v[232:235], a[8:11], v[72:75]\n"
    "buffer_load_dwordx4 v[144:147], v244, s[40:43], s71 offen\n"
    "v_mfma_f32_16x16x32_bf16 v[104:107], v[232:235], a[12:15], v[104:107]\n"
    "v_perm_b32 v228, v166, v162, s76\n"
    "v_perm_b32 v229, v174, v170, s76\n"
    "v_mfma_f32_16x16x32_bf16 a[184:187], v[232:235], a[16:19], a[184:187]\n"
    "v_perm_b32 v230, v182, v178, s76\n"
    "v_perm_b32 v231, v190, v186, s76\n"
    "v_mfma_f32_16x16x32_bf16 v[12:15], v[236:239], a[0:3], v[12:15]\n"
    "ds_read_b128 a[48:51], v246 offset:4096\n"
    "v_mfma_f32_16x16x32_bf16 v[44:47], v[236:239], a[4:7], v[44:47]\n"
    "ds_read_b128 a[52:55], v246 offset:6144\n"
    "v_mfma_f32_16x16x32_bf16 v[76:79], v[236:239], a[8:11], v[76:79]\n"
    "buffer_load_dwordx4 v[148:151], v244, s[40:43], s72 offen\n"
    "v_mfma_f32_16x16x32_bf16 v[108:111], v[236:239], a[12:15], v[108:111]\n"
    "ds_read_b128 a[56:59], v246 offset:8192\n"
    "v_mfma_f32_16x16x32_bf16 a[188:191], v[236:239], a[16:19], a[188:191]\n"
    "v_mfma_f32_16x16x32_bf16 v[16:19], v[224:227], a[0:3], v[16:19]\n"
    "v_perm_b32 v232, v167, v163, s75\n"
    "v_perm_b32 v233, v175, v171, s75\n"
    "v_mfma_f32_16x16x32_bf16 v[48:51], v[224:227], a[4:7], v[48:51]\n"
    "v_perm_b32 v234, v183, v179, s75\n"
    "v_perm_b32 v235, v191, v187, s75\n"
    "v_mfma_f32_16x16x32_bf16 v[80:83], v[224:227], a[8:11], v[80:83]\n"
    "buffer_load_dwordx4 v[152:155], v244, s[40:43], s73 offen\n"
    "v_mfma_f32_16x16x32_bf16 v[112:115], v[224:227], a[12:15], v[112:115]\n"
    "v_perm_b32 v236, v167, v163, s76\n"
    "v_perm_b32 v237, v175, v171, s76\n"
    "v_mfma_f32_16x16x32_bf16 a[192:195], v[224:227], a[16:19], a[192:195]\n"
    "v_perm_b32 v238, v183, v179, s76\n"
    "v_perm_b32 v239, v191, v187, s76\n"
    "v_mfma_f32_16x16x32_bf16 v[20:23], v[228:231], a[0:3], v[20:23]\n"
    "v_mfma_f32_16x16x32_bf16 v[52:55], v[228:231], a[4:7], v[52:55]\n"
    "v_mfma_f32_16x16x32_bf16 v[84:87], v[228:231], a[8:11], v[84:87]\n"
    "buffer_load_dwordx4 v[156:159], v244, s[40:43], s74 offen\n"
    "v_mfma_f32_16x16x32_bf16 v[116:119], v[228:231], a[12:15], v[116:119]\n"
    "v_lshrrev_b32 v160, 4, v240\n"
    "v_and_b32 v161, 15, v240\n"
    "v_mfma_f32_16x16x32_bf16 a[196:199], v[228:231], a[16:19], a[196:199]\n"
    "s_waitcnt vmcnt(8)\n"
    "v_lshlrev_b32 v161, 4, v161\n"
    "v_lshl_add_u32 v161, v160, 8, v161\n"
    "v_mfma_f32_16x16x32_bf16 v[24:27], v[232:235], a[0:3], v[24:27]\n"
    "v_perm_b32 v224, v196, v192, s75\n"
    "v_perm_b32 v225, v204, v200, s75\n"
    "v_lshlrev_b32 v160, 6, v160\n"
    "v_xor_b32 v188, v160, v161\n"
    "v_mfma_f32_16x16x32_bf16 v[56:59], v[232:235], a[4:7], v[56:59]\n"
    "v_perm_b32 v226, v212, v208, s75\n"
    "v_perm_b32 v227, v220, v216, s75\n"
    "v_xor_b32 v189, 16, v188\n"
    "v_xor_b32 v190, 32, v188\n"
    "v_mfma_f32_16x16x32_bf16 v[88:91], v[232:235], a[8:11], v[88:91]\n"
    "v_perm_b32 v228, v196, v192, s76\n"
    "v_perm_b32 v229, v204, v200, s76\n"
    "v_xor_b32 v191, 48, v188\n"
    "s_sub_u32 s71, s86, 4096\n"
    "v_mfma_f32_16x16x32_bf16 v[120:123], v[232:235], a[12:15], v[120:123]\n"
    "v_perm_b32 v230, v212, v208, s76\n"
    "v_perm_b32 v231, v220, v216, s76\n"
    "s_sub_u32 s85, s86, 4096\n"
    "s_mov_b32 s74, s99\n"
    "v_mfma_f32_16x16x32_bf16 a[200:203], v[232:235], a[16:19], a[200:203]\n"
    "v_mfma_f32_16x16x32_bf16 v[28:31], v[236:239], a[0:3], v[28:31]\n"
    "v_mfma_f32_16x16x32_bf16 v[60:63], v[236:239], a[4:7], v[60:63]\n"
    "v_mfma_f32_16x16x32_bf16 v[92:95], v[236:239], a[8:11], v[92:95]\n"
    "v_mfma_f32_16x16x32_bf16 v[124:127], v[236:239], a[12:15], v[124:127]\n"
    "v_mfma_f32_16x16x32_bf16 a[204:207], v[236:239], a[16:19], a[204:207]\n"
    "s_waitcnt lgkmcnt(0)\n"
    "v_mfma_f32_16x16x32_bf16 v[0:3], v[224:227], a[20:23], v[0:3]\n"
    "s_barrier\n"
    "v_mfma_f32_16x16x32_bf16 v[32:35], v[224:227], a[24:27], v[32:35]\n"
    "v_perm_b32 v232, v197, v193, s75\n"
    "v_perm_b32 v233, v205, v201, s75\n"
    "s_add_u32 m0, s74, 0\n"
    "s_add_u32 s71, s71, 4096\n"
    "s_and_b32 s71, s71, 0x1ffff\n"
    "v_mfma_f32_16x16x32_bf16 v[64:67], v[224:227], a[28:31], v[64:67]\n"
    "buffer_load_dwordx4 v188, s[44:47], s71 offen offset:0 nt lds\n"
    "v_mfma_f32_16x16x32_bf16 v[96:99], v[224:227], a[32:35], v[96:99]\n"
    "v_perm_b32 v234, v213, v209, s75\n"
    "v_perm_b32 v235, v221, v217, s75\n"
    "v_mfma_f32_16x16x32_bf16 a[176:179], v[224:227], a[36:39], a[176:179]\n"
    "v_perm_b32 v236, v197, v193, s76\n"
    "v_perm_b32 v237, v205, v201, s76\n"
    "v_mfma_f32_16x16x32_bf16 v[4:7], v[228:231], a[20:23], v[4:7]\n"
    "v_perm_b32 v238, v213, v209, s76\n"
    "v_perm_b32 v239, v221, v217, s76\n"
    "v_mfma_f32_16x16x32_bf16 v[36:39], v[228:231], a[24:27], v[36:39]\n"
    "buffer_load_dwordx4 v189, s[44:47], s71 offen offset:1024 nt lds\n"
    "v_mfma_f32_16x16x32_bf16 v[68:71], v[228:231], a[28:31], v[68:71]\n"
    "v_mfma_f32_16x16x32_bf16 v[100:103], v[228:231], a[32:35], v[100:103]\n"
    "v_mfma_f32_16x16x32_bf16 a[180:183], v[228:231], a[36:39], a[180:183]\n"
    "v_mfma_f32_16x16x32_bf16 v[8:11], v[232:235], a[20:23], v[8:11]\n"
    "buffer_load_dwordx4 v190, s[44:47], s71 offen offset:2048 nt lds\n"
    "v_mfma_f32_16x16x32_bf16 v[40:43], v[232:235], a[24:27], v[40:43]\n"
    "v_perm_b32 v224, v198, v194, s75\n"
    "v_perm_b32 v225, v206, v202, s75\n"
    "v_mfma_f32_16x16x32_bf16 v[72:75], v[232:235], a[28:31], v[72:75]\n"
    "v_perm_b32 v226, v214, v210, s75\n"
    "v_perm_b32 v227, v222, v218, s75\n"
    "v_mfma_f32_16x16x32_bf16 v[104:107], v[232:235], a[32:35], v[104:107]\n"
    "v_perm_b32 v228, v198, v194, s76\n"
    "v_perm_b32 v229, v206, v202, s76\n"
    "v_mfma_f32_16x16x32_bf16 a[184:187], v[232:235], a[36:39], a[184:187]\n"
    "buffer_load_dwordx4 v191, s[44:47], s71 offen offset:3072 nt lds\n"
    "v_mfma_f32_16x16x32_bf16 v[12:15], v[236:239], a[20:23], v[12:15]\n"
    "v_perm_b32 v230, v214, v210, s76\n"
    "v_perm_b32 v231, v222, v218, s76\n"
    "v_mfma_f32_16x16x32_bf16 v[44:47], v[236:239], a[24:27], v[44:47]\n"
    "v_mfma_f32_16x16x32_bf16 v[76:79], v[236:239], a[28:31], v[76:79]\n"
    "s_add_u32 m0, s74, 4096\n"
    "s_add_u32 s71, s71, 4096\n"
    "s_and_b32 s71, s71, 0x1ffff\n"
    "v_mfma_f32_16x16x32_bf16 v[108:111], v[236:239], a[32:35], v[108:111]\n"
    "buffer_load_dwordx4 v188, s[44:47], s71 offen offset:0 nt lds\n"
    "v_mfma_f32_16x16x32_bf16 a[188:191], v[236:239], a[36:39], a[188:191]\n"
    "v_mfma_f32_16x16x32_bf16 v[16:19], v[224:227], a[20:23], v[16:19]\n"
    "v_perm_b32 v232, v199, v195, s75\n"
    "v_perm_b32 v233, v207, v203, s75\n"
    "v_mfma_f32_16x16x32_bf16 v[48:51], v[224:227], a[24:27], v[48:51]\n"
    "v_perm_b32 v234, v215, v211, s75\n"
    "v_perm_b32 v235, v223, v219, s75\n"
    "v_mfma_f32_16x16x32_bf16 v[80:83], v[224:227], a[28:31], v[80:83]\n"
    "buffer_load_dwordx4 v189, s[44:47], s71 offen offset:1024 nt lds\n"
    "v_mfma_f32_16x16x32_bf16 v[112:115], v[224:227], a[32:35], v[112:115]\n"
    "v_perm_b32 v236, v199, v195, s76\n"
    "v_perm_b32 v237, v207, v203, s76\n"
    "v_mfma_f32_16x16x32_bf16 a[192:195], v[224:227], a[36:39], a[192:195]\n"
    "v_perm_b32 v238, v215, v211, s76\n"
    "v_perm_b32 v239, v223, v219, s76\n"
    "v_mfma_f32_16x16x32_bf16 v[20:23], v[228:231], a[20:23], v[20:23]\n"
    "v_mfma_f32_16x16x32_bf16 v[52:55], v[228:231], a[24:27], v[52:55]\n"
    "buffer_load_dwordx4 v190, s[44:47], s71 offen offset:2048 nt lds\n"
    "v_mfma_f32_16x16x32_bf16 v[84:87], v[228:231], a[28:31], v[84:87]\n"
    "v_mfma_f32_16x16x32_bf16 v[116:119], v[228:231], a[32:35], v[116:119]\n"
    "v_mfma_f32_16x16x32_bf16 a[196:199], v[228:231], a[36:39], a[196:199]\n"
    "s_waitcnt vmcnt(7)\n"
    "v_mfma_f32_16x16x32_bf16 v[24:27], v[232:235], a[20:23], v[24:27]\n"
    "v_perm_b32 v224, v132, v128, s75\n"
    "v_perm_b32 v225, v140, v136, s75\n"
    "v_mfma_f32_16x16x32_bf16 v[56:59], v[232:235], a[24:27], v[56:59]\n"
    "v_perm_b32 v226, v148, v144, s75\n"
    "v_perm_b32 v227, v156, v152, s75\n"
    "v_mfma_f32_16x16x32_bf16 v[88:91], v[232:235], a[28:31], v[88:91]\n"
    "v_perm_b32 v228, v132, v128, s76\n"
    "v_perm_b32 v229, v140, v136, s76\n"
    "v_mfma_f32_16x16x32_bf16 v[120:123], v[232:235], a[32:35], v[120:123]\n"
    "buffer_load_dwordx4 v191, s[44:47], s71 offen offset:3072 nt lds\n"
    "v_mfma_f32_16x16x32_bf16 a[200:203], v[232:235], a[36:39], a[200:203]\n"
    "v_perm_b32 v230, v148, v144, s76\n"
    "v_perm_b32 v231, v156, v152, s76\n"
    "v_mfma_f32_16x16x32_bf16 v[28:31], v[236:239], a[20:23], v[28:31]\n"
    "v_mfma_f32_16x16x32_bf16 v[60:63], v[236:239], a[24:27], v[60:63]\n"
    "v_mfma_f32_16x16x32_bf16 v[92:95], v[236:239], a[28:31], v[92:95]\n"
    "v_mfma_f32_16x16x32_bf16 v[124:127], v[236:239], a[32:35], v[124:127]\n"
    "v_mfma_f32_16x16x32_bf16 a[204:207], v[236:239], a[36:39], a[204:207]\n"
    "v_mfma_f32_16x16x32_bf16 v[0:3], v[224:227], a[40:43], v[0:3]\n"
    "v_perm_b32 v232, v133, v129, s75\n"
    "v_perm_b32 v233, v141, v137, s75\n"
    "v_mfma_f32_16x16x32_bf16 v[32:35], v[224:227], a[44:47], v[32:35]\n"
    "v_perm_b32 v234, v149, v145, s75\n"
    "v_perm_b32 v235, v157, v153, s75\n"
    "s_add_u32 m0, s87, 0\n"
    "s_add_u32 s85, s85, 4096\n"
    "s_and_b32 s85, s85, 0x1ffff\n"
    "v_mfma_f32_16x16x32_bf16 v[64:67], v[224:227], a[48:51], v[64:67]\n"
    "buffer_load_dwordx4 v242, s[48:51], s85 offen offset:0 nt lds\n"
    "v_mfma_f32_16x16x32_bf16 v[96:99], v[224:227], a[52:55], v[96:99]\n"
    "v_perm_b32 v236, v133, v129, s76\n"
    "v_perm_b32 v237, v141, v137, s76\n"
    "v_mfma_f32_16x16x32_bf16 a[176:179], v[224:227], a[56:59], a[176:179]\n"
    "v_perm_b32 v238, v149, v145, s76\n"
    "v_perm_b32 v239, v157, v153, s76\n"
    "v_mfma_f32_16x16x32_bf16 v[4:7], v[228:231], a[40:43], v[4:7]\n"
    "v_mfma_f32_16x16x32_bf16 v[36:39], v[228:231], a[44:47], v[36:39]\n"
    "v_mfma_f32_16x16x32_bf16 v[68:71], v[228:231], a[48:51], v[68:71]\n"
    "buffer_load_dwordx4 v243, s[48:51], s85 offen offset:1024 nt lds\n"
    "v_mfma_f32_16x16x32_bf16 v[100:103], v[228:231], a[52:55], v[100:103]\n"
    "v_mfma_f32_16x16x32_bf16 a[180:183], v[228:231], a[56:59], a[180:183]\n"
    "v_mfma_f32_16x16x32_bf16 v[8:11], v[232:235], a[40:43], v[8:11]\n"
    "v_perm_b32 v224, v134, v130, s75\n"
    "v_perm_b32 v225, v142, v138, s75\n"
    "v_mfma_f32_16x16x32_bf16 v[40:43], v[232:235], a[44:47], v[40:43]\n"
    "v_perm_b32 v226, v150, v146, s75\n"
    "v_perm_b32 v227, v158, v154, s75\n"
    "v_mfma_f32_16x16x32_bf16 v[72:75], v[232:235], a[48:51], v[72:75]\n"
    "buffer_load_dwordx4 v242, s[48:51], s85 offen offset:2048 nt lds\n"
    "v_mfma_f32_16x16x32_bf16 v[104:107], v[232:235], a[52:55], v[104:107]\n"
    "v_perm_b32 v228, v134, v130, s76\n"
    "v_perm_b32 v229, v142, v138, s76\n"
    "v_mfma_f32_16x16x32_bf16 a[184:187], v[232:235], a[56:59], a[184:187]\n"
    "v_perm_b32 v230, v150, v146, s76\n"
    "v_perm_b32 v231, v158, v154, s76\n"
    "v_mfma_f32_16x16x32_bf16 v[12:15], v[236:239], a[40:43], v[12:15]\n"
    "v_mfma_f32_16x16x32_bf16 v[44:47], v[236:239], a[44:47], v[44:47]\n"
    "v_mfma_f32_16x16x32_bf16 v[76:79], v[236:239], a[48:51], v[76:79]\n"
    "buffer_load_dwordx4 v243, s[48:51], s85 offen offset:3072 nt lds\n"
    "v_mfma_f32_16x16x32_bf16 v[108:111], v[236:239], a[52:55], v[108:111]\n"
    "v_mfma_f32_16x16x32_bf16 a[188:191], v[236:239], a[56:59], a[188:191]\n"
    "v_mfma_f32_16x16x32_bf16 v[16:19], v[224:227], a[40:43], v[16:19]\n"
    "v_perm_b32 v232, v135, v131, s75\n"
    "v_perm_b32 v233, v143, v139, s75\n"
    "v_mfma_f32_16x16x32_bf16 v[48:51], v[224:227], a[44:47], v[48:51]\n"
    "v_perm_b32 v234, v151, v147, s75\n"
    "v_perm_b32 v235, v159, v155, s75\n"
    "s_add_u32 m0, s87, 4096\n"
    "s_add_u32 s85, s85, 4096\n"
    "s_and_b32 s85, s85, 0x1ffff\n"
    "v_mfma_f32_16x16x32_bf16 v[80:83], v[224:227], a[48:51], v[80:83]\n"
    "buffer_load_dwordx4 v242, s[48:51], s85 offen offset:0 nt lds\n"
    "v_mfma_f32_16x16x32_bf16 v[112:115], v[224:227], a[52:55], v[112:115]\n"
    "v_perm_b32 v236, v135, v131, s76\n"
    "v_perm_b32 v237, v143, v139, s76\n"
    "v_mfma_f32_16x16x32_bf16 a[192:195], v[224:227], a[56:59], a[192:195]\n"
    "v_perm_b32 v238, v151, v147, s76\n"
    "v_perm_b32 v239, v159, v155, s76\n"
    "v_mfma_f32_16x16x32_bf16 v[20:23], v[228:231], a[40:43], v[20:23]\n"
    "v_mfma_f32_16x16x32_bf16 v[52:55], v[228:231], a[44:47], v[52:55]\n"
    "v_mfma_f32_16x16x32_bf16 v[84:87], v[228:231], a[48:51], v[84:87]\n"
    "buffer_load_dwordx4 v243, s[48:51], s85 offen offset:1024 nt lds\n"
    "v_mfma_f32_16x16x32_bf16 v[116:119], v[228:231], a[52:55], v[116:119]\n"
    "v_mfma_f32_16x16x32_bf16 a[196:199], v[228:231], a[56:59], a[196:199]\n"
    "v_mfma_f32_16x16x32_bf16 v[24:27], v[232:235], a[40:43], v[24:27]\n"
    "v_mfma_f32_16x16x32_bf16 v[56:59], v[232:235], a[44:47], v[56:59]\n"
    "v_mfma_f32_16x16x32_bf16 v[88:91], v[232:235], a[48:51], v[88:91]\n"
    "buffer_load_dwordx4 v242, s[48:51], s85 offen offset:2048 nt lds\n"
    "v_mfma_f32_16x16x32_bf16 v[120:123], v[232:235], a[52:55], v[120:123]\n"
    "v_mfma_f32_16x16x32_bf16 a[200:203], v[232:235], a[56:59], a[200:203]\n"
    "v_mfma_f32_16x16x32_bf16 v[28:31], v[236:239], a[40:43], v[28:31]\n"
    "v_mfma_f32_16x16x32_bf16 v[60:63], v[236:239], a[44:47], v[60:63]\n"
    "v_mfma_f32_16x16x32_bf16 v[92:95], v[236:239], a[48:51], v[92:95]\n"
    "buffer_load_dwordx4 v243, s[48:51], s85 offen offset:3072 nt lds\n"
    "v_mfma_f32_16x16x32_bf16 v[124:127], v[236:239], a[52:55], v[124:127]\n"
    "v_mfma_f32_16x16x32_bf16 a[204:207], v[236:239], a[56:59], a[204:207]\n"
    "s_add_u32 m0, s87, 32768\n"
    "s_add_u32 s85, s85, 4096\n"
    "s_and_b32 s85, s85, 0x1ffff\n"
    "buffer_load_dwordx4 v242, s[48:51], s85 offen offset:0 nt lds\n"
    "v_pk_mul_f32 v[0:1], v[0:1], s[78:79]\n"
    "v_pk_mul_f32 v[2:3], v[2:3], s[78:79]\n"
    "v_pk_mul_f32 v[4:5], v[4:5], s[78:79]\n"
    "v_pk_mul_f32 v[6:7], v[6:7], s[78:79]\n"
    "v_pk_mul_f32 v[8:9], v[8:9], s[78:79]\n"
    "v_pk_mul_f32 v[10:11], v[10:11], s[78:79]\n"
    "v_pk_mul_f32 v[12:13], v[12:13], s[78:79]\n"
    "v_pk_mul_f32 v[14:15], v[14:15], s[78:79]\n"
    "v_pk_mul_f32 v[16:17], v[16:17], s[78:79]\n"
    "v_pk_mul_f32 v[18:19], v[18:19], s[78:79]\n"
    "v_pk_mul_f32 v[20:21], v[20:21], s[78:79]\n"
    "v_pk_mul_f32 v[22:23], v[22:23], s[78:79]\n"
    "v_pk_mul_f32 v[24:25], v[24:25], s[78:79]\n"
    "v_pk_mul_f32 v[26:27], v[26:27], s[78:79]\n"
    "v_pk_mul_f32 v[28:29], v[28:29], s[78:79]\n"
    "v_pk_mul_f32 v[30:31], v[30:31], s[78:79]\n"
    "v_cvt_pk_bf16_f32 v160, v0, v4\n"
    "v_cvt_pk_bf16_f32 v161, v8, v12\n"
    "v_cvt_pk_bf16_f32 v162, v16, v20\n"
    "v_cvt_pk_bf16_f32 v163, v24, v28\n"
    "v_accvgpr_write_b32 a0, v160\n"
    "v_accvgpr_write_b32 a1, v161\n"
    "v_accvgpr_write_b32 a2, v162\n"
    "v_accvgpr_write_b32 a3, v163\n"
    "v_cvt_pk_bf16_f32 v164, v1, v5\n"
    "v_cvt_pk_bf16_f32 v165, v9, v13\n"
    "v_cvt_pk_bf16_f32 v166, v17, v21\n"
    "v_cvt_pk_bf16_f32 v167, v25, v29\n"
    "v_accvgpr_write_b32 a4, v164\n"
    "v_accvgpr_write_b32 a5, v165\n"
    "buffer_load_dwordx4 v243, s[48:51], s85 offen offset:1024 nt lds\n"
    "v_accvgpr_write_b32 a6, v166\n"
    "v_accvgpr_write_b32 a7, v167\n"
    "v_cvt_pk_bf16_f32 v160, v2, v6\n"
    "v_cvt_pk_bf16_f32 v161, v10, v14\n"
    "v_cvt_pk_bf16_f32 v162, v18, v22\n"
    "v_cvt_pk_bf16_f32 v163, v26, v30\n"
    "v_accvgpr_write_b32 a8, v160\n"
    "v_accvgpr_write_b32 a9, v161\n"
    "v_accvgpr_write_b32 a10, v162\n"
    "v_accvgpr_write_b32 a11, v163\n"
    "v_cvt_pk_bf16_f32 v164, v3, v7\n"
    "v_cvt_pk_bf16_f32 v165, v11, v15\n"
    "v_cvt_pk_bf16_f32 v166, v19, v23\n"
    "v_cvt_pk_bf16_f32 v167, v27, v31\n"
    "v_accvgpr_write_b32 a12, v164\n"
    "v_accvgpr_write_b32 a13, v165\n"
    "v_accvgpr_write_b32 a14, v166\n"
    "v_accvgpr_write_b32 a15, v167\n"
    "v_pk_mul_f32 v[32:33], v[32:33], s[78:79]\n"
    "v_pk_mul_f32 v[34:35], v[34:35], s[78:79]\n"
    "v_pk_mul_f32 v[36:37], v[36:37], s[78:79]\n"
    "v_pk_mul_f32 v[38:39], v[38:39], s[78:79]\n"
    "v_pk_mul_f32 v[40:41], v[40:41], s[78:79]\n"
    "v_pk_mul_f32 v[42:43], v[42:43], s[78:79]\n"
    "v_pk_mul_f32 v[44:45], v[44:45], s[78:79]\n"
    "v_pk_mul_f32 v[46:47], v[46:47], s[78:79]\n"
    "v_pk_mul_f32 v[48:49], v[48:49], s[78:79]\n"
    "v_pk_mul_f32 v[50:51], v[50:51], s[78:79]\n"
    "v_pk_mul_f32 v[52:53], v[52:53], s[78:79]\n"
    "v_pk_mul_f32 v[54:55], v[54:55], s[78:79]\n"
    "buffer_load_dwordx4 v242, s[48:51], s85 offen offset:2048 nt lds\n"
    "v_pk_mul_f32 v[56:57], v[56:57], s[78:79]\n"
    "v_pk_mul_f32 v[58:59], v[58:59], s[78:79]\n"
    "v_pk_mul_f32 v[60:61], v[60:61], s[78:79]\n"
    "v_pk_mul_f32 v[62:63], v[62:63], s[78:79]\n"
    "v_cvt_pk_bf16_f32 v160, v32, v36\n"
    "v_cvt_pk_bf16_f32 v161, v40, v44\n"
    "v_cvt_pk_bf16_f32 v162, v48, v52\n"
    "v_cvt_pk_bf16_f32 v163, v56, v60\n"
    "v_accvgpr_write_b32 a16, v160\n"
    "v_accvgpr_write_b32 a17, v161\n"
    "v_accvgpr_write_b32 a18, v162\n"
    "v_accvgpr_write_b32 a19, v163\n"
    "v_cvt_pk_bf16_f32 v164, v33, v37\n"
    "v_cvt_pk_bf16_f32 v165, v41, v45\n"
    "v_cvt_pk_bf16_f32 v166, v49, v53\n"
    "v_cvt_pk_bf16_f32 v167, v57, v61\n"
    "v_accvgpr_write_b32 a20, v164\n"
    "v_accvgpr_write_b32 a21, v165\n"
    "v_accvgpr_write_b32 a22, v166\n"
    "v_accvgpr_write_b32 a23, v167\n"
    "v_cvt_pk_bf16_f32 v160, v34, v38\n"
    "v_cvt_pk_bf16_f32 v161, v42, v46\n"
    "v_cvt_pk_bf16_f32 v162, v50, v54\n"
    "v_cvt_pk_bf16_f32 v163, v58, v62\n"
    "v_accvgpr_write_b32 a24, v160\n"
    "v_accvgpr_write_b32 a25, v161\n"
    "v_accvgpr_write_b32 a26, v162\n"
    "v_accvgpr_write_b32 a27, v163\n"
    "v_cvt_pk_bf16_f32 v164, v35, v39\n"
    "v_cvt_pk_bf16_f32 v165, v43, v47\n"
    "buffer_load_dwordx4 v243, s[48:51], s85 offen offset:3072 nt lds\n"
    "v_cvt_pk_bf16_f32 v166, v51, v55\n"
    "v_cvt_pk_bf16_f32 v167, v59, v63\n"
    "v_accvgpr_write_b32 a28, v164\n"
    "v_accvgpr_write_b32 a29, v165\n"
    "v_accvgpr_write_b32 a30, v166\n"
    "v_accvgpr_write_b32 a31, v167\n"
    "v_pk_mul_f32 v[64:65], v[64:65], s[78:79]\n"
    "v_pk_mul_f32 v[66:67], v[66:67], s[78:79]\n"
    "v_pk_mul_f32 v[68:69], v[68:69], s[78:79]\n"
    "v_pk_mul_f32 v[70:71], v[70:71], s[78:79]\n"
    "v_pk_mul_f32 v[72:73], v[72:73], s[78:79]\n"
    "v_pk_mul_f32 v[74:75], v[74:75], s[78:79]\n"
    "v_pk_mul_f32 v[76:77], v[76:77], s[78:79]\n"
    "v_pk_mul_f32 v[78:79], v[78:79], s[78:79]\n"
    "v_pk_mul_f32 v[80:81], v[80:81], s[78:79]\n"
    "v_pk_mul_f32 v[82:83], v[82:83], s[78:79]\n"
    "v_pk_mul_f32 v[84:85], v[84:85], s[78:79]\n"
    "v_pk_mul_f32 v[86:87], v[86:87], s[78:79]\n"
    "v_pk_mul_f32 v[88:89], v[88:89], s[78:79]\n"
    "v_pk_mul_f32 v[90:91], v[90:91], s[78:79]\n"
    "v_pk_mul_f32 v[92:93], v[92:93], s[78:79]\n"
    "v_pk_mul_f32 v[94:95], v[94:95], s[78:79]\n"
    "v_cvt_pk_bf16_f32 v160, v64, v68\n"
    "v_cvt_pk_bf16_f32 v161, v72, v76\n"
    "v_cvt_pk_bf16_f32 v162, v80, v84\n"
    "v_cvt_pk_bf16_f32 v163, v88, v92\n"
    "v_accvgpr_write_b32 a32, v160\n"
    "v_accvgpr_write_b32 a33, v161\n"
    "v_accvgpr_write_b32 a34, v162\n"
    "v_accvgpr_write_b32 a35, v163\n"
    "s_add_u32 m0, s87, 36864\n"
    "s_add_u32 s85, s85, 4096\n"
    "s_and_b32 s85, s85, 0x1ffff\n"
    "buffer_load_dwordx4 v242, s[48:51], s85 offen offset:0 nt lds\n"
    "v_cvt_pk_bf16_f32 v164, v65, v69\n"
    "v_cvt_pk_bf16_f32 v165, v73, v77\n"
    "v_cvt_pk_bf16_f32 v166, v81, v85\n"
    "v_cvt_pk_bf16_f32 v167, v89, v93\n"
    "v_accvgpr_write_b32 a36, v164\n"
    "v_accvgpr_write_b32 a37, v165\n"
    "v_accvgpr_write_b32 a38, v166\n"
    "v_accvgpr_write_b32 a39, v167\n"
    "v_cvt_pk_bf16_f32 v160, v66, v70\n"
    "v_cvt_pk_bf16_f32 v161, v74, v78\n"
    "v_cvt_pk_bf16_f32 v162, v82, v86\n"
    "v_cvt_pk_bf16_f32 v163, v90, v94\n"
    "v_accvgpr_write_b32 a40, v160\n"
    "v_accvgpr_write_b32 a41, v161\n"
    "v_accvgpr_write_b32 a42, v162\n"
    "v_accvgpr_write_b32 a43, v163\n"
    "v_cvt_pk_bf16_f32 v164, v67, v71\n"
    "v_cvt_pk_bf16_f32 v165, v75, v79\n"
    "v_cvt_pk_bf16_f32 v166, v83, v87\n"
    "v_cvt_pk_bf16_f32 v167, v91, v95\n"
    "v_accvgpr_write_b32 a44, v164\n"
    "v_accvgpr_write_b32 a45, v165\n"
    "v_accvgpr_write_b32 a46, v166\n"
    "v_accvgpr_write_b32 a47, v167\n"
    "v_pk_mul_f32 v[96:97], v[96:97], s[78:79]\n"
    "v_pk_mul_f32 v[98:99], v[98:99], s[78:79]\n"
    "v_pk_mul_f32 v[100:101], v[100:101], s[78:79]\n"
    "v_pk_mul_f32 v[102:103], v[102:103], s[78:79]\n"
    "v_pk_mul_f32 v[104:105], v[104:105], s[78:79]\n"
    "v_pk_mul_f32 v[106:107], v[106:107], s[78:79]\n"
    "buffer_load_dwordx4 v243, s[48:51], s85 offen offset:1024 nt lds\n"
    "v_pk_mul_f32 v[108:109], v[108:109], s[78:79]\n"
    "v_pk_mul_f32 v[110:111], v[110:111], s[78:79]\n"
    "v_pk_mul_f32 v[112:113], v[112:113], s[78:79]\n"
    "v_pk_mul_f32 v[114:115], v[114:115], s[78:79]\n"
    "v_pk_mul_f32 v[116:117], v[116:117], s[78:79]\n"
    "v_pk_mul_f32 v[118:119], v[118:119], s[78:79]\n"
    "v_pk_mul_f32 v[120:121], v[120:121], s[78:79]\n"
    "v_pk_mul_f32 v[122:123], v[122:123], s[78:79]\n"
    "v_pk_mul_f32 v[124:125], v[124:125], s[78:79]\n"
    "v_pk_mul_f32 v[126:127], v[126:127], s[78:79]\n"
    "v_cvt_pk_bf16_f32 v160, v96, v100\n"
    "v_cvt_pk_bf16_f32 v161, v104, v108\n"
    "v_cvt_pk_bf16_f32 v162, v112, v116\n"
    "v_cvt_pk_bf16_f32 v163, v120, v124\n"
    "v_accvgpr_write_b32 a48, v160\n"
    "v_accvgpr_write_b32 a49, v161\n"
    "v_accvgpr_write_b32 a50, v162\n"
    "v_accvgpr_write_b32 a51, v163\n"
    "v_cvt_pk_bf16_f32 v164, v97, v101\n"
    "v_cvt_pk_bf16_f32 v165, v105, v109\n"
    "v_cvt_pk_bf16_f32 v166, v113, v117\n"
    "v_cvt_pk_bf16_f32 v167, v121, v125\n"
    "v_accvgpr_write_b32 a52, v164\n"
    "v_accvgpr_write_b32 a53, v165\n"
    "v_accvgpr_write_b32 a54, v166\n"
    "v_accvgpr_write_b32 a55, v167\n"
    "v_cvt_pk_bf16_f32 v160, v98, v102\n"
    "v_cvt_pk_bf16_f32 v161, v106, v110\n"
    "v_cvt_pk_bf16_f32 v162, v114, v118\n"
    "v_cvt_pk_bf16_f32 v163, v122, v126\n"
    "buffer_load_dwordx4 v242, s[48:51], s85 offen offset:2048 nt lds\n"
    "v_accvgpr_write_b32 a56, v160\n"
    "v_accvgpr_write_b32 a57, v161\n"
    "v_accvgpr_write_b32 a58, v162\n"
    "v_accvgpr_write_b32 a59, v163\n"
    "v_cvt_pk_bf16_f32 v164, v99, v103\n"
    "v_cvt_pk_bf16_f32 v165, v107, v111\n"
    "v_cvt_pk_bf16_f32 v166, v115, v119\n"
    "v_cvt_pk_bf16_f32 v167, v123, v127\n"
    "v_accvgpr_write_b32 a60, v164\n"
    "v_accvgpr_write_b32 a61, v165\n"
    "v_accvgpr_write_b32 a62, v166\n"
    "v_accvgpr_write_b32 a63, v167\n"
    "v_accvgpr_read_b32 v128, a176\n"
    "v_accvgpr_read_b32 v129, a177\n"
    "v_accvgpr_read_b32 v130, a178\n"
    "v_accvgpr_read_b32 v131, a179\n"
    "v_accvgpr_read_b32 v132, a180\n"
    "v_accvgpr_read_b32 v133, a181\n"
    "v_accvgpr_read_b32 v134, a182\n"
    "v_accvgpr_read_b32 v135, a183\n"
    "v_accvgpr_read_b32 v136, a184\n"
    "v_accvgpr_read_b32 v137, a185\n"
    "v_accvgpr_read_b32 v138, a186\n"
    "v_accvgpr_read_b32 v139, a187\n"
    "v_accvgpr_read_b32 v140, a188\n"
    "v_accvgpr_read_b32 v141, a189\n"
    "v_accvgpr_read_b32 v142, a190\n"
    "v_accvgpr_read_b32 v143, a191\n"
    "v_accvgpr_read_b32 v144, a192\n"
    "v_accvgpr_read_b32 v145, a193\n"
    "buffer_load_dwordx4 v243, s[48:51], s85 offen offset:3072 nt lds\n"
    "v_accvgpr_read_b32 v146, a194\n"
    "v_accvgpr_read_b32 v147, a195\n"
    "v_accvgpr_read_b32 v148, a196\n"
    "v_accvgpr_read_b32 v149, a197\n"
    "v_accvgpr_read_b32 v150, a198\n"
    "v_accvgpr_read_b32 v151, a199\n"
    "v_accvgpr_read_b32 v152, a200\n"
    "v_accvgpr_read_b32 v153, a201\n"
    "v_accvgpr_read_b32 v154, a202\n"
    "v_accvgpr_read_b32 v155, a203\n"
    "v_accvgpr_read_b32 v156, a204\n"
    "v_accvgpr_read_b32 v157, a205\n"
    "v_accvgpr_read_b32 v158, a206\n"
    "v_accvgpr_read_b32 v159, a207\n"
    "v_pk_mul_f32 v[128:129], v[128:129], s[78:79]\n"
    "v_pk_mul_f32 v[130:131], v[130:131], s[78:79]\n"
    "v_pk_mul_f32 v[132:133], v[132:133], s[78:79]\n"
    "v_pk_mul_f32 v[134:135], v[134:135], s[78:79]\n"
    "v_pk_mul_f32 v[136:137], v[136:137], s[78:79]\n"
    "v_pk_mul_f32 v[138:139], v[138:139], s[78:79]\n"
    "v_pk_mul_f32 v[140:141], v[140:141], s[78:79]\n"
    "v_pk_mul_f32 v[142:143], v[142:143], s[78:79]\n"
    "v_pk_mul_f32 v[144:145], v[144:145], s[78:79]\n"
    "v_pk_mul_f32 v[146:147], v[146:147], s[78:79]\n"
    "v_pk_mul_f32 v[148:149], v[148:149], s[78:79]\n"
    "v_pk_mul_f32 v[150:151], v[150:151], s[78:79]\n"
    "v_pk_mul_f32 v[152:153], v[152:153], s[78:79]\n"
    "v_pk_mul_f32 v[154:155], v[154:155], s[78:79]\n"
    "v_pk_mul_f32 v[156:157], v[156:157], s[78:79]\n"
    "v_pk_mul_f32 v[158:159], v[158:159], s[78:79]\n"
    "v_cvt_pk_bf16_f32 v160, v128, v132\n"
    "v_cvt_pk_bf16_f32 v161, v136, v140\n"
    "v_cvt_pk_bf16_f32 v162, v144, v148\n"
    "v_cvt_pk_bf16_f32 v163, v152, v156\n"
    "v_accvgpr_write_b32 a64, v160\n"
    "v_accvgpr_write_b32 a65, v161\n"
    "v_accvgpr_write_b32 a66, v162\n"
    "v_accvgpr_write_b32 a67, v163\n"
    "v_cvt_pk_bf16_f32 v164, v129, v133\n"
    "v_cvt_pk_bf16_f32 v165, v137, v141\n"
    "v_cvt_pk_bf16_f32 v166, v145, v149\n"
    "v_cvt_pk_bf16_f32 v167, v153, v157\n"
    "v_accvgpr_write_b32 a68, v164\n"
    "v_accvgpr_write_b32 a69, v165\n"
    "v_accvgpr_write_b32 a70, v166\n"
    "v_accvgpr_write_b32 a71, v167\n"
    "v_cvt_pk_bf16_f32 v160, v130, v134\n"
    "v_cvt_pk_bf16_f32 v161, v138, v142\n"
    "v_cvt_pk_bf16_f32 v162, v146, v150\n"
    "v_cvt_pk_bf16_f32 v163, v154, v158\n"
    "v_accvgpr_write_b32 a72, v160\n"
    "v_accvgpr_write_b32 a73, v161\n"
    "v_accvgpr_write_b32 a74, v162\n"
    "v_accvgpr_write_b32 a75, v163\n"
    "v_cvt_pk_bf16_f32 v164, v131, v135\n"
    "v_cvt_pk_bf16_f32 v165, v139, v143\n"
    "v_cvt_pk_bf16_f32 v166, v147, v151\n"
    "v_cvt_pk_bf16_f32 v167, v155, v159\n"
    "v_accvgpr_write_b32 a76, v164\n"
    "v_accvgpr_write_b32 a77, v165\n"
    "v_accvgpr_write_b32 a78, v166\n"
    "v_accvgpr_write_b32 a79, v167\n"
    "v_lshrrev_b32 v237, 4, v240\n"
    "v_xor_b32 v237, v237, v240\n"
    "v_and_b32 v237, 3, v237\n"
    "v_lshlrev_b32 v237, 6, v237\n"
    "v_bfe_u32 v238, v240, 2, 2\n"
    "v_lshl_add_u32 v237, v238, 4, v237\n"
    "v_and_b32 v238, 15, v240\n"
    "v_lshl_add_u32 v237, v238, 8, v237\n"
    "v_add_u32 v220, s74, v237\n"
    "v_xor_b32 v221, 16, v220\n"
    "v_xor_b32 v222, 32, v220\n"
    "v_xor_b32 v223, 48, v220\n"
    "v_lshrrev_b32 v237, 2, v240\n"
    "v_and_b32 v238, 7, v237\n"
    "v_lshlrev_b32 v238, 5, v238\n"
    "v_lshl_add_u32 v238, v237, 8, v238\n"
    "v_and_b32 v237, 3, v240\n"
    "v_lshl_add_u32 v238, v237, 3, v238\n"
    "v_add_u32 v212, s87, v238\n"
    "v_xor_b32 v213, 32, v212\n"
    "v_xor_b32 v214, 64, v212\n"
    "v_xor_b32 v215, 96, v212\n"
    "v_xor_b32 v216, 128, v212\n"
    "v_xor_b32 v217, 160, v212\n"
    "v_xor_b32 v218, 192, v212\n"
    "v_xor_b32 v219, 224, v212\n"
    "s_waitcnt vmcnt(20)\n"
    "ds_read_b128 a[112:115], v220 offset:0\n"
    "s_waitcnt vmcnt(16)\n"
    "ds_read_b128 a[128:131], v220 offset:4096\n"
    "ds_read_b128 a[116:119], v221 offset:0\n"
    "ds_read_b128 a[132:135], v221 offset:4096\n"
    "ds_read_b128 a[120:123], v222 offset:0\n"
    "ds_read_b128 a[136:139], v222 offset:4096\n"
    "ds_read_b128 a[124:127], v223 offset:0\n"
    "ds_read_b128 a[140:143], v223 offset:4096\n"
    "s_waitcnt lgkmcnt(0)\n"
    "s_add_u32 m0, s74, 0\n"
    "s_add_u32 s71, s71, 4096\n"
    "s_and_b32 s71, s71, 0x1ffff\n"
    "v_mfma_f32_16x16x32_bf16 v[160:163], a[112:115], a[0:3], 0\n"
    "buffer_load_dwordx4 v188, s[44:47], s71 offen offset:0 nt lds\n"
    "v_mfma_f32_16x16x32_bf16 v[164:167], a[128:131], a[0:3], 0\n"
    "buffer_load_dwordx4 v189, s[44:47], s71 offen offset:1024 nt lds\n"
    "v_mfma_f32_16x16x32_bf16 v[160:163], a[116:119], a[4:7], v[160:163]\n"
    "buffer_load_dwordx4 v190, s[44:47], s71 offen offset:2048 nt lds\n"
    "v_mfma_f32_16x16x32_bf16 v[164:167], a[132:135], a[4:7], v[164:167]\n"
    "buffer_load_dwordx4 v191, s[44:47], s71 offen offset:3072 nt lds\n"
    "s_add_u32 m0, s74, 4096\n"
    "s_add_u32 s71, s71, 4096\n"
    "s_and_b32 s71, s71, 0x1ffff\n"
    "v_mfma_f32_16x16x32_bf16 v[160:163], a[120:123], a[8:11], v[160:163]\n"
    "buffer_load_dwordx4 v188, s[44:47], s71 offen offset:0 nt lds\n"
    "v_mfma_f32_16x16x32_bf16 v[164:167], a[136:139], a[8:11], v[164:167]\n"
    "buffer_load_dwordx4 v189, s[44:47], s71 offen offset:1024 nt lds\n"
    "v_mfma_f32_16x16x32_bf16 v[160:163], a[124:127], a[12:15], v[160:163]\n"
    "buffer_load_dwordx4 v190, s[44:47], s71 offen offset:2048 nt lds\n"
    "v_mfma_f32_16x16x32_bf16 v[164:167], a[140:143], a[12:15], v[164:167]\n"
    "buffer_load_dwordx4 v191, s[44:47], s71 offen offset:3072 nt lds\n"
    "v_mfma_f32_16x16x32_bf16 v[168:171], a[112:115], a[16:19], 0\n"
    "s_waitcnt vmcnt(20)\n"
    "ds_read_b64_tr_b16 a[80:81], v212 offset:0\n"
    "s_nop 1\n"
    "v_max3_f32 v237, v160, v161, v162\n"
    "s_nop 0\n"
    "v_max3_f32 v238, v163, v164, v165\n"
    "v_max3_f32 v237, v237, v166, v167\n"
    "v_max_f32 v237, v237, v238\n"
    "v_mov_b32 v238, v237\n"
    "s_nop 1\n"
    "v_permlane16_swap_b32 v237, v238\n"
    "v_mfma_f32_16x16x32_bf16 v[172:175], a[128:131], a[16:19], 0\n"
    "s_waitcnt vmcnt(16)\n"
    "ds_read_b64_tr_b16 a[82:83], v212 offset:4096\n"
    "v_max_f32 v237, v237, v238\n"
    "v_mov_b32 v238, v237\n"
    "s_nop 1\n"
    "v_permlane32_swap_b32 v237, v238\n"
    "v_max_f32 v237, v237, v238\n"
    "v_sub_f32 v160, v160, v237\n"
    "v_sub_f32 v161, v161, v237\n"
    "v_mfma_f32_16x16x32_bf16 v[168:171], a[116:119], a[20:23], v[168:171]\n"
    "ds_read_b64_tr_b16 a[84:85], v213 offset:0\n"
    "v_sub_f32 v162, v162, v237\n"
    "v_sub_f32 v163, v163, v237\n"
    "v_sub_f32 v164, v164, v237\n"
    "v_sub_f32 v165, v165, v237\n"
    "v_sub_f32 v166, v166, v237\n"
    "v_sub_f32 v167, v167, v237\n"
    "v_mfma_f32_16x16x32_bf16 v[172:175], a[132:135], a[20:23], v[172:175]\n"
    "ds_read_b64_tr_b16 a[86:87], v213 offset:4096\n"
    "v_exp_f32 v176, v160\n"
    "v_exp_f32 v177, v161\n"
    "v_exp_f32 v178, v162\n"
    "v_exp_f32 v179, v163\n"
    "v_exp_f32 v180, v164\n"
    "v_exp_f32 v181, v165\n"
    "v_mfma_f32_16x16x32_bf16 v[168:171], a[120:123], a[24:27], v[168:171]\n"
    "ds_read_b64_tr_b16 a[88:89], v214 offset:0\n"
    "v_exp_f32 v182, v166\n"
    "v_exp_f32 v183, v167\n"
    "v_sub_f32 v192, 0, v237\n"
    "v_sub_f32 v193, 0, v237\n"
    "v_sub_f32 v194, 0, v237\n"
    "v_sub_f32 v195, 0, v237\n"
    "v_mfma_f32_16x16x32_bf16 v[172:175], a[136:139], a[24:27], v[172:175]\n"
    "ds_read_b64_tr_b16 a[90:91], v214 offset:4096\n"
    "v_add_f32 v184, v176, v177\n"
    "v_add_f32 v185, v178, v179\n"
    "v_add_f32 v186, v180, v181\n"
    "v_add_f32 v187, v182, v183\n"
    "v_add_f32 v184, v184, v185\n"
    "v_add_f32 v186, v186, v187\n"
    "v_mfma_f32_16x16x32_bf16 v[168:171], a[124:127], a[28:31], v[168:171]\n"
    "ds_read_b64_tr_b16 a[92:93], v215 offset:0\n"
    "v_add_f32 v184, v184, v186\n"
    "v_cvt_pk_bf16_f32 v224, v176, v177\n"
    "v_cvt_pk_bf16_f32 v225, v178, v179\n"
    "v_cvt_pk_bf16_f32 v226, v180, v181\n"
    "v_cvt_pk_bf16_f32 v227, v182, v183\n"
    "v_mov_b32 v232, v184\n"
    "v_mfma_f32_16x16x32_bf16 v[172:175], a[140:143], a[28:31], v[172:175]\n"
    "ds_read_b64_tr_b16 a[94:95], v215 offset:4096\n"
    "v_mfma_f32_16x16x32_bf16 v[160:163], a[112:115], a[32:35], 0\n"
    "ds_read_b64_tr_b16 a[96:97], v216 offset:0\n"
    "v_max3_f32 v237, v168, v169, v170\n"
    "s_nop 3\n"
    "v_max3_f32 v238, v171, v172, v173\n"
    "v_max3_f32 v237, v237, v174, v175\n"
    "v_mfma_f32_16x16x32_bf16 v[164:167], a[128:131], a[32:35], 0\n"
    "ds_read_b64_tr_b16 a[98:99], v216 offset:4096\n"
    "v_max_f32 v237, v237, v238\n"
    "v_mov_b32 v238, v237\n"
    "s_nop 1\n"
    "v_permlane16_swap_b32 v237, v238\n"
    "v_mfma_f32_16x16x32_bf16 v[160:163], a[116:119], a[36:39], v[160:163]\n"
    "ds_read_b64_tr_b16 a[100:101], v217 offset:0\n"
    "v_max_f32 v237, v237, v238\n"
    "v_mov_b32 v238, v237\n"
    "s_nop 1\n"
    "v_permlane32_swap_b32 v237, v238\n"
    "v_mfma_f32_16x16x32_bf16 v[164:167], a[132:135], a[36:39], v[164:167]\n"
    "ds_read_b64_tr_b16 a[102:103], v217 offset:4096\n"
    "v_max_f32 v237, v237, v238\n"
    "v_sub_f32 v168, v168, v237\n"
    "v_sub_f32 v169, v169, v237\n"
    "v_mfma_f32_16x16x32_bf16 v[160:163], a[120:123], a[40:43], v[160:163]\n"
    "ds_read_b64_tr_b16 a[104:105], v218 offset:0\n"
    "v_sub_f32 v170, v170, v237\n"
    "v_sub_f32 v171, v171, v237\n"
    "v_sub_f32 v172, v172, v237\n"
    "v_mfma_f32_16x16x32_bf16 v[164:167], a[136:139], a[40:43], v[164:167]\n"
    "ds_read_b64_tr_b16 a[106:107], v218 offset:4096\n"
    "v_sub_f32 v173, v173, v237\n"
    "v_sub_f32 v174, v174, v237\n"
    "v_sub_f32 v175, v175, v237\n"
    "v_mfma_f32_16x16x32_bf16 v[160:163], a[124:127], a[44:47], v[160:163]\n"
    "ds_read_b64_tr_b16 a[108:109], v219 offset:0\n"
    "v_exp_f32 v176, v168\n"
    "v_exp_f32 v177, v169\n"
    "v_exp_f32 v178, v170\n"
    "v_mfma_f32_16x16x32_bf16 v[164:167], a[140:143], a[44:47], v[164:167]\n"
    "ds_read_b64_tr_b16 a[110:111], v219 offset:4096\n"
    "v_exp_f32 v179, v171\n"
    "v_exp_f32 v180, v172\n"
    "v_exp_f32 v181, v173\n"
    "s_waitcnt lgkmcnt(4)\n"
    "v_mfma_f32_16x16x32_bf16 v[0:3], a[80:83], v[224:227], 0\n"
    "v_exp_f32 v182, v174\n"
    "v_exp_f32 v183, v175\n"
    "v_sub_f32 v196, 0, v237\n"
    "v_mfma_f32_16x16x32_bf16 v[4:7], a[84:87], v[224:227], 0\n"
    "v_sub_f32 v197, 0, v237\n"
    "v_sub_f32 v198, 0, v237\n"
    "v_sub_f32 v199, 0, v237\n"
    "v_mfma_f32_16x16x32_bf16 v[8:11], a[88:91], v[224:227], 0\n"
    "v_add_f32 v184, v176, v177\n"
    "v_add_f32 v185, v178, v179\n"
    "v_add_f32 v186, v180, v181\n"
    "v_mfma_f32_16x16x32_bf16 v[12:15], a[92:95], v[224:227], 0\n"
    "v_add_f32 v187, v182, v183\n"
    "v_add_f32 v184, v184, v185\n"
    "v_add_f32 v186, v186, v187\n"
    "v_mfma_f32_16x16x32_bf16 v[16:19], a[96:99], v[224:227], 0\n"
    "v_add_f32 v184, v184, v186\n"
    "v_cvt_pk_bf16_f32 v228, v176, v177\n"
    "v_cvt_pk_bf16_f32 v229, v178, v179\n"
    "v_mfma_f32_16x16x32_bf16 v[20:23], a[100:103], v[224:227], 0\n"
    "v_cvt_pk_bf16_f32 v230, v180, v181\n"
    "v_cvt_pk_bf16_f32 v231, v182, v183\n"
    "v_mov_b32 v233, v184\n"
    "s_waitcnt lgkmcnt(2)\n"
    "v_mfma_f32_16x16x32_bf16 v[24:27], a[104:107], v[224:227], 0\n"
    "s_waitcnt lgkmcnt(0)\n"
    "v_mfma_f32_16x16x32_bf16 v[28:31], a[108:111], v[224:227], 0\n"
    "v_mfma_f32_16x16x32_bf16 v[168:171], a[112:115], a[48:51], 0\n"
    "v_max3_f32 v237, v160, v161, v162\n"
    "v_max3_f32 v238, v163, v164, v165\n"
    "v_max3_f32 v237, v237, v166, v167\n"
    "v_mfma_f32_16x16x32_bf16 v[172:175], a[128:131], a[48:51], 0\n"
    "v_max_f32 v237, v237, v238\n"
    "v_mov_b32 v238, v237\n"
    "s_nop 1\n"
    "v_permlane16_swap_b32 v237, v238\n"
    "v_mfma_f32_16x16x32_bf16 v[168:171], a[116:119], a[52:55], v[168:171]\n"
    "v_max_f32 v237, v237, v238\n"
    "v_mov_b32 v238, v237\n"
    "s_nop 1\n"
    "v_permlane32_swap_b32 v237, v238\n"
    "v_mfma_f32_16x16x32_bf16 v[172:175], a[132:135], a[52:55], v[172:175]\n"
    "v_max_f32 v237, v237, v238\n"
    "v_sub_f32 v160, v160, v237\n"
    "v_sub_f32 v161, v161, v237\n"
    "v_mfma_f32_16x16x32_bf16 v[168:171], a[120:123], a[56:59], v[168:171]\n"
    "v_sub_f32 v162, v162, v237\n"
    "v_sub_f32 v163, v163, v237\n"
    "v_sub_f32 v164, v164, v237\n"
    "v_mfma_f32_16x16x32_bf16 v[172:175], a[136:139], a[56:59], v[172:175]\n"
    "v_sub_f32 v165, v165, v237\n"
    "v_sub_f32 v166, v166, v237\n"
    "v_sub_f32 v167, v167, v237\n"
    "v_mfma_f32_16x16x32_bf16 v[168:171], a[124:127], a[60:63], v[168:171]\n"
    "v_exp_f32 v176, v160\n"
    "v_exp_f32 v177, v161\n"
    "v_exp_f32 v178, v162\n"
    "v_mfma_f32_16x16x32_bf16 v[172:175], a[140:143], a[60:63], v[172:175]\n"
    "v_exp_f32 v179, v163\n"
    "v_exp_f32 v180, v164\n"
    "v_exp_f32 v181, v165\n"
    "v_mfma_f32_16x16x32_bf16 v[32:35], a[80:83], v[228:231], 0\n"
    "v_exp_f32 v182, v166\n"
    "v_exp_f32 v183, v167\n"
    "v_sub_f32 v200, 0, v237\n"
    "v_mfma_f32_16x16x32_bf16 v[36:39], a[84:87], v[228:231], 0\n"
    "v_sub_f32 v201, 0, v237\n"
    "v_sub_f32 v202, 0, v237\n"
    "v_sub_f32 v203, 0, v237\n"
    "v_mfma_f32_16x16x32_bf16 v[40:43], a[88:91], v[228:231], 0\n"
    "v_add_f32 v184, v176, v177\n"
    "v_add_f32 v185, v178, v179\n"
    "v_add_f32 v186, v180, v181\n"
    "v_mfma_f32_16x16x32_bf16 v[44:47], a[92:95], v[228:231], 0\n"
    "v_add_f32 v187, v182, v183\n"
    "v_add_f32 v184, v184, v185\n"
    "v_add_f32 v186, v186, v187\n"
    "v_mfma_f32_16x16x32_bf16 v[48:51], a[96:99], v[228:231], 0\n"
    "v_add_f32 v184, v184, v186\n"
    "v_cvt_pk_bf16_f32 v224, v176, v177\n"
    "v_cvt_pk_bf16_f32 v225, v178, v179\n"
    "v_mfma_f32_16x16x32_bf16 v[52:55], a[100:103], v[228:231], 0\n"
    "v_cvt_pk_bf16_f32 v226, v180, v181\n"
    "v_cvt_pk_bf16_f32 v227, v182, v183\n"
    "v_mov_b32 v234, v184\n"
    "v_mfma_f32_16x16x32_bf16 v[56:59], a[104:107], v[228:231], 0\n"
    "v_mfma_f32_16x16x32_bf16 v[60:63], a[108:111], v[228:231], 0\n"
    "v_mfma_f32_16x16x32_bf16 v[160:163], a[112:115], a[64:67], 0\n"
    "s_waitcnt vmcnt(4)\n"
    "ds_read_b128 a[144:147], v220 offset:0\n"
    "v_max3_f32 v237, v168, v169, v170\n"
    "v_max3_f32 v238, v171, v172, v173\n"
    "v_max3_f32 v237, v237, v174, v175\n"
    "v_mfma_f32_16x16x32_bf16 v[164:167], a[128:131], a[64:67], 0\n"
    "s_waitcnt vmcnt(0)\n"
    "ds_read_b128 a[160:163], v220 offset:4096\n"
    "v_max_f32 v237, v237, v238\n"
    "v_mov_b32 v238, v237\n"
    "s_nop 1\n"
    "v_permlane16_swap_b32 v237, v238\n"
    "v_mfma_f32_16x16x32_bf16 v[160:163], a[116:119], a[68:71], v[160:163]\n"
    "ds_read_b128 a[148:151], v221 offset:0\n"
    "v_max_f32 v237, v237, v238\n"
    "v_mov_b32 v238, v237\n"
    "s_nop 1\n"
    "v_permlane32_swap_b32 v237, v238\n"
    "v_mfma_f32_16x16x32_bf16 v[164:167], a[132:135], a[68:71], v[164:167]\n"
    "ds_read_b128 a[164:167], v221 offset:4096\n"
    "v_max_f32 v237, v237, v238\n"
    "v_sub_f32 v168, v168, v237\n"
    "v_sub_f32 v169, v169, v237\n"
    "v_mfma_f32_16x16x32_bf16 v[160:163], a[120:123], a[72:75], v[160:163]\n"
    "ds_read_b128 a[152:155], v222 offset:0\n"
    "v_sub_f32 v170, v170, v237\n"
    "v_sub_f32 v171, v171, v237\n"
    "v_sub_f32 v172, v172, v237\n"
    "v_mfma_f32_16x16x32_bf16 v[164:167], a[136:139], a[72:75], v[164:167]\n"
    "ds_read_b128 a[168:171], v222 offset:4096\n"
    "v_sub_f32 v173, v173, v237\n"
    "v_sub_f32 v174, v174, v237\n"
    "v_sub_f32 v175, v175, v237\n"
    "v_mfma_f32_16x16x32_bf16 v[160:163], a[124:127], a[76:79], v[160:163]\n"
    "ds_read_b128 a[156:159], v223 offset:0\n"
    "v_exp_f32 v176, v168\n"
    "v_exp_f32 v177, v169\n"
    "v_exp_f32 v178, v170\n"
    "v_mfma_f32_16x16x32_bf16 v[164:167], a[140:143], a[76:79], v[164:167]\n"
    "ds_read_b128 a[172:175], v223 offset:4096\n"
    "v_exp_f32 v179, v171\n"
    "v_exp_f32 v180, v172\n"
    "v_exp_f32 v181, v173\n"
    "v_mfma_f32_16x16x32_bf16 v[64:67], a[80:83], v[224:227], 0\n"
    "v_exp_f32 v182, v174\n"
    "v_exp_f32 v183, v175\n"
    "v_sub_f32 v204, 0, v237\n"
    "v_mfma_f32_16x16x32_bf16 v[68:71], a[84:87], v[224:227], 0\n"
    "v_sub_f32 v205, 0, v237\n"
    "v_sub_f32 v206, 0, v237\n"
    "v_sub_f32 v207, 0, v237\n"
    "v_mfma_f32_16x16x32_bf16 v[72:75], a[88:91], v[224:227], 0\n"
    "v_add_f32 v184, v176, v177\n"
    "v_add_f32 v185, v178, v179\n"
    "v_add_f32 v186, v180, v181\n"
    "s_add_u32 m0, s74, 0\n"
    "s_add_u32 s71, s71, 4096\n"
    "s_and_b32 s71, s71, 0x1ffff\n"
    "s_waitcnt lgkmcnt(0)\n"
    "v_mfma_f32_16x16x32_bf16 v[76:79], a[92:95], v[224:227], 0\n"
    "buffer_load_dwordx4 v188, s[44:47], s71 offen offset:0 nt lds\n"
    "v_add_f32 v187, v182, v183\n"
    "v_add_f32 v184, v184, v185\n"
    "v_add_f32 v186, v186, v187\n"
    "v_mfma_f32_16x16x32_bf16 v[80:83], a[96:99], v[224:227], 0\n"
    "buffer_load_dwordx4 v189, s[44:47], s71 offen offset:1024 nt lds\n"
    "v_add_f32 v184, v184, v186\n"
    "v_cvt_pk_bf16_f32 v228, v176, v177\n"
    "v_cvt_pk_bf16_f32 v229, v178, v179\n"
    "v_mfma_f32_16x16x32_bf16 v[84:87], a[100:103], v[224:227], 0\n"
    "buffer_load_dwordx4 v190, s[44:47], s71 offen offset:2048 nt lds\n"
    "v_cvt_pk_bf16_f32 v230, v180, v181\n"
    "v_cvt_pk_bf16_f32 v231, v182, v183\n"
    "v_mov_b32 v235, v184\n"
    "v_mfma_f32_16x16x32_bf16 v[88:91], a[104:107], v[224:227], 0\n"
    "v_mfma_f32_16x16x32_bf16 v[92:95], a[108:111], v[224:227], 0\n"
    "buffer_load_dwordx4 v191, s[44:47], s71 offen offset:3072 nt lds\n"
    "v_mfma_f32_16x16x32_bf16 v[168:171], a[144:147], a[0:3], v[192:195]\n"
    "v_max3_f32 v237, v160, v161, v162\n"
    "v_max3_f32 v238, v163, v164, v165\n"
    "v_max3_f32 v237, v237, v166, v167\n"
    "s_add_u32 m0, s74, 4096\n"
    "s_add_u32 s71, s71, 4096\n"
    "s_and_b32 s71, s71, 0x1ffff\n"
    "v_mfma_f32_16x16x32_bf16 v[172:175], a[160:163], a[0:3], v[192:195]\n"
    "buffer_load_dwordx4 v188, s[44:47], s71 offen offset:0 nt lds\n"
    "v_max_f32 v237, v237, v238\n"
    "v_mov_b32 v238, v237\n"
    "s_nop 1\n"
    "v_permlane16_swap_b32 v237, v238\n"
    "v_mfma_f32_16x16x32_bf16 v[168:171], a[148:151], a[4:7], v[168:171]\n"
    "v_max_f32 v237, v237, v238\n"
    "v_mov_b32 v238, v237\n"
    "s_nop 1\n"
    "v_permlane32_swap_b32 v237, v238\n"
    "v_mfma_f32_16x16x32_bf16 v[172:175], a[164:167], a[4:7], v[172:175]\n"
    "buffer_load_dwordx4 v189, s[44:47], s71 offen offset:1024 nt lds\n"
    "v_max_f32 v237, v237, v238\n"
    "v_sub_f32 v160, v160, v237\n"
    "v_sub_f32 v161, v161, v237\n"
    "v_mfma_f32_16x16x32_bf16 v[168:171], a[152:155], a[8:11], v[168:171]\n"
    "v_sub_f32 v162, v162, v237\n"
    "v_sub_f32 v163, v163, v237\n"
    "v_sub_f32 v164, v164, v237\n"
    "v_mfma_f32_16x16x32_bf16 v[172:175], a[168:171], a[8:11], v[172:175]\n"
    "buffer_load_dwordx4 v190, s[44:47], s71 offen offset:2048 nt lds\n"
    "v_sub_f32 v165, v165, v237\n"
    "v_sub_f32 v166, v166, v237\n"
    "v_sub_f32 v167, v167, v237\n"
    "v_mfma_f32_16x16x32_bf16 v[168:171], a[156:159], a[12:15], v[168:171]\n"
    "v_exp_f32 v176, v160\n"
    "v_exp_f32 v177, v161\n"
    "v_exp_f32 v178, v162\n"
    "v_mfma_f32_16x16x32_bf16 v[172:175], a[172:175], a[12:15], v[172:175]\n"
    "buffer_load_dwordx4 v191, s[44:47], s71 offen offset:3072 nt lds\n"
    "v_exp_f32 v179, v163\n"
    "v_exp_f32 v180, v164\n"
    "v_exp_f32 v181, v165\n"
    "v_mfma_f32_16x16x32_bf16 v[96:99], a[80:83], v[228:231], 0\n"
    "v_exp_f32 v182, v166\n"
    "v_exp_f32 v183, v167\n"
    "v_sub_f32 v208, 0, v237\n"
    "v_mfma_f32_16x16x32_bf16 v[100:103], a[84:87], v[228:231], 0\n"
    "v_sub_f32 v209, 0, v237\n"
    "v_sub_f32 v210, 0, v237\n"
    "v_sub_f32 v211, 0, v237\n"
    "v_mfma_f32_16x16x32_bf16 v[104:107], a[88:91], v[228:231], 0\n"
    "v_add_f32 v184, v176, v177\n"
    "v_add_f32 v185, v178, v179\n"
    "v_add_f32 v186, v180, v181\n"
    "v_mfma_f32_16x16x32_bf16 v[108:111], a[92:95], v[228:231], 0\n"
    "v_add_f32 v187, v182, v183\n"
    "v_add_f32 v184, v184, v185\n"
    "v_add_f32 v186, v186, v187\n"
    "v_mfma_f32_16x16x32_bf16 v[112:115], a[96:99], v[228:231], 0\n"
    "v_add_f32 v184, v184, v186\n"
    "v_cvt_pk_bf16_f32 v224, v176, v177\n"
    "v_cvt_pk_bf16_f32 v225, v178, v179\n"
    "v_mfma_f32_16x16x32_bf16 v[116:119], a[100:103], v[228:231], 0\n"
    "v_cvt_pk_bf16_f32 v226, v180, v181\n"
    "v_cvt_pk_bf16_f32 v227, v182, v183\n"
    "v_mov_b32 v236, v184\n"
    "v_mfma_f32_16x16x32_bf16 v[120:123], a[104:107], v[228:231], 0\n"
    "v_mfma_f32_16x16x32_bf16 v[124:127], a[108:111], v[228:231], 0\n"
    "v_mfma_f32_16x16x32_bf16 v[160:163], a[144:147], a[16:19], v[196:199]\n"
    "v_exp_f32 v176, v168\n"
    "v_mfma_f32_16x16x32_bf16 v[164:167], a[160:163], a[16:19], v[196:199]\n"
    "v_exp_f32 v177, v169\n"
    "v_mfma_f32_16x16x32_bf16 v[160:163], a[148:151], a[20:23], v[160:163]\n"
    "v_exp_f32 v178, v170\n"
    "v_mfma_f32_16x16x32_bf16 v[164:167], a[164:167], a[20:23], v[164:167]\n"
    "v_exp_f32 v179, v171\n"
    "v_mfma_f32_16x16x32_bf16 v[160:163], a[152:155], a[24:27], v[160:163]\n"
    "v_exp_f32 v180, v172\n"
    "v_mfma_f32_16x16x32_bf16 v[164:167], a[168:171], a[24:27], v[164:167]\n"
    "v_exp_f32 v181, v173\n"
    "v_mfma_f32_16x16x32_bf16 v[160:163], a[156:159], a[28:31], v[160:163]\n"
    "v_exp_f32 v182, v174\n"
    "v_mfma_f32_16x16x32_bf16 v[164:167], a[172:175], a[28:31], v[164:167]\n"
    "v_exp_f32 v183, v175\n"
    "v_mfma_f32_16x16x32_bf16 v[128:131], a[80:83], v[224:227], 0\n"
    "ds_read_b64_tr_b16 a[80:81], v212 offset:32768\n"
    "v_add_f32 v184, v176, v177\n"
    "v_add_f32 v185, v178, v179\n"
    "v_mfma_f32_16x16x32_bf16 v[132:135], a[84:87], v[224:227], 0\n"
    "ds_read_b64_tr_b16 a[82:83], v212 offset:36864\n"
    "v_add_f32 v186, v180, v181\n"
    "v_add_f32 v187, v182, v183\n"
    "v_mfma_f32_16x16x32_bf16 v[136:139], a[88:91], v[224:227], 0\n"
    "ds_read_b64_tr_b16 a[84:85], v213 offset:32768\n"
    "v_add_f32 v184, v184, v185\n"
    "v_add_f32 v186, v186, v187\n"
    "v_mfma_f32_16x16x32_bf16 v[140:143], a[92:95], v[224:227], 0\n"
    "ds_read_b64_tr_b16 a[86:87], v213 offset:36864\n"
    "v_add_f32 v184, v184, v186\n"
    "v_cvt_pk_bf16_f32 v228, v176, v177\n"
    "v_mfma_f32_16x16x32_bf16 v[144:147], a[96:99], v[224:227], 0\n"
    "ds_read_b64_tr_b16 a[88:89], v214 offset:32768\n"
    "v_cvt_pk_bf16_f32 v229, v178, v179\n"
    "v_cmp_lt_f32 vcc, s77, v184\n"
    "v_mfma_f32_16x16x32_bf16 v[148:151], a[100:103], v[224:227], 0\n"
    "ds_read_b64_tr_b16 a[90:91], v214 offset:36864\n"
    "v_cvt_pk_bf16_f32 v230, v180, v181\n"
    "v_cvt_pk_bf16_f32 v231, v182, v183\n"
    "v_mfma_f32_16x16x32_bf16 v[152:155], a[104:107], v[224:227], 0\n"
    "ds_read_b64_tr_b16 a[92:93], v215 offset:32768\n"
    "s_cbranch_vccnz L_rareB_h5%=\n"
    "L_rareB_h5_r%=:\n"
    "v_add_f32 v232, v232, v184\n"
    "v_mfma_f32_16x16x32_bf16 v[156:159], a[108:111], v[224:227], 0\n"
    "ds_read_b64_tr_b16 a[94:95], v215 offset:36864\n"
    "s_mov_b32 s82, 6\n"
    "L_attB%=:\n"
    "v_mfma_f32_16x16x32_bf16 v[168:171], a[144:147], a[32:35], v[200:203]\n"
    "ds_read_b64_tr_b16 a[96:97], v216 offset:32768\n"
    "v_exp_f32 v176, v160\n"
    "v_mfma_f32_16x16x32_bf16 v[172:175], a[160:163], a[32:35], v[200:203]\n"
    "ds_read_b64_tr_b16 a[98:99], v216 offset:36864\n"
    "v_exp_f32 v177, v161\n"
    "v_mfma_f32_16x16x32_bf16 v[168:171], a[148:151], a[36:39], v[168:171]\n"
    "ds_read_b64_tr_b16 a[100:101], v217 offset:32768\n"
    "v_exp_f32 v178, v162\n"
    "v_mfma_f32_16x16x32_bf16 v[172:175], a[164:167], a[36:39], v[172:175]\n"
    "ds_read_b64_tr_b16 a[102:103], v217 offset:36864\n"
    "v_exp_f32 v179, v163\n"
    "v_mfma_f32_16x16x32_bf16 v[168:171], a[152:155], a[40:43], v[168:171]\n"
    "ds_read_b64_tr_b16 a[104:105], v218 offset:32768\n"
    "v_exp_f32 v180, v164\n"
    "v_mfma_f32_16x16x32_bf16 v[172:175], a[168:171], a[40:43], v[172:175]\n"
    "ds_read_b64_tr_b16 a[106:107], v218 offset:36864\n"
    "v_exp_f32 v181, v165\n"
    "v_mfma_f32_16x16x32_bf16 v[168:171], a[156:159], a[44:47], v[168:171]\n"
    "ds_read_b64_tr_b16 a[108:109], v219 offset:32768\n"
    "v_exp_f32 v182, v166\n"
    "v_mfma_f32_16x16x32_bf16 v[172:175], a[172:175], a[44:47], v[172:175]\n"
    "ds_read_b64_tr_b16 a[110:111], v219 offset:36864\n"
    "v_exp_f32 v183, v167\n"
    "s_waitcnt lgkmcnt(4)\n"
    "v_mfma_f32_16x16x32_bf16 v[0:3], a[80:83], v[228:231], v[0:3]\n"
    "v_add_f32 v184, v176, v177\n"
    "v_add_f32 v185, v178, v179\n"
    "s_add_u32 m0, s87, 0\n"
    "s_add_u32 s85, s85, 4096\n"
    "s_and_b32 s85, s85, 0x1ffff\n"
    "v_mfma_f32_16x16x32_bf16 v[4:7], a[84:87], v[228:231], v[4:7]\n"
    "buffer_load_dwordx4 v242, s[48:51], s85 offen offset:0 nt lds\n"
    "v_add_f32 v186, v180, v181\n"
    "v_add_f32 v187, v182, v183\n"
    "v_mfma_f32_16x16x32_bf16 v[8:11], a[88:91], v[228:231], v[8:11]\n"
    "v_add_f32 v184, v184, v185\n"
    "v_add_f32 v186, v186, v187\n"
    "v_mfma_f32_16x16x32_bf16 v[12:15], a[92:95], v[228:231], v[12:15]\n"
    "buffer_load_dwordx4 v243, s[48:51], s85 offen offset:1024 nt lds\n"
    "v_add_f32 v184, v184, v186\n"
    "v_cvt_pk_bf16_f32 v224, v176, v177\n"
    "v_mfma_f32_16x16x32_bf16 v[16:19], a[96:99], v[228:231], v[16:19]\n"
    "v_cvt_pk_bf16_f32 v225, v178, v179\n"
    "v_cmp_lt_f32 vcc, s77, v184\n"
    "v_mfma_f32_16x16x32_bf16 v[20:23], a[100:103], v[228:231], v[20:23]\n"
    "buffer_load_dwordx4 v242, s[48:51], s85 offen offset:2048 nt lds\n"
    "v_cvt_pk_bf16_f32 v226, v180, v181\n"
    "v_cvt_pk_bf16_f32 v227, v182, v183\n"
    "s_waitcnt lgkmcnt(2)\n"
    "v_mfma_f32_16x16x32_bf16 v[24:27], a[104:107], v[228:231], v[24:27]\n"
    "s_cbranch_vccnz L_rareB_l0%=\n"
    "L_rareB_l0_r%=:\n"
    "v_add_f32 v233, v233, v184\n"
    "s_waitcnt lgkmcnt(0)\n"
    "v_mfma_f32_16x16x32_bf16 v[28:31], a[108:111], v[228:231], v[28:31]\n"
    "buffer_load_dwordx4 v243, s[48:51], s85 offen offset:3072 nt lds\n"
    "v_mfma_f32_16x16x32_bf16 v[160:163], a[144:147], a[48:51], v[204:207]\n"
    "v_exp_f32 v176, v168\n"
    "s_add_u32 m0, s87, 4096\n"
    "s_add_u32 s85, s85, 4096\n"
    "s_and_b32 s85, s85, 0x1ffff\n"
    "v_mfma_f32_16x16x32_bf16 v[164:167], a[160:163], a[48:51], v[204:207]\n"
    "buffer_load_dwordx4 v242, s[48:51], s85 offen offset:0 nt lds\n"
    "v_exp_f32 v177, v169\n"
    "v_mfma_f32_16x16x32_bf16 v[160:163], a[148:151], a[52:55], v[160:163]\n"
    "v_exp_f32 v178, v170\n"
    "v_mfma_f32_16x16x32_bf16 v[164:167], a[164:167], a[52:55], v[164:167]\n"
    "buffer_load_dwordx4 v243, s[48:51], s85 offen offset:1024 nt lds\n"
    "v_exp_f32 v179, v171\n"
    "v_mfma_f32_16x16x32_bf16 v[160:163], a[152:155], a[56:59], v[160:163]\n"
    "v_exp_f32 v180, v172\n"
    "v_mfma_f32_16x16x32_bf16 v[164:167], a[168:171], a[56:59], v[164:167]\n"
    "buffer_load_dwordx4 v242, s[48:51], s85 offen offset:2048 nt lds\n"
    "v_exp_f32 v181, v173\n"
    "v_mfma_f32_16x16x32_bf16 v[160:163], a[156:159], a[60:63], v[160:163]\n"
    "v_exp_f32 v182, v174\n"
    "v_mfma_f32_16x16x32_bf16 v[164:167], a[172:175], a[60:63], v[164:167]\n"
    "buffer_load_dwordx4 v243, s[48:51], s85 offen offset:3072 nt lds\n"
    "v_exp_f32 v183, v175\n"
    "v_mfma_f32_16x16x32_bf16 v[32:35], a[80:83], v[224:227], v[32:35]\n"
    "v_add_f32 v184, v176, v177\n"
    "v_add_f32 v185, v178, v179\n"
    "s_barrier\n"
    "v_mfma_f32_16x16x32_bf16 v[36:39], a[84:87], v[224:227], v[36:39]\n"
    "v_add_f32 v186, v180, v181\n"
    "v_add_f32 v187, v182, v183\n"
    "v_mfma_f32_16x16x32_bf16 v[40:43], a[88:91], v[224:227], v[40:43]\n"
    "v_add_f32 v184, v184, v185\n"
    "v_add_f32 v186, v186, v187\n"
    "v_mfma_f32_16x16x32_bf16 v[44:47], a[92:95], v[224:227], v[44:47]\n"
    "v_add_f32 v184, v184, v186\n"
    "v_cvt_pk_bf16_f32 v228, v176, v177\n"
    "v_mfma_f32_16x16x32_bf16 v[48:51], a[96:99], v[224:227], v[48:51]\n"
    "v_cvt_pk_bf16_f32 v229, v178, v179\n"
    "v_cmp_lt_f32 vcc, s77, v184\n"
    "v_mfma_f32_16x16x32_bf16 v[52:55], a[100:103], v[224:227], v[52:55]\n"
    "v_cvt_pk_bf16_f32 v230, v180, v181\n"
    "v_cvt_pk_bf16_f32 v231, v182, v183\n"
    "v_mfma_f32_16x16x32_bf16 v[56:59], a[104:107], v[224:227], v[56:59]\n"
    "s_cbranch_vccnz L_rareB_l1%=\n"
    "L_rareB_l1_r%=:\n"
    "v_add_f32 v234, v234, v184\n"
    "v_mfma_f32_16x16x32_bf16 v[60:63], a[108:111], v[224:227], v[60:63]\n"
    "v_mfma_f32_16x16x32_bf16 v[168:171], a[144:147], a[64:67], v[208:211]\n"
    "s_waitcnt vmcnt(12)\n"
    "ds_read_b128 a[112:115], v220 offset:0\n"
    "v_exp_f32 v176, v160\n"
    "v_mfma_f32_16x16x32_bf16 v[172:175], a[160:163], a[64:67], v[208:211]\n"
    "s_waitcnt vmcnt(8)\n"
    "ds_read_b128 a[128:131], v220 offset:4096\n"
    "v_exp_f32 v177, v161\n"
    "v_mfma_f32_16x16x32_bf16 v[168:171], a[148:151], a[68:71], v[168:171]\n"
    "ds_read_b128 a[116:119], v221 offset:0\n"
    "v_exp_f32 v178, v162\n"
    "v_mfma_f32_16x16x32_bf16 v[172:175], a[164:167], a[68:71], v[172:175]\n"
    "ds_read_b128 a[132:135], v221 offset:4096\n"
    "v_exp_f32 v179, v163\n"
    "v_mfma_f32_16x16x32_bf16 v[168:171], a[152:155], a[72:75], v[168:171]\n"
    "ds_read_b128 a[120:123], v222 offset:0\n"
    "v_exp_f32 v180, v164\n"
    "v_mfma_f32_16x16x32_bf16 v[172:175], a[168:171], a[72:75], v[172:175]\n"
    "ds_read_b128 a[136:139], v222 offset:4096\n"
    "v_exp_f32 v181, v165\n"
    "v_mfma_f32_16x16x32_bf16 v[168:171], a[156:159], a[76:79], v[168:171]\n"
    "ds_read_b128 a[124:127], v223 offset:0\n"
    "v_exp_f32 v182, v166\n"
    "v_mfma_f32_16x16x32_bf16 v[172:175], a[172:175], a[76:79], v[172:175]\n"
    "ds_read_b128 a[140:143], v223 offset:4096\n"
    "v_exp_f32 v183, v167\n"
    "v_mfma_f32_16x16x32_bf16 v[64:67], a[80:83], v[228:231], v[64:67]\n"
    "v_add_f32 v184, v176, v177\n"
    "v_add_f32 v185, v178, v179\n"
    "v_mfma_f32_16x16x32_bf16 v[68:71], a[84:87], v[228:231], v[68:71]\n"
    "v_add_f32 v186, v180, v181\n"
    "v_add_f32 v187, v182, v183\n"
    "v_mfma_f32_16x16x32_bf16 v[72:75], a[88:91], v[228:231], v[72:75]\n"
    "v_add_f32 v184, v184, v185\n"
    "v_add_f32 v186, v186, v187\n"
    "s_add_u32 m0, s74, 0\n"
    "s_add_u32 s71, s71, 4096\n"
    "s_and_b32 s71, s71, 0x1ffff\n"
    "s_waitcnt lgkmcnt(0)\n"
    "v_mfma_f32_16x16x32_bf16 v[76:79], a[92:95], v[228:231], v[76:79]\n"
    "buffer_load_dwordx4 v188, s[44:47], s71 offen offset:0 nt lds\n"
    "v_add_f32 v184, v184, v186\n"
    "v_cvt_pk_bf16_f32 v224, v176, v177\n"
    "v_mfma_f32_16x16x32_bf16 v[80:83], a[96:99], v[228:231], v[80:83]\n"
    "buffer_load_dwordx4 v189, s[44:47], s71 offen offset:1024 nt lds\n"
    "v_cvt_pk_bf16_f32 v225, v178, v179\n"
    "v_cmp_lt_f32 vcc, s77, v184\n"
    "v_mfma_f32_16x16x32_bf16 v[84:87], a[100:103], v[228:231], v[84:87]\n"
    "buffer_load_dwordx4 v190, s[44:47], s71 offen offset:2048 nt lds\n"
    "v_cvt_pk_bf16_f32 v226, v180, v181\n"
    "v_cvt_pk_bf16_f32 v227, v182, v183\n"
    "v_mfma_f32_16x16x32_bf16 v[88:91], a[104:107], v[228:231], v[88:91]\n"
    "s_cbranch_vccnz L_rareB_l2%=\n"
    "L_rareB_l2_r%=:\n"
    "v_add_f32 v235, v235, v184\n"
    "v_mfma_f32_16x16x32_bf16 v[92:95], a[108:111], v[228:231], v[92:95]\n"
    "buffer_load_dwordx4 v191, s[44:47], s71 offen offset:3072 nt lds\n"
    "v_mfma_f32_16x16x32_bf16 v[160:163], a[112:115], a[0:3], v[192:195]\n"
    "v_exp_f32 v176, v168\n"
    "s_add_u32 m0, s74, 4096\n"
    "s_add_u32 s71, s71, 4096\n"
    "s_and_b32 s71, s71, 0x1ffff\n"
    "v_mfma_f32_16x16x32_bf16 v[164:167], a[128:131], a[0:3], v[192:195]\n"
    "buffer_load_dwordx4 v188, s[44:47], s71 offen offset:0 nt lds\n"
    "v_exp_f32 v177, v169\n"
    "v_mfma_f32_16x16x32_bf16 v[160:163], a[116:119], a[4:7], v[160:163]\n"
    "v_exp_f32 v178, v170\n"
    "v_mfma_f32_16x16x32_bf16 v[164:167], a[132:135], a[4:7], v[164:167]\n"
    "buffer_load_dwordx4 v189, s[44:47], s71 offen offset:1024 nt lds\n"
    "v_exp_f32 v179, v171\n"
    "v_mfma_f32_16x16x32_bf16 v[160:163], a[120:123], a[8:11], v[160:163]\n"
    "v_exp_f32 v180, v172\n"
    "v_mfma_f32_16x16x32_bf16 v[164:167], a[136:139], a[8:11], v[164:167]\n"
    "buffer_load_dwordx4 v190, s[44:47], s71 offen offset:2048 nt lds\n"
    "v_exp_f32 v181, v173\n"
    "v_mfma_f32_16x16x32_bf16 v[160:163], a[124:127], a[12:15], v[160:163]\n"
    "v_exp_f32 v182, v174\n"
    "v_mfma_f32_16x16x32_bf16 v[164:167], a[140:143], a[12:15], v[164:167]\n"
    "buffer_load_dwordx4 v191, s[44:47], s71 offen offset:3072 nt lds\n"
    "v_exp_f32 v183, v175\n"
    "v_mfma_f32_16x16x32_bf16 v[96:99], a[80:83], v[224:227], v[96:99]\n"
    "v_add_f32 v184, v176, v177\n"
    "v_add_f32 v185, v178, v179\n"
    "v_mfma_f32_16x16x32_bf16 v[100:103], a[84:87], v[224:227], v[100:103]\n"
    "v_add_f32 v186, v180, v181\n"
    "v_add_f32 v187, v182, v183\n"
    "v_mfma_f32_16x16x32_bf16 v[104:107], a[88:91], v[224:227], v[104:107]\n"
    "v_add_f32 v184, v184, v185\n"
    "v_add_f32 v186, v186, v187\n"
    "v_mfma_f32_16x16x32_bf16 v[108:111], a[92:95], v[224:227], v[108:111]\n"
    "v_add_f32 v184, v184, v186\n"
    "v_cvt_pk_bf16_f32 v228, v176, v177\n"
    "v_mfma_f32_16x16x32_bf16 v[112:115], a[96:99], v[224:227], v[112:115]\n"
    "v_cvt_pk_bf16_f32 v229, v178, v179\n"
    "v_cmp_lt_f32 vcc, s77, v184\n"
    "v_mfma_f32_16x16x32_bf16 v[116:119], a[100:103], v[224:227], v[116:119]\n"
    "v_cvt_pk_bf16_f32 v230, v180, v181\n"
    "v_cvt_pk_bf16_f32 v231, v182, v183\n"
    "v_mfma_f32_16x16x32_bf16 v[120:123], a[104:107], v[224:227], v[120:123]\n"
    "s_cbranch_vccnz L_rareB_l3%=\n"
    "L_rareB_l3_r%=:\n"
    "v_add_f32 v236, v236, v184\n"
    "v_mfma_f32_16x16x32_bf16 v[124:127], a[108:111], v[224:227], v[124:127]\n"
    "v_mfma_f32_16x16x32_bf16 v[168:171], a[112:115], a[16:19], v[196:199]\n"
    "v_exp_f32 v176, v160\n"
    "v_mfma_f32_16x16x32_bf16 v[172:175], a[128:131], a[16:19], v[196:199]\n"
    "v_exp_f32 v177, v161\n"
    "v_mfma_f32_16x16x32_bf16 v[168:171], a[116:119], a[20:23], v[168:171]\n"
    "v_exp_f32 v178, v162\n"
    "v_mfma_f32_16x16x32_bf16 v[172:175], a[132:135], a[20:23], v[172:175]\n"
    "v_exp_f32 v179, v163\n"
    "v_mfma_f32_16x16x32_bf16 v[168:171], a[120:123], a[24:27], v[168:171]\n"
    "v_exp_f32 v180, v164\n"
    "v_mfma_f32_16x16x32_bf16 v[172:175], a[136:139], a[24:27], v[172:175]\n"
    "v_exp_f32 v181, v165\n"
    "v_mfma_f32_16x16x32_bf16 v[168:171], a[124:127], a[28:31], v[168:171]\n"
    "v_exp_f32 v182, v166\n"
    "v_mfma_f32_16x16x32_bf16 v[172:175], a[140:143], a[28:31], v[172:175]\n"
    "v_exp_f32 v183, v167\n"
    "v_mfma_f32_16x16x32_bf16 v[128:131], a[80:83], v[228:231], v[128:131]\n"
    "s_waitcnt vmcnt(12)\n"
    "ds_read_b64_tr_b16 a[80:81], v212 offset:0\n"
    "v_add_f32 v184, v176, v177\n"
    "v_add_f32 v185, v178, v179\n"
    "v_mfma_f32_16x16x32_bf16 v[132:135], a[84:87], v[228:231], v[132:135]\n"
    "s_waitcnt vmcnt(8)\n"
    "ds_read_b64_tr_b16 a[82:83], v212 offset:4096\n"
    "v_add_f32 v186, v180, v181\n"
    "v_add_f32 v187, v182, v183\n"
    "v_mfma_f32_16x16x32_bf16 v[136:139], a[88:91], v[228:231], v[136:139]\n"
    "ds_read_b64_tr_b16 a[84:85], v213 offset:0\n"
    "v_add_f32 v184, v184, v185\n"
    "v_add_f32 v186, v186, v187\n"
    "v_mfma_f32_16x16x32_bf16 v[140:143], a[92:95], v[228:231], v[140:143]\n"
    "ds_read_b64_tr_b16 a[86:87], v213 offset:4096\n"
    "v_add_f32 v184, v184, v186\n"
    "v_cvt_pk_bf16_f32 v224, v176, v177\n"
    "v_mfma_f32_16x16x32_bf16 v[144:147], a[96:99], v[228:231], v[144:147]\n"
    "ds_read_b64_tr_b16 a[88:89], v214 offset:0\n"
    "v_cvt_pk_bf16_f32 v225, v178, v179\n"
    "v_cmp_lt_f32 vcc, s77, v184\n"
    "v_mfma_f32_16x16x32_bf16 v[148:151], a[100:103], v[228:231], v[148:151]\n"
    "ds_read_b64_tr_b16 a[90:91], v214 offset:4096\n"
    "v_cvt_pk_bf16_f32 v226, v180, v181\n"
    "v_cvt_pk_bf16_f32 v227, v182, v183\n"
    "v_mfma_f32_16x16x32_bf16 v[152:155], a[104:107], v[228:231], v[152:155]\n"
    "ds_read_b64_tr_b16 a[92:93], v215 offset:0\n"
    "s_cbranch_vccnz L_rareB_l4%=\n"
    "L_rareB_l4_r%=:\n"
    "v_add_f32 v232, v232, v184\n"
    "v_mfma_f32_16x16x32_bf16 v[156:159], a[108:111], v[228:231], v[156:159]\n"
    "ds_read_b64_tr_b16 a[94:95], v215 offset:4096\n"
    "v_mfma_f32_16x16x32_bf16 v[160:163], a[112:115], a[32:35], v[200:203]\n"
    "ds_read_b64_tr_b16 a[96:97], v216 offset:0\n"
    "v_exp_f32 v176, v168\n"
    "v_mfma_f32_16x16x32_bf16 v[164:167], a[128:131], a[32:35], v[200:203]\n"
    "ds_read_b64_tr_b16 a[98:99], v216 offset:4096\n"
    "v_exp_f32 v177, v169\n"
    "v_mfma_f32_16x16x32_bf16 v[160:163], a[116:119], a[36:39], v[160:163]\n"
    "ds_read_b64_tr_b16 a[100:101], v217 offset:0\n"
    "v_exp_f32 v178, v170\n"
    "v_mfma_f32_16x16x32_bf16 v[164:167], a[132:135], a[36:39], v[164:167]\n"
    "ds_read_b64_tr_b16 a[102:103], v217 offset:4096\n"
    "v_exp_f32 v179, v171\n"
    "v_mfma_f32_16x16x32_bf16 v[160:163], a[120:123], a[40:43], v[160:163]\n"
    "ds_read_b64_tr_b16 a[104:105], v218 offset:0\n"
    "v_exp_f32 v180, v172\n"
    "v_mfma_f32_16x16x32_bf16 v[164:167], a[136:139], a[40:43], v[164:167]\n"
    "ds_read_b64_tr_b16 a[106:107], v218 offset:4096\n"
    "v_exp_f32 v181, v173\n"
    "v_mfma_f32_16x16x32_bf16 v[160:163], a[124:127], a[44:47], v[160:163]\n"
    "ds_read_b64_tr_b16 a[108:109], v219 offset:0\n"
    "v_exp_f32 v182, v174\n"
    "v_mfma_f32_16x16x32_bf16 v[164:167], a[140:143], a[44:47], v[164:167]\n"
    "ds_read_b64_tr_b16 a[110:111], v219 offset:4096\n"
    "v_exp_f32 v183, v175\n"
    "s_waitcnt lgkmcnt(4)\n"
    "v_mfma_f32_16x16x32_bf16 v[0:3], a[80:83], v[224:227], v[0:3]\n"
    "v_add_f32 v184, v176, v177\n"
    "v_add_f32 v185, v178, v179\n"
    "s_add_u32 m0, s87, 32768\n"
    "s_add_u32 s85, s85, 4096\n"
    "s_and_b32 s85, s85, 0x1ffff\n"
    "v_mfma_f32_16x16x32_bf16 v[4:7], a[84:87], v[224:227], v[4:7]\n"
    "buffer_load_dwordx4 v242, s[48:51], s85 offen offset:0 nt lds\n"
    "v_add_f32 v186, v180, v181\n"
    "v_add_f32 v187, v182, v183\n"
    "v_mfma_f32_16x16x32_bf16 v[8:11], a[88:91], v[224:227], v[8:11]\n"
    "v_add_f32 v184, v184, v185\n"
    "v_add_f32 v186, v186, v187\n"
    "v_mfma_f32_16x16x32_bf16 v[12:15], a[92:95], v[224:227], v[12:15]\n"
    "buffer_load_dwordx4 v243, s[48:51], s85 offen offset:1024 nt lds\n"
    "v_add_f32 v184, v184, v186\n"
    "v_cvt_pk_bf16_f32 v228, v176, v177\n"
    "v_mfma_f32_16x16x32_bf16 v[16:19], a[96:99], v[224:227], v[16:19]\n"
    "v_cvt_pk_bf16_f32 v229, v178, v179\n"
    "v_cmp_lt_f32 vcc, s77, v184\n"
    "v_mfma_f32_16x16x32_bf16 v[20:23], a[100:103], v[224:227], v[20:23]\n"
    "buffer_load_dwordx4 v242, s[48:51], s85 offen offset:2048 nt lds\n"
    "v_cvt_pk_bf16_f32 v230, v180, v181\n"
    "v_cvt_pk_bf16_f32 v231, v182, v183\n"
    "s_waitcnt lgkmcnt(2)\n"
    "v_mfma_f32_16x16x32_bf16 v[24:27], a[104:107], v[224:227], v[24:27]\n"
    "s_cbranch_vccnz L_rareB_l5%=\n"
    "L_rareB_l5_r%=:\n"
    "v_add_f32 v233, v233, v184\n"
    "s_waitcnt lgkmcnt(0)\n"
    "v_mfma_f32_16x16x32_bf16 v[28:31], a[108:111], v[224:227], v[28:31]\n"
    "buffer_load_dwordx4 v243, s[48:51], s85 offen offset:3072 nt lds\n"
    "v_mfma_f32_16x16x32_bf16 v[168:171], a[112:115], a[48:51], v[204:207]\n"
    "v_exp_f32 v176, v160\n"
    "s_add_u32 m0, s87, 36864\n"
    "s_add_u32 s85, s85, 4096\n"
    "s_and_b32 s85, s85, 0x1ffff\n"
    "v_mfma_f32_16x16x32_bf16 v[172:175], a[128:131], a[48:51], v[204:207]\n"
    "buffer_load_dwordx4 v242, s[48:51], s85 offen offset:0 nt lds\n"
    "v_exp_f32 v177, v161\n"
    "v_mfma_f32_16x16x32_bf16 v[168:171], a[116:119], a[52:55], v[168:171]\n"
    "v_exp_f32 v178, v162\n"
    "v_mfma_f32_16x16x32_bf16 v[172:175], a[132:135], a[52:55], v[172:175]\n"
    "buffer_load_dwordx4 v243, s[48:51], s85 offen offset:1024 nt lds\n"
    "v_exp_f32 v179, v163\n"
    "v_mfma_f32_16x16x32_bf16 v[168:171], a[120:123], a[56:59], v[168:171]\n"
    "v_exp_f32 v180, v164\n"
    "v_mfma_f32_16x16x32_bf16 v[172:175], a[136:139], a[56:59], v[172:175]\n"
    "buffer_load_dwordx4 v242, s[48:51], s85 offen offset:2048 nt lds\n"
    "v_exp_f32 v181, v165\n"
    "v_mfma_f32_16x16x32_bf16 v[168:171], a[124:127], a[60:63], v[168:171]\n"
    "v_exp_f32 v182, v166\n"
    "v_mfma_f32_16x16x32_bf16 v[172:175], a[140:143], a[60:63], v[172:175]\n"
    "buffer_load_dwordx4 v243, s[48:51], s85 offen offset:3072 nt lds\n"
    "v_exp_f32 v183, v167\n"
    "v_mfma_f32_16x16x32_bf16 v[32:35], a[80:83], v[228:231], v[32:35]\n"
    "v_add_f32 v184, v176, v177\n"
    "v_add_f32 v185, v178, v179\n"
    "v_mfma_f32_16x16x32_bf16 v[36:39], a[84:87], v[228:231], v[36:39]\n"
    "v_add_f32 v186, v180, v181\n"
    "v_add_f32 v187, v182, v183\n"
    "v_mfma_f32_16x16x32_bf16 v[40:43], a[88:91], v[228:231], v[40:43]\n"
    "v_add_f32 v184, v184, v185\n"
    "v_add_f32 v186, v186, v187\n"
    "v_mfma_f32_16x16x32_bf16 v[44:47], a[92:95], v[228:231], v[44:47]\n"
    "v_add_f32 v184, v184, v186\n"
    "v_cvt_pk_bf16_f32 v224, v176, v177\n"
    "v_mfma_f32_16x16x32_bf16 v[48:51], a[96:99], v[228:231], v[48:51]\n"
    "v_cvt_pk_bf16_f32 v225, v178, v179\n"
    "v_cmp_lt_f32 vcc, s77, v184\n"
    "v_mfma_f32_16x16x32_bf16 v[52:55], a[100:103], v[228:231], v[52:55]\n"
    "v_cvt_pk_bf16_f32 v226, v180, v181\n"
    "v_cvt_pk_bf16_f32 v227, v182, v183\n"
    "v_mfma_f32_16x16x32_bf16 v[56:59], a[104:107], v[228:231], v[56:59]\n"
    "s_cbranch_vccnz L_rareB_l6%=\n"
    "L_rareB_l6_r%=:\n"
    "v_add_f32 v234, v234, v184\n"
    "v_mfma_f32_16x16x32_bf16 v[60:63], a[108:111], v[228:231], v[60:63]\n"
    "v_mfma_f32_16x16x32_bf16 v[160:163], a[112:115], a[64:67], v[208:211]\n"
    "s_waitcnt vmcnt(12)\n"
    "ds_read_b128 a[144:147], v220 offset:0\n"
    "v_exp_f32 v176, v168\n"
    "v_mfma_f32_16x16x32_bf16 v[164:167], a[128:131], a[64:67], v[208:211]\n"
    "s_waitcnt vmcnt(8)\n"
    "ds_read_b128 a[160:163], v220 offset:4096\n"
    "v_exp_f32 v177, v169\n"
    "v_mfma_f32_16x16x32_bf16 v[160:163], a[116:119], a[68:71], v[160:163]\n"
    "ds_read_b128 a[148:151], v221 offset:0\n"
    "v_exp_f32 v178, v170\n"
    "v_mfma_f32_16x16x32_bf16 v[164:167], a[132:135], a[68:71], v[164:167]\n"
    "ds_read_b128 a[164:167], v221 offset:4096\n"
    "v_exp_f32 v179, v171\n"
    "v_mfma_f32_16x16x32_bf16 v[160:163], a[120:123], a[72:75], v[160:163]\n"
    "ds_read_b128 a[152:155], v222 offset:0\n"
    "v_exp_f32 v180, v172\n"
    "v_mfma_f32_16x16x32_bf16 v[164:167], a[136:139], a[72:75], v[164:167]\n"
    "ds_read_b128 a[168:171], v222 offset:4096\n"
    "v_exp_f32 v181, v173\n"
    "v_mfma_f32_16x16x32_bf16 v[160:163], a[124:127], a[76:79], v[160:163]\n"
    "ds_read_b128 a[156:159], v223 offset:0\n"
    "v_exp_f32 v182, v174\n"
    "v_mfma_f32_16x16x32_bf16 v[164:167], a[140:143], a[76:79], v[164:167]\n"
    "ds_read_b128 a[172:175], v223 offset:4096\n"
    "v_exp_f32 v183, v175\n"
    "v_mfma_f32_16x16x32_bf16 v[64:67], a[80:83], v[224:227], v[64:67]\n"
    "v_add_f32 v184, v176, v177\n"
    "v_add_f32 v185, v178, v179\n"
    "v_mfma_f32_16x16x32_bf16 v[68:71], a[84:87], v[224:227], v[68:71]\n"
    "v_add_f32 v186, v180, v181\n"
    "v_add_f32 v187, v182, v183\n"
    "v_mfma_f32_16x16x32_bf16 v[72:75], a[88:91], v[224:227], v[72:75]\n"
    "v_add_f32 v184, v184, v185\n"
    "v_add_f32 v186, v186, v187\n"
    "s_add_u32 m0, s74, 0\n"
    "s_add_u32 s71, s71, 4096\n"
    "s_and_b32 s71, s71, 0x1ffff\n"
    "s_waitcnt lgkmcnt(0)\n"
    "v_mfma_f32_16x16x32_bf16 v[76:79], a[92:95], v[224:227], v[76:79]\n"
    "buffer_load_dwordx4 v188, s[44:47], s71 offen offset:0 nt lds\n"
    "v_add_f32 v184, v184, v186\n"
    "v_cvt_pk_bf16_f32 v228, v176, v177\n"
    "v_mfma_f32_16x16x32_bf16 v[80:83], a[96:99], v[224:227], v[80:83]\n"
    "buffer_load_dwordx4 v189, s[44:47], s71 offen offset:1024 nt lds\n"
    "v_cvt_pk_bf16_f32 v229, v178, v179\n"
    "v_cmp_lt_f32 vcc, s77, v184\n"
    "v_mfma_f32_16x16x32_bf16 v[84:87], a[100:103], v[224:227], v[84:87]\n"
    "buffer_load_dwordx4 v190, s[44:47], s71 offen offset:2048 nt lds\n"
    "v_cvt_pk_bf16_f32 v230, v180, v181\n"
    "v_cvt_pk_bf16_f32 v231, v182, v183\n"
    "v_mfma_f32_16x16x32_bf16 v[88:91], a[104:107], v[224:227], v[88:91]\n"
    "s_cbranch_vccnz L_rareB_l7%=\n"
    "L_rareB_l7_r%=:\n"
    "v_add_f32 v235, v235, v184\n"
    "v_mfma_f32_16x16x32_bf16 v[92:95], a[108:111], v[224:227], v[92:95]\n"
    "buffer_load_dwordx4 v191, s[44:47], s71 offen offset:3072 nt lds\n"
    "v_mfma_f32_16x16x32_bf16 v[168:171], a[144:147], a[0:3], v[192:195]\n"
    "v_exp_f32 v176, v160\n"
    "s_add_u32 m0, s74, 4096\n"
    "s_add_u32 s71, s71, 4096\n"
    "s_and_b32 s71, s71, 0x1ffff\n"
    "v_mfma_f32_16x16x32_bf16 v[172:175], a[160:163], a[0:3], v[192:195]\n"
    "buffer_load_dwordx4 v188, s[44:47], s71 offen offset:0 nt lds\n"
    "v_exp_f32 v177, v161\n"
    "v_mfma_f32_16x16x32_bf16 v[168:171], a[148:151], a[4:7], v[168:171]\n"
    "v_exp_f32 v178, v162\n"
    "v_mfma_f32_16x16x32_bf16 v[172:175], a[164:167], a[4:7], v[172:175]\n"
    "buffer_load_dwordx4 v189, s[44:47], s71 offen offset:1024 nt lds\n"
    "v_exp_f32 v179, v163\n"
    "v_mfma_f32_16x16x32_bf16 v[168:171], a[152:155], a[8:11], v[168:171]\n"
    "v_exp_f32 v180, v164\n"
    "v_mfma_f32_16x16x32_bf16 v[172:175], a[168:171], a[8:11], v[172:175]\n"
    "buffer_load_dwordx4 v190, s[44:47], s71 offen offset:2048 nt lds\n"
    "v_exp_f32 v181, v165\n"
    "v_mfma_f32_16x16x32_bf16 v[168:171], a[156:159], a[12:15], v[168:171]\n"
    "v_exp_f32 v182, v166\n"
    "v_mfma_f32_16x16x32_bf16 v[172:175], a[172:175], a[12:15], v[172:175]\n"
    "buffer_load_dwordx4 v191, s[44:47], s71 offen offset:3072 nt lds\n"
    "v_exp_f32 v183, v167\n"
    "v_mfma_f32_16x16x32_bf16 v[96:99], a[80:83], v[228:231], v[96:99]\n"
    "v_add_f32 v184, v176, v177\n"
    "v_add_f32 v185, v178, v179\n"
    "v_mfma_f32_16x16x32_bf16 v[100:103], a[84:87], v[228:231], v[100:103]\n"
    "v_add_f32 v186, v180, v181\n"
    "v_add_f32 v187, v182, v183\n"
    "v_mfma_f32_16x16x32_bf16 v[104:107], a[88:91], v[228:231], v[104:107]\n"
    "v_add_f32 v184, v184, v185\n"
    "v_add_f32 v186, v186, v187\n"
    "v_mfma_f32_16x16x32_bf16 v[108:111], a[92:95], v[228:231], v[108:111]\n"
    "v_add_f32 v184, v184, v186\n"
    "v_cvt_pk_bf16_f32 v224, v176, v177\n"
    "v_mfma_f32_16x16x32_bf16 v[112:115], a[96:99], v[228:231], v[112:115]\n"
    "v_cvt_pk_bf16_f32 v225, v178, v179\n"
    "v_cmp_lt_f32 vcc, s77, v184\n"
    "v_mfma_f32_16x16x32_bf16 v[116:119], a[100:103], v[228:231], v[116:119]\n"
    "v_cvt_pk_bf16_f32 v226, v180, v181\n"
    "v_cvt_pk_bf16_f32 v227, v182, v183\n"
    "v_mfma_f32_16x16x32_bf16 v[120:123], a[104:107], v[228:231], v[120:123]\n"
    "s_cbranch_vccnz L_rareB_l8%=\n"
    "L_rareB_l8_r%=:\n"
    "v_add_f32 v236, v236, v184\n"
    "v_mfma_f32_16x16x32_bf16 v[124:127], a[108:111], v[228:231], v[124:127]\n"
    "v_mfma_f32_16x16x32_bf16 v[160:163], a[144:147], a[16:19], v[196:199]\n"
    "v_exp_f32 v176, v168\n"
    "v_mfma_f32_16x16x32_bf16 v[164:167], a[160:163], a[16:19], v[196:199]\n"
    "v_exp_f32 v177, v169\n"
    "v_mfma_f32_16x16x32_bf16 v[160:163], a[148:151], a[20:23], v[160:163]\n"
    "v_exp_f32 v178, v170\n"
    "v_mfma_f32_16x16x32_bf16 v[164:167], a[164:167], a[20:23], v[164:167]\n"
    "v_exp_f32 v179, v171\n"
    "v_mfma_f32_16x16x32_bf16 v[160:163], a[152:155], a[24:27], v[160:163]\n"
    "v_exp_f32 v180, v172\n"
    "v_mfma_f32_16x16x32_bf16 v[164:167], a[168:171], a[24:27], v[164:167]\n"
    "v_exp_f32 v181, v173\n"
    "v_mfma_f32_16x16x32_bf16 v[160:163], a[156:159], a[28:31], v[160:163]\n"
    "v_exp_f32 v182, v174\n"
    "v_mfma_f32_16x16x32_bf16 v[164:167], a[172:175], a[28:31], v[164:167]\n"
    "v_exp_f32 v183, v175\n"
    "v_mfma_f32_16x16x32_bf16 v[128:131], a[80:83], v[224:227], v[128:131]\n"
    "s_waitcnt vmcnt(12)\n"
    "ds_read_b64_tr_b16 a[80:81], v212 offset:32768\n"
    "v_add_f32 v184, v176, v177\n"
    "v_add_f32 v185, v178, v179\n"
    "v_mfma_f32_16x16x32_bf16 v[132:135], a[84:87], v[224:227], v[132:135]\n"
    "s_waitcnt vmcnt(8)\n"
    "ds_read_b64_tr_b16 a[82:83], v212 offset:36864\n"
    "v_add_f32 v186, v180, v181\n"
    "v_add_f32 v187, v182, v183\n"
    "v_mfma_f32_16x16x32_bf16 v[136:139], a[88:91], v[224:227], v[136:139]\n"
    "ds_read_b64_tr_b16 a[84:85], v213 offset:32768\n"
    "v_add_f32 v184, v184, v185\n"
    "v_add_f32 v186, v186, v187\n"
    "v_mfma_f32_16x16x32_bf16 v[140:143], a[92:95], v[224:227], v[140:143]\n"
    "ds_read_b64_tr_b16 a[86:87], v213 offset:36864\n"
    "v_add_f32 v184, v184, v186\n"
    "v_cvt_pk_bf16_f32 v228, v176, v177\n"
    "v_mfma_f32_16x16x32_bf16 v[144:147], a[96:99], v[224:227], v[144:147]\n"
    "ds_read_b64_tr_b16 a[88:89], v214 offset:32768\n"
    "v_cvt_pk_bf16_f32 v229, v178, v179\n"
    "v_cmp_lt_f32 vcc, s77, v184\n"
    "v_mfma_f32_16x16x32_bf16 v[148:151], a[100:103], v[224:227], v[148:151]\n"
    "ds_read_b64_tr_b16 a[90:91], v214 offset:36864\n"
    "v_cvt_pk_bf16_f32 v230, v180, v181\n"
    "v_cvt_pk_bf16_f32 v231, v182, v183\n"
    "v_mfma_f32_16x16x32_bf16 v[152:155], a[104:107], v[224:227], v[152:155]\n"
    "ds_read_b64_tr_b16 a[92:93], v215 offset:32768\n"
    "s_cbranch_vccnz L_rareB_l9%=\n"
    "L_rareB_l9_r%=:\n"
    "v_add_f32 v232, v232, v184\n"
    "s_sub_u32 s82, s82, 1\n"
    "v_mfma_f32_16x16x32_bf16 v[156:159], a[108:111], v[224:227], v[156:159]\n"
    "ds_read_b64_tr_b16 a[94:95], v215 offset:36864\n"
    "s_bitcmp1_b32 8, s82\n"
    "s_cbranch_scc1 L_attBt%=\n"
    "s_bitcmp1_b32 20, s82\n"
    "s_cbranch_scc1 L_attBu%=\n"
    "s_cmp_lg_u32 s82, 0\n"
    "s_cbranch_scc1 L_attB%=\n"
    "s_branch L_attxB%=\n"
    "L_attBu%=:\n"
    "v_mfma_f32_16x16x32_bf16 v[168:171], a[144:147], a[32:35], v[200:203]\n"
    "ds_read_b64_tr_b16 a[96:97], v216 offset:32768\n"
    "v_exp_f32 v176, v160\n"
    "v_mfma_f32_16x16x32_bf16 v[172:175], a[160:163], a[32:35], v[200:203]\n"
    "ds_read_b64_tr_b16 a[98:99], v216 offset:36864\n"
    "v_exp_f32 v177, v161\n"
    "v_mfma_f32_16x16x32_bf16 v[168:171], a[148:151], a[36:39], v[168:171]\n"
    "ds_read_b64_tr_b16 a[100:101], v217 offset:32768\n"
    "v_exp_f32 v178, v162\n"
    "v_mfma_f32_16x16x32_bf16 v[172:175], a[164:167], a[36:39], v[172:175]\n"
    "ds_read_b64_tr_b16 a[102:103], v217 offset:36864\n"
    "v_exp_f32 v179, v163\n"
    "v_mfma_f32_16x16x32_bf16 v[168:171], a[152:155], a[40:43], v[168:171]\n"
    "ds_read_b64_tr_b16 a[104:105], v218 offset:32768\n"
    "v_exp_f32 v180, v164\n"
    "v_mfma_f32_16x16x32_bf16 v[172:175], a[168:171], a[40:43], v[172:175]\n"
    "ds_read_b64_tr_b16 a[106:107], v218 offset:36864\n"
    "v_exp_f32 v181, v165\n"
    "v_mfma_f32_16x16x32_bf16 v[168:171], a[156:159], a[44:47], v[168:171]\n"
    "ds_read_b64_tr_b16 a[108:109], v219 offset:32768\n"
    "v_exp_f32 v182, v166\n"
    "v_mfma_f32_16x16x32_bf16 v[172:175], a[172:175], a[44:47], v[172:175]\n"
    "ds_read_b64_tr_b16 a[110:111], v219 offset:36864\n"
    "v_exp_f32 v183, v167\n"
    "s_waitcnt lgkmcnt(4)\n"
    "v_mfma_f32_16x16x32_bf16 v[0:3], a[80:83], v[228:231], v[0:3]\n"
    "v_add_f32 v184, v176, v177\n"
    "v_add_f32 v185, v178, v179\n"
    "s_add_u32 m0, s87, 0\n"
    "s_add_u32 s85, s85, 4096\n"
    "s_and_b32 s85, s85, 0x1ffff\n"
    "v_mfma_f32_16x16x32_bf16 v[4:7], a[84:87], v[228:231], v[4:7]\n"
    "s_waitcnt vmcnt(7)\n"
    "buffer_load_dwordx4 v242, s[48:51], s85 offen offset:0 nt lds\n"
    "v_add_f32 v186, v180, v181\n"
    "v_add_f32 v187, v182, v183\n"
    "v_mfma_f32_16x16x32_bf16 v[8:11], a[88:91], v[228:231], v[8:11]\n"
    "v_add_f32 v184, v184, v185\n"
    "v_add_f32 v186, v186, v187\n"
    "v_mfma_f32_16x16x32_bf16 v[12:15], a[92:95], v[228:231], v[12:15]\n"
    "s_waitcnt vmcnt(7)\n"
    "buffer_load_dwordx4 v243, s[48:51], s85 offen offset:1024 nt lds\n"
    "v_add_f32 v184, v184, v186\n"
    "v_cvt_pk_bf16_f32 v224, v176, v177\n"
    "v_mfma_f32_16x16x32_bf16 v[16:19], a[96:99], v[228:231], v[16:19]\n"
    "v_cvt_pk_bf16_f32 v225, v178, v179\n"
    "v_cmp_lt_f32 vcc, s77, v184\n"
    "v_mfma_f32_16x16x32_bf16 v[20:23], a[100:103], v[228:231], v[20:23]\n"
    "s_waitcnt vmcnt(7)\n"
    "buffer_load_dwordx4 v242, s[48:51], s85 offen offset:2048 nt lds\n"
    "v_cvt_pk_bf16_f32 v226, v180, v181\n"
    "v_cvt_pk_bf16_f32 v227, v182, v183\n"
    "s_waitcnt lgkmcnt(2)\n"
    "v_mfma_f32_16x16x32_bf16 v[24:27], a[104:107], v[228:231], v[24:27]\n"
    "s_cbranch_vccnz L_rareBu_l0%=\n"
    "L_rareBu_l0_r%=:\n"
    "v_add_f32 v233, v233, v184\n"
    "s_waitcnt lgkmcnt(0)\n"
    "v_mfma_f32_16x16x32_bf16 v[28:31], a[108:111], v[228:231], v[28:31]\n"
    "s_waitcnt vmcnt(7)\n"
    "buffer_load_dwordx4 v243, s[48:51], s85 offen offset:3072 nt lds\n"
    "v_mfma_f32_16x16x32_bf16 v[160:163], a[144:147], a[48:51], v[204:207]\n"
    "v_exp_f32 v176, v168\n"
    "s_add_u32 m0, s87, 4096\n"
    "s_add_u32 s85, s85, 4096\n"
    "s_and_b32 s85, s85, 0x1ffff\n"
    "v_mfma_f32_16x16x32_bf16 v[164:167], a[160:163], a[48:51], v[204:207]\n"
    "s_waitcnt vmcnt(7)\n"
    "buffer_load_dwordx4 v242, s[48:51], s85 offen offset:0 nt lds\n"
    "v_exp_f32 v177, v169\n"
    "v_mfma_f32_16x16x32_bf16 v[160:163], a[148:151], a[52:55], v[160:163]\n"
    "v_exp_f32 v178, v170\n"
    "v_mfma_f32_16x16x32_bf16 v[164:167], a[164:167], a[52:55], v[164:167]\n"
    "s_waitcnt vmcnt(7)\n"
    "buffer_load_dwordx4 v243, s[48:51], s85 offen offset:1024 nt lds\n"
    "v_exp_f32 v179, v171\n"
    "v_mfma_f32_16x16x32_bf16 v[160:163], a[152:155], a[56:59], v[160:163]\n"
    "v_exp_f32 v180, v172\n"
    "v_mfma_f32_16x16x32_bf16 v[164:167], a[168:171], a[56:59], v[164:167]\n"
    "s_waitcnt vmcnt(7)\n"
    "buffer_load_dwordx4 v242, s[48:51], s85 offen offset:2048 nt lds\n"
    "v_exp_f32 v181, v173\n"
    "v_mfma_f32_16x16x32_bf16 v[160:163], a[156:159], a[60:63], v[160:163]\n"
    "v_exp_f32 v182, v174\n"
    "v_mfma_f32_16x16x32_bf16 v[164:167], a[172:175], a[60:63], v[164:167]\n"
    "s_waitcnt vmcnt(7)\n"
    "buffer_load_dwordx4 v243, s[48:51], s85 offen offset:3072 nt lds\n"
    "v_exp_f32 v183, v175\n"
    "v_mfma_f32_16x16x32_bf16 v[32:35], a[80:83], v[224:227], v[32:35]\n"
    "v_add_f32 v184, v176, v177\n"
    "v_add_f32 v185, v178, v179\n"
    "s_barrier\n"
    "v_mfma_f32_16x16x32_bf16 v[36:39], a[84:87], v[224:227], v[36:39]\n"
    "v_add_f32 v186, v180, v181\n"
    "v_add_f32 v187, v182, v183\n"
    "v_mfma_f32_16x16x32_bf16 v[40:43], a[88:91], v[224:227], v[40:43]\n"
    "v_add_f32 v184, v184, v185\n"
    "v_add_f32 v186, v186, v187\n"
    "v_mfma_f32_16x16x32_bf16 v[44:47], a[92:95], v[224:227], v[44:47]\n"
    "v_add_f32 v184, v184, v186\n"
    "v_cvt_pk_bf16_f32 v228, v176, v177\n"
    "v_mfma_f32_16x16x32_bf16 v[48:51], a[96:99], v[224:227], v[48:51]\n"
    "v_cvt_pk_bf16_f32 v229, v178, v179\n"
    "v_cmp_lt_f32 vcc, s77, v184\n"
    "v_mfma_f32_16x16x32_bf16 v[52:55], a[100:103], v[224:227], v[52:55]\n"
    "v_cvt_pk_bf16_f32 v230, v180, v181\n"
    "v_cvt_pk_bf16_f32 v231, v182, v183\n"
    "v_mfma_f32_16x16x32_bf16 v[56:59], a[104:107], v[224:227], v[56:59]\n"
    "s_cbranch_vccnz L_rareBu_l1%=\n"
    "L_rareBu_l1_r%=:\n"
    "v_add_f32 v234, v234, v184\n"
    "v_mfma_f32_16x16x32_bf16 v[60:63], a[108:111], v[224:227], v[60:63]\n"
    "v_mfma_f32_16x16x32_bf16 v[168:171], a[144:147], a[64:67], v[208:211]\n"
    "ds_read_b128 a[112:115], v220 offset:0\n"
    "v_exp_f32 v176, v160\n"
    "v_mfma_f32_16x16x32_bf16 v[172:175], a[160:163], a[64:67], v[208:211]\n"
    "ds_read_b128 a[128:131], v220 offset:4096\n"
    "v_exp_f32 v177, v161\n"
    "v_mfma_f32_16x16x32_bf16 v[168:171], a[148:151], a[68:71], v[168:171]\n"
    "ds_read_b128 a[116:119], v221 offset:0\n"
    "v_exp_f32 v178, v162\n"
    "v_mfma_f32_16x16x32_bf16 v[172:175], a[164:167], a[68:71], v[172:175]\n"
    "ds_read_b128 a[132:135], v221 offset:4096\n"
    "v_exp_f32 v179, v163\n"
    "v_mfma_f32_16x16x32_bf16 v[168:171], a[152:155], a[72:75], v[168:171]\n"
    "ds_read_b128 a[120:123], v222 offset:0\n"
    "v_exp_f32 v180, v164\n"
    "v_mfma_f32_16x16x32_bf16 v[172:175], a[168:171], a[72:75], v[172:175]\n"
    "ds_read_b128 a[136:139], v222 offset:4096\n"
    "v_exp_f32 v181, v165\n"
    "v_mfma_f32_16x16x32_bf16 v[168:171], a[156:159], a[76:79], v[168:171]\n"
    "ds_read_b128 a[124:127], v223 offset:0\n"
    "v_exp_f32 v182, v166\n"
    "v_mfma_f32_16x16x32_bf16 v[172:175], a[172:175], a[76:79], v[172:175]\n"
    "ds_read_b128 a[140:143], v223 offset:4096\n"
    "v_exp_f32 v183, v167\n"
    "v_mfma_f32_16x16x32_bf16 v[64:67], a[80:83], v[228:231], v[64:67]\n"
    "v_add_f32 v184, v176, v177\n"
    "v_add_f32 v185, v178, v179\n"
    "v_mfma_f32_16x16x32_bf16 v[68:71], a[84:87], v[228:231], v[68:71]\n"
    "v_add_f32 v186, v180, v181\n"
    "v_add_f32 v187, v182, v183\n"
    "v_mfma_f32_16x16x32_bf16 v[72:75], a[88:91], v[228:231], v[72:75]\n"
    "v_add_f32 v184, v184, v185\n"
    "v_add_f32 v186, v186, v187\n"
    "s_add_u32 m0, s74, 0\n"
    "s_add_u32 s71, s71, 4096\n"
    "s_and_b32 s71, s71, 0x1ffff\n"
    "s_waitcnt lgkmcnt(0)\n"
    "v_mfma_f32_16x16x32_bf16 v[76:79], a[92:95], v[228:231], v[76:79]\n"
    "s_waitcnt vmcnt(7)\n"
    "buffer_load_dwordx4 v188, s[44:47], s71 offen offset:0 nt lds\n"
    "v_add_f32 v184, v184, v186\n"
    "v_cvt_pk_bf16_f32 v224, v176, v177\n"
    "v_mfma_f32_16x16x32_bf16 v[80:83], a[96:99], v[228:231], v[80:83]\n"
    "s_waitcnt vmcnt(7)\n"
    "buffer_load_dwordx4 v189, s[44:47], s71 offen offset:1024 nt lds\n"
    "v_cvt_pk_bf16_f32 v225, v178, v179\n"
    "v_cmp_lt_f32 vcc, s77, v184\n"
    "v_mfma_f32_16x16x32_bf16 v[84:87], a[100:103], v[228:231], v[84:87]\n"
    "s_waitcnt vmcnt(7)\n"
    "buffer_load_dwordx4 v190, s[44:47], s71 offen offset:2048 nt lds\n"
    "v_cvt_pk_bf16_f32 v226, v180, v181\n"
    "v_cvt_pk_bf16_f32 v227, v182, v183\n"
    "v_mfma_f32_16x16x32_bf16 v[88:91], a[104:107], v[228:231], v[88:91]\n"
    "s_cbranch_vccnz L_rareBu_l2%=\n"
    "L_rareBu_l2_r%=:\n"
    "v_add_f32 v235, v235, v184\n"
    "v_mfma_f32_16x16x32_bf16 v[92:95], a[108:111], v[228:231], v[92:95]\n"
    "s_waitcnt vmcnt(7)\n"
    "buffer_load_dwordx4 v191, s[44:47], s71 offen offset:3072 nt lds\n"
    "v_mfma_f32_16x16x32_bf16 v[160:163], a[112:115], a[0:3], v[192:195]\n"
    "v_exp_f32 v176, v168\n"
    "s_add_u32 m0, s74, 4096\n"
    "s_add_u32 s71, s71, 4096\n"
    "s_and_b32 s71, s71, 0x1ffff\n"
    "v_mfma_f32_16x16x32_bf16 v[164:167], a[128:131], a[0:3], v[192:195]\n"
    "s_waitcnt vmcnt(7)\n"
    "buffer_load_dwordx4 v188, s[44:47], s71 offen offset:0 nt lds\n"
    "v_exp_f32 v177, v169\n"
    "v_mfma_f32_16x16x32_bf16 v[160:163], a[116:119], a[4:7], v[160:163]\n"
    "v_exp_f32 v178, v170\n"
    "v_mfma_f32_16x16x32_bf16 v[164:167], a[132:135], a[4:7], v[164:167]\n"
    "s_waitcnt vmcnt(7)\n"
    "buffer_load_dwordx4 v189, s[44:47], s71 offen offset:1024 nt lds\n"
    "v_exp_f32 v179, v171\n"
    "v_mfma_f32_16x16x32_bf16 v[160:163], a[120:123], a[8:11], v[160:163]\n"
    "v_exp_f32 v180, v172\n"
    "v_mfma_f32_16x16x32_bf16 v[164:167], a[136:139], a[8:11], v[164:167]\n"
    "s_waitcnt vmcnt(7)\n"
    "buffer_load_dwordx4 v190, s[44:47], s71 offen offset:2048 nt lds\n"
    "v_exp_f32 v181, v173\n"
    "v_mfma_f32_16x16x32_bf16 v[160:163], a[124:127], a[12:15], v[160:163]\n"
    "v_exp_f32 v182, v174\n"
    "v_mfma_f32_16x16x32_bf16 v[164:167], a[140:143], a[12:15], v[164:167]\n"
    "s_waitcnt vmcnt(7)\n"
    "buffer_load_dwordx4 v191, s[44:47], s71 offen offset:3072 nt lds\n"
    "v_exp_f32 v183, v175\n"
    "v_mfma_f32_16x16x32_bf16 v[96:99], a[80:83], v[224:227], v[96:99]\n"
    "v_add_f32 v184, v176, v177\n"
    "v_add_f32 v185, v178, v179\n"
    "v_mfma_f32_16x16x32_bf16 v[100:103], a[84:87], v[224:227], v[100:103]\n"
    "v_add_f32 v186, v180, v181\n"
    "v_add_f32 v187, v182, v183\n"
    "v_mfma_f32_16x16x32_bf16 v[104:107], a[88:91], v[224:227], v[104:107]\n"
    "v_add_f32 v184, v184, v185\n"
    "v_add_f32 v186, v186, v187\n"
    "v_mfma_f32_16x16x32_bf16 v[108:111], a[92:95], v[224:227], v[108:111]\n"
    "v_add_f32 v184, v184, v186\n"
    "v_cvt_pk_bf16_f32 v228, v176, v177\n"
    "v_mfma_f32_16x16x32_bf16 v[112:115], a[96:99], v[224:227], v[112:115]\n"
    "v_cvt_pk_bf16_f32 v229, v178, v179\n"
    "v_cmp_lt_f32 vcc, s77, v184\n"
    "v_mfma_f32_16x16x32_bf16 v[116:119], a[100:103], v[224:227], v[116:119]\n"
    "v_cvt_pk_bf16_f32 v230, v180, v181\n"
    "v_cvt_pk_bf16_f32 v231, v182, v183\n"
    "v_mfma_f32_16x16x32_bf16 v[120:123], a[104:107], v[224:227], v[120:123]\n"
    "s_cbranch_vccnz L_rareBu_l3%=\n"
    "L_rareBu_l3_r%=:\n"
    "v_add_f32 v236, v236, v184\n"
    "v_mfma_f32_16x16x32_bf16 v[124:127], a[108:111], v[224:227], v[124:127]\n"
    "v_mfma_f32_16x16x32_bf16 v[168:171], a[112:115], a[16:19], v[196:199]\n"
    "v_exp_f32 v176, v160\n"
    "v_mfma_f32_16x16x32_bf16 v[172:175], a[128:131], a[16:19], v[196:199]\n"
    "v_exp_f32 v177, v161\n"
    "v_mfma_f32_16x16x32_bf16 v[168:171], a[116:119], a[20:23], v[168:171]\n"
    "v_exp_f32 v178, v162\n"
    "v_mfma_f32_16x16x32_bf16 v[172:175], a[132:135], a[20:23], v[172:175]\n"
    "v_exp_f32 v179, v163\n"
    "v_mfma_f32_16x16x32_bf16 v[168:171], a[120:123], a[24:27], v[168:171]\n"
    "v_exp_f32 v180, v164\n"
    "v_mfma_f32_16x16x32_bf16 v[172:175], a[136:139], a[24:27], v[172:175]\n"
    "v_exp_f32 v181, v165\n"
    "v_mfma_f32_16x16x32_bf16 v[168:171], a[124:127], a[28:31], v[168:171]\n"
    "v_exp_f32 v182, v166\n"
    "v_mfma_f32_16x16x32_bf16 v[172:175], a[140:143], a[28:31], v[172:175]\n"
    "v_exp_f32 v183, v167\n"
    "v_mfma_f32_16x16x32_bf16 v[128:131], a[80:83], v[228:231], v[128:131]\n"
    "ds_read_b64_tr_b16 a[80:81], v212 offset:0\n"
    "v_add_f32 v184, v176, v177\n"
    "v_add_f32 v185, v178, v179\n"
    "v_mfma_f32_16x16x32_bf16 v[132:135], a[84:87], v[228:231], v[132:135]\n"
    "ds_read_b64_tr_b16 a[82:83], v212 offset:4096\n"
    "v_add_f32 v186, v180, v181\n"
    "v_add_f32 v187, v182, v183\n"
    "v_mfma_f32_16x16x32_bf16 v[136:139], a[88:91], v[228:231], v[136:139]\n"
    "ds_read_b64_tr_b16 a[84:85], v213 offset:0\n"
    "v_add_f32 v184, v184, v185\n"
    "v_add_f32 v186, v186, v187\n"
    "v_mfma_f32_16x16x32_bf16 v[140:143], a[92:95], v[228:231], v[140:143]\n"
    "ds_read_b64_tr_b16 a[86:87], v213 offset:4096\n"
    "v_add_f32 v184, v184, v186\n"
    "v_cvt_pk_bf16_f32 v224, v176, v177\n"
    "v_mfma_f32_16x16x32_bf16 v[144:147], a[96:99], v[228:231], v[144:147]\n"
    "ds_read_b64_tr_b16 a[88:89], v214 offset:0\n"
    "v_cvt_pk_bf16_f32 v225, v178, v179\n"
    "v_cmp_lt_f32 vcc, s77, v184\n"
    "v_mfma_f32_16x16x32_bf16 v[148:151], a[100:103], v[228:231], v[148:151]\n"
    "ds_read_b64_tr_b16 a[90:91], v214 offset:4096\n"
    "v_cvt_pk_bf16_f32 v226, v180, v181\n"
    "v_cvt_pk_bf16_f32 v227, v182, v183\n"
    "v_mfma_f32_16x16x32_bf16 v[152:155], a[104:107], v[228:231], v[152:155]\n"
    "ds_read_b64_tr_b16 a[92:93], v215 offset:0\n"
    "s_cbranch_vccnz L_rareBu_l4%=\n"
    "L_rareBu_l4_r%=:\n"
    "v_add_f32 v232, v232, v184\n"
    "v_mfma_f32_16x16x32_bf16 v[156:159], a[108:111], v[228:231], v[156:159]\n"
    "ds_read_b64_tr_b16 a[94:95], v215 offset:4096\n"
    "v_mfma_f32_16x16x32_bf16 v[160:163], a[112:115], a[32:35], v[200:203]\n"
    "ds_read_b64_tr_b16 a[96:97], v216 offset:0\n"
    "v_exp_f32 v176, v168\n"
    "v_mfma_f32_16x16x32_bf16 v[164:167], a[128:131], a[32:35], v[200:203]\n"
    "ds_read_b64_tr_b16 a[98:99], v216 offset:4096\n"
    "v_exp_f32 v177, v169\n"
    "v_mfma_f32_16x16x32_bf16 v[160:163], a[116:119], a[36:39], v[160:163]\n"
    "ds_read_b64_tr_b16 a[100:101], v217 offset:0\n"
    "v_exp_f32 v178, v170\n"
    "v_mfma_f32_16x16x32_bf16 v[164:167], a[132:135], a[36:39], v[164:167]\n"
    "ds_read_b64_tr_b16 a[102:103], v217 offset:4096\n"
    "v_exp_f32 v179, v171\n"
    "v_mfma_f32_16x16x32_bf16 v[160:163], a[120:123], a[40:43], v[160:163]\n"
    "ds_read_b64_tr_b16 a[104:105], v218 offset:0\n"
    "v_exp_f32 v180, v172\n"
    "v_mfma_f32_16x16x32_bf16 v[164:167], a[136:139], a[40:43], v[164:167]\n"
    "ds_read_b64_tr_b16 a[106:107], v218 offset:4096\n"
    "v_exp_f32 v181, v173\n"
    "v_mfma_f32_16x16x32_bf16 v[160:163], a[124:127], a[44:47], v[160:163]\n"
    "ds_read_b64_tr_b16 a[108:109], v219 offset:0\n"
    "v_exp_f32 v182, v174\n"
    "v_mfma_f32_16x16x32_bf16 v[164:167], a[140:143], a[44:47], v[164:167]\n"
    "ds_read_b64_tr_b16 a[110:111], v219 offset:4096\n"
    "v_exp_f32 v183, v175\n"
    "s_waitcnt lgkmcnt(4)\n"
    "v_mfma_f32_16x16x32_bf16 v[0:3], a[80:83], v[224:227], v[0:3]\n"
    "v_add_f32 v184, v176, v177\n"
    "v_add_f32 v185, v178, v179\n"
    "s_add_u32 m0, s87, 32768\n"
    "s_add_u32 s85, s85, 4096\n"
    "s_and_b32 s85, s85, 0x1ffff\n"
    "v_mfma_f32_16x16x32_bf16 v[4:7], a[84:87], v[224:227], v[4:7]\n"
    "s_waitcnt vmcnt(7)\n"
    "buffer_load_dwordx4 v242, s[48:51], s85 offen offset:0 nt lds\n"
    "v_add_f32 v186, v180, v181\n"
    "v_add_f32 v187, v182, v183\n"
    "v_mfma_f32_16x16x32_bf16 v[8:11], a[88:91], v[224:227], v[8:11]\n"
    "v_add_f32 v184, v184, v185\n"
    "v_add_f32 v186, v186, v187\n"
    "v_mfma_f32_16x16x32_bf16 v[12:15], a[92:95], v[224:227], v[12:15]\n"
    "s_waitcnt vmcnt(7)\n"
    "buffer_load_dwordx4 v243, s[48:51], s85 offen offset:1024 nt lds\n"
    "v_add_f32 v184, v184, v186\n"
    "v_cvt_pk_bf16_f32 v228, v176, v177\n"
    "v_mfma_f32_16x16x32_bf16 v[16:19], a[96:99], v[224:227], v[16:19]\n"
    "v_cvt_pk_bf16_f32 v229, v178, v179\n"
    "v_cmp_lt_f32 vcc, s77, v184\n"
    "v_mfma_f32_16x16x32_bf16 v[20:23], a[100:103], v[224:227], v[20:23]\n"
    "s_waitcnt vmcnt(7)\n"
    "buffer_load_dwordx4 v242, s[48:51], s85 offen offset:2048 nt lds\n"
    "v_cvt_pk_bf16_f32 v230, v180, v181\n"
    "v_cvt_pk_bf16_f32 v231, v182, v183\n"
    "s_waitcnt lgkmcnt(2)\n"
    "v_mfma_f32_16x16x32_bf16 v[24:27], a[104:107], v[224:227], v[24:27]\n"
    "s_cbranch_vccnz L_rareBu_l5%=\n"
    "L_rareBu_l5_r%=:\n"
    "v_add_f32 v233, v233, v184\n"
    "s_waitcnt lgkmcnt(0)\n"
    "v_mfma_f32_16x16x32_bf16 v[28:31], a[108:111], v[224:227], v[28:31]\n"
    "s_waitcnt vmcnt(7)\n"
    "buffer_load_dwordx4 v243, s[48:51], s85 offen offset:3072 nt lds\n"
    "v_mfma_f32_16x16x32_bf16 v[168:171], a[112:115], a[48:51], v[204:207]\n"
    "v_exp_f32 v176, v160\n"
    "s_add_u32 m0, s87, 36864\n"
    "s_add_u32 s85, s85, 4096\n"
    "s_and_b32 s85, s85, 0x1ffff\n"
    "v_mfma_f32_16x16x32_bf16 v[172:175], a[128:131], a[48:51], v[204:207]\n"
    "s_waitcnt vmcnt(7)\n"
    "buffer_load_dwordx4 v242, s[48:51], s85 offen offset:0 nt lds\n"
    "v_exp_f32 v177, v161\n"
    "v_mfma_f32_16x16x32_bf16 v[168:171], a[116:119], a[52:55], v[168:171]\n"
    "v_exp_f32 v178, v162\n"
    "v_mfma_f32_16x16x32_bf16 v[172:175], a[132:135], a[52:55], v[172:175]\n"
    "s_waitcnt vmcnt(7)\n"
    "buffer_load_dwordx4 v243, s[48:51], s85 offen offset:1024 nt lds\n"
    "v_exp_f32 v179, v163\n"
    "v_mfma_f32_16x16x32_bf16 v[168:171], a[120:123], a[56:59], v[168:171]\n"
    "v_exp_f32 v180, v164\n"
    "v_mfma_f32_16x16x32_bf16 v[172:175], a[136:139], a[56:59], v[172:175]\n"
    "s_waitcnt vmcnt(7)\n"
    "buffer_load_dwordx4 v242, s[48:51], s85 offen offset:2048 nt lds\n"
    "v_exp_f32 v181, v165\n"
    "v_mfma_f32_16x16x32_bf16 v[168:171], a[124:127], a[60:63], v[168:171]\n"
    "v_exp_f32 v182, v166\n"
    "v_mfma_f32_16x16x32_bf16 v[172:175], a[140:143], a[60:63], v[172:175]\n"
    "s_waitcnt vmcnt(7)\n"
    "buffer_load_dwordx4 v243, s[48:51], s85 offen offset:3072 nt lds\n"
    "v_exp_f32 v183, v167\n"
    "v_mfma_f32_16x16x32_bf16 v[32:35], a[80:83], v[228:231], v[32:35]\n"
    "v_add_f32 v184, v176, v177\n"
    "v_add_f32 v185, v178, v179\n"
    "v_mfma_f32_16x16x32_bf16 v[36:39], a[84:87], v[228:231], v[36:39]\n"
    "v_add_f32 v186, v180, v181\n"
    "v_add_f32 v187, v182, v183\n"
    "v_mfma_f32_16x16x32_bf16 v[40:43], a[88:91], v[228:231], v[40:43]\n"
    "v_add_f32 v184, v184, v185\n"
    "v_add_f32 v186, v186, v187\n"
    "v_mfma_f32_16x16x32_bf16 v[44:47], a[92:95], v[228:231], v[44:47]\n"
    "v_add_f32 v184, v184, v186\n"
    "v_cvt_pk_bf16_f32 v224, v176, v177\n"
    "v_mfma_f32_16x16x32_bf16 v[48:51], a[96:99], v[228:231], v[48:51]\n"
    "v_cvt_pk_bf16_f32 v225, v178, v179\n"
    "v_cmp_lt_f32 vcc, s77, v184\n"
    "v_mfma_f32_16x16x32_bf16 v[52:55], a[100:103], v[228:231], v[52:55]\n"
    "v_cvt_pk_bf16_f32 v226, v180, v181\n"
    "v_cvt_pk_bf16_f32 v227, v182, v183\n"
    "v_mfma_f32_16x16x32_bf16 v[56:59], a[104:107], v[228:231], v[56:59]\n"
    "s_cbranch_vccnz L_rareBu_l6%=\n"
    "L_rareBu_l6_r%=:\n"
    "v_add_f32 v234, v234, v184\n"
    "v_mfma_f32_16x16x32_bf16 v[60:63], a[108:111], v[228:231], v[60:63]\n"
    "v_mfma_f32_16x16x32_bf16 v[160:163], a[112:115], a[64:67], v[208:211]\n"
    "ds_read_b128 a[144:147], v220 offset:0\n"
    "v_exp_f32 v176, v168\n"
    "v_mfma_f32_16x16x32_bf16 v[164:167], a[128:131], a[64:67], v[208:211]\n"
    "ds_read_b128 a[160:163], v220 offset:4096\n"
    "v_exp_f32 v177, v169\n"
    "v_mfma_f32_16x16x32_bf16 v[160:163], a[116:119], a[68:71], v[160:163]\n"
    "ds_read_b128 a[148:151], v221 offset:0\n"
    "v_exp_f32 v178, v170\n"
    "v_mfma_f32_16x16x32_bf16 v[164:167], a[132:135], a[68:71], v[164:167]\n"
    "ds_read_b128 a[164:167], v221 offset:4096\n"
    "v_exp_f32 v179, v171\n"
    "v_mfma_f32_16x16x32_bf16 v[160:163], a[120:123], a[72:75], v[160:163]\n"
    "ds_read_b128 a[152:155], v222 offset:0\n"
    "v_exp_f32 v180, v172\n"
    "v_mfma_f32_16x16x32_bf16 v[164:167], a[136:139], a[72:75], v[164:167]\n"
    "ds_read_b128 a[168:171], v222 offset:4096\n"
    "v_exp_f32 v181, v173\n"
    "v_mfma_f32_16x16x32_bf16 v[160:163], a[124:127], a[76:79], v[160:163]\n"
    "ds_read_b128 a[156:159], v223 offset:0\n"
    "v_exp_f32 v182, v174\n"
    "v_mfma_f32_16x16x32_bf16 v[164:167], a[140:143], a[76:79], v[164:167]\n"
    "ds_read_b128 a[172:175], v223 offset:4096\n"
    "v_exp_f32 v183, v175\n"
    "v_mfma_f32_16x16x32_bf16 v[64:67], a[80:83], v[224:227], v[64:67]\n"
    "v_add_f32 v184, v176, v177\n"
    "v_add_f32 v185, v178, v179\n"
    "v_mfma_f32_16x16x32_bf16 v[68:71], a[84:87], v[224:227], v[68:71]\n"
    "v_add_f32 v186, v180, v181\n"
    "v_add_f32 v187, v182, v183\n"
    "v_mfma_f32_16x16x32_bf16 v[72:75], a[88:91], v[224:227], v[72:75]\n"
    "v_add_f32 v184, v184, v185\n"
    "v_add_f32 v186, v186, v187\n"
    "s_add_u32 m0, s74, 0\n"
    "s_add_u32 s71, s71, 4096\n"
    "s_and_b32 s71, s71, 0x1ffff\n"
    "s_waitcnt lgkmcnt(0)\n"
    "v_mfma_f32_16x16x32_bf16 v[76:79], a[92:95], v[224:227], v[76:79]\n"
    "s_waitcnt vmcnt(7)\n"
    "buffer_load_dwordx4 v188, s[44:47], s71 offen offset:0 nt lds\n"
    "v_add_f32 v184, v184, v186\n"
    "v_cvt_pk_bf16_f32 v228, v176, v177\n"
    "v_mfma_f32_16x16x32_bf16 v[80:83], a[96:99], v[224:227], v[80:83]\n"
    "s_waitcnt vmcnt(7)\n"
    "buffer_load_dwordx4 v189, s[44:47], s71 offen offset:1024 nt lds\n"
    "v_cvt_pk_bf16_f32 v229, v178, v179\n"
    "v_cmp_lt_f32 vcc, s77, v184\n"
    "v_mfma_f32_16x16x32_bf16 v[84:87], a[100:103], v[224:227], v[84:87]\n"
    "s_waitcnt vmcnt(7)\n"
    "buffer_load_dwordx4 v190, s[44:47], s71 offen offset:2048 nt lds\n"
    "v_cvt_pk_bf16_f32 v230, v180, v181\n"
    "v_cvt_pk_bf16_f32 v231, v182, v183\n"
    "v_mfma_f32_16x16x32_bf16 v[88:91], a[104:107], v[224:227], v[88:91]\n"
    "s_cbranch_vccnz L_rareBu_l7%=\n"
    "L_rareBu_l7_r%=:\n"
    "v_add_f32 v235, v235, v184\n"
    "v_mfma_f32_16x16x32_bf16 v[92:95], a[108:111], v[224:227], v[92:95]\n"
    "s_waitcnt vmcnt(7)\n"
    "buffer_load_dwordx4 v191, s[44:47], s71 offen offset:3072 nt lds\n"
    "v_mfma_f32_16x16x32_bf16 v[168:171], a[144:147], a[0:3], v[192:195]\n"
    "v_exp_f32 v176, v160\n"
    "s_add_u32 m0, s74, 4096\n"
    "s_add_u32 s71, s71, 4096\n"
    "s_and_b32 s71, s71, 0x1ffff\n"
    "v_mfma_f32_16x16x32_bf16 v[172:175], a[160:163], a[0:3], v[192:195]\n"
    "s_waitcnt vmcnt(7)\n"
    "buffer_load_dwordx4 v188, s[44:47], s71 offen offset:0 nt lds\n"
    "v_exp_f32 v177, v161\n"
    "v_mfma_f32_16x16x32_bf16 v[168:171], a[148:151], a[4:7], v[168:171]\n"
    "v_exp_f32 v178, v162\n"
    "v_mfma_f32_16x16x32_bf16 v[172:175], a[164:167], a[4:7], v[172:175]\n"
    "s_waitcnt vmcnt(7)\n"
    "buffer_load_dwordx4 v189, s[44:47], s71 offen offset:1024 nt lds\n"
    "v_exp_f32 v179, v163\n"
    "v_mfma_f32_16x16x32_bf16 v[168:171], a[152:155], a[8:11], v[168:171]\n"
    "v_exp_f32 v180, v164\n"
    "v_mfma_f32_16x16x32_bf16 v[172:175], a[168:171], a[8:11], v[172:175]\n"
    "s_waitcnt vmcnt(7)\n"
    "buffer_load_dwordx4 v190, s[44:47], s71 offen offset:2048 nt lds\n"
    "v_exp_f32 v181, v165\n"
    "v_mfma_f32_16x16x32_bf16 v[168:171], a[156:159], a[12:15], v[168:171]\n"
    "v_exp_f32 v182, v166\n"
    "v_mfma_f32_16x16x32_bf16 v[172:175], a[172:175], a[12:15], v[172:175]\n"
    "s_waitcnt vmcnt(7)\n"
    "buffer_load_dwordx4 v191, s[44:47], s71 offen offset:3072 nt lds\n"
    "v_exp_f32 v183, v167\n"
    "v_mfma_f32_16x16x32_bf16 v[96:99], a[80:83], v[228:231], v[96:99]\n"
    "v_add_f32 v184, v176, v177\n"
    "v_add_f32 v185, v178, v179\n"
    "v_mfma_f32_16x16x32_bf16 v[100:103], a[84:87], v[228:231], v[100:103]\n"
    "v_add_f32 v186, v180, v181\n"
    "v_add_f32 v187, v182, v183\n"
    "v_mfma_f32_16x16x32_bf16 v[104:107], a[88:91], v[228:231], v[104:107]\n"
    "v_add_f32 v184, v184, v185\n"
    "v_add_f32 v186, v186, v187\n"
    "v_mfma_f32_16x16x32_bf16 v[108:111], a[92:95], v[228:231], v[108:111]\n"
    "v_add_f32 v184, v184, v186\n"
    "v_cvt_pk_bf16_f32 v224, v176, v177\n"
    "v_mfma_f32_16x16x32_bf16 v[112:115], a[96:99], v[228:231], v[112:115]\n"
    "v_cvt_pk_bf16_f32 v225, v178, v179\n"
    "v_cmp_lt_f32 vcc, s77, v184\n"
    "v_mfma_f32_16x16x32_bf16 v[116:119], a[100:103], v[228:231], v[116:119]\n"
    "v_cvt_pk_bf16_f32 v226, v180, v181\n"
    "v_cvt_pk_bf16_f32 v227, v182, v183\n"
    "v_mfma_f32_16x16x32_bf16 v[120:123], a[104:107], v[228:231], v[120:123]\n"
    "s_cbranch_vccnz L_rareBu_l8%=\n"
    "L_rareBu_l8_r%=:\n"
    "v_add_f32 v236, v236, v184\n"
    "v_mfma_f32_16x16x32_bf16 v[124:127], a[108:111], v[228:231], v[124:127]\n"
    "v_mfma_f32_16x16x32_bf16 v[160:163], a[144:147], a[16:19], v[196:199]\n"
    "v_exp_f32 v176, v168\n"
    "v_mfma_f32_16x16x32_bf16 v[164:167], a[160:163], a[16:19], v[196:199]\n"
    "v_exp_f32 v177, v169\n"
    "v_mfma_f32_16x16x32_bf16 v[160:163], a[148:151], a[20:23], v[160:163]\n"
    "v_exp_f32 v178, v170\n"
    "v_mfma_f32_16x16x32_bf16 v[164:167], a[164:167], a[20:23], v[164:167]\n"
    "v_exp_f32 v179, v171\n"
    "v_mfma_f32_16x16x32_bf16 v[160:163], a[152:155], a[24:27], v[160:163]\n"
    "v_exp_f32 v180, v172\n"
    "v_mfma_f32_16x16x32_bf16 v[164:167], a[168:171], a[24:27], v[164:167]\n"
    "v_exp_f32 v181, v173\n"
    "v_mfma_f32_16x16x32_bf16 v[160:163], a[156:159], a[28:31], v[160:163]\n"
    "v_exp_f32 v182, v174\n"
    "v_mfma_f32_16x16x32_bf16 v[164:167], a[172:175], a[28:31], v[164:167]\n"
    "v_exp_f32 v183, v175\n"
    "v_mfma_f32_16x16x32_bf16 v[128:131], a[80:83], v[224:227], v[128:131]\n"
    "ds_read_b64_tr_b16 a[80:81], v212 offset:32768\n"
    "v_add_f32 v184, v176, v177\n"
    "v_add_f32 v185, v178, v179\n"
    "v_mfma_f32_16x16x32_bf16 v[132:135], a[84:87], v[224:227], v[132:135]\n"
    "ds_read_b64_tr_b16 a[82:83], v212 offset:36864\n"
    "v_add_f32 v186, v180, v181\n"
    "v_add_f32 v187, v182, v183\n"
    "v_mfma_f32_16x16x32_bf16 v[136:139], a[88:91], v[224:227], v[136:139]\n"
    "ds_read_b64_tr_b16 a[84:85], v213 offset:32768\n"
    "v_add_f32 v184, v184, v185\n"
    "v_add_f32 v186, v186, v187\n"
    "v_mfma_f32_16x16x32_bf16 v[140:143], a[92:95], v[224:227], v[140:143]\n"
    "ds_read_b64_tr_b16 a[86:87], v213 offset:36864\n"
    "v_add_f32 v184, v184, v186\n"
    "v_cvt_pk_bf16_f32 v228, v176, v177\n"
    "v_mfma_f32_16x16x32_bf16 v[144:147], a[96:99], v[224:227], v[144:147]\n"
    "ds_read_b64_tr_b16 a[88:89], v214 offset:32768\n"
    "v_cvt_pk_bf16_f32 v229, v178, v179\n"
    "v_cmp_lt_f32 vcc, s77, v184\n"
    "v_mfma_f32_16x16x32_bf16 v[148:151], a[100:103], v[224:227], v[148:151]\n"
    "ds_read_b64_tr_b16 a[90:91], v214 offset:36864\n"
    "v_cvt_pk_bf16_f32 v230, v180, v181\n"
    "v_cvt_pk_bf16_f32 v231, v182, v183\n"
    "v_mfma_f32_16x16x32_bf16 v[152:155], a[104:107], v[224:227], v[152:155]\n"
    "ds_read_b64_tr_b16 a[92:93], v215 offset:32768\n"
    "s_cbranch_vccnz L_rareBu_l9%=\n"
    "L_rareBu_l9_r%=:\n"
    "v_add_f32 v232, v232, v184\n"
    "s_sub_u32 s82, s82, 1\n"
    "v_mfma_f32_16x16x32_bf16 v[156:159], a[108:111], v[224:227], v[156:159]\n"
    "ds_read_b64_tr_b16 a[94:95], v215 offset:36864\n"
    "s_bitcmp1_b32 8, s82\n"
    "s_cbranch_scc1 L_attBt%=\n"
    "s_bitcmp1_b32 20, s82\n"
    "s_cbranch_scc1 L_attBu%=\n"
    "s_branch L_attB%=\n"
    "L_attBt%=:\n"
    "v_mfma_f32_16x16x32_bf16 v[168:171], a[144:147], a[32:35], v[200:203]\n"
    "ds_read_b64_tr_b16 a[96:97], v216 offset:32768\n"
    "v_exp_f32 v176, v160\n"
    "v_mfma_f32_16x16x32_bf16 v[172:175], a[160:163], a[32:35], v[200:203]\n"
    "ds_read_b64_tr_b16 a[98:99], v216 offset:36864\n"
    "v_exp_f32 v177, v161\n"
    "v_mfma_f32_16x16x32_bf16 v[168:171], a[148:151], a[36:39], v[168:171]\n"
    "ds_read_b64_tr_b16 a[100:101], v217 offset:32768\n"
    "v_exp_f32 v178, v162\n"
    "v_mfma_f32_16x16x32_bf16 v[172:175], a[164:167], a[36:39], v[172:175]\n"
    "ds_read_b64_tr_b16 a[102:103], v217 offset:36864\n"
    "v_exp_f32 v179, v163\n"
    "v_mfma_f32_16x16x32_bf16 v[168:171], a[152:155], a[40:43], v[168:171]\n"
    "ds_read_b64_tr_b16 a[104:105], v218 offset:32768\n"
    "v_exp_f32 v180, v164\n"
    "v_mfma_f32_16x16x32_bf16 v[172:175], a[168:171], a[40:43], v[172:175]\n"
    "ds_read_b64_tr_b16 a[106:107], v218 offset:36864\n"
    "v_exp_f32 v181, v165\n"
    "v_mfma_f32_16x16x32_bf16 v[168:171], a[156:159], a[44:47], v[168:171]\n"
    "ds_read_b64_tr_b16 a[108:109], v219 offset:32768\n"
    "v_exp_f32 v182, v166\n"
    "v_mfma_f32_16x16x32_bf16 v[172:175], a[172:175], a[44:47], v[172:175]\n"
    "ds_read_b64_tr_b16 a[110:111], v219 offset:36864\n"
    "v_exp_f32 v183, v167\n"
    "s_waitcnt lgkmcnt(4)\n"
    "v_mfma_f32_16x16x32_bf16 v[0:3], a[80:83], v[228:231], v[0:3]\n"
    "v_add_f32 v184, v176, v177\n"
    "v_add_f32 v185, v178, v179\n"
    "s_add_u32 m0, s87, 0\n"
    "s_add_u32 s85, s85, 4096\n"
    "s_and_b32 s85, s85, 0x1ffff\n"
    "v_mfma_f32_16x16x32_bf16 v[4:7], a[84:87], v[228:231], v[4:7]\n"
    "s_waitcnt vmcnt(3)\n"
    "buffer_load_dwordx4 v242, s[48:51], s85 offen offset:0 nt lds\n"
    "v_add_f32 v186, v180, v181\n"
    "v_add_f32 v187, v182, v183\n"
    "v_mfma_f32_16x16x32_bf16 v[8:11], a[88:91], v[228:231], v[8:11]\n"
    "v_add_f32 v184, v184, v185\n"
    "v_add_f32 v186, v186, v187\n"
    "v_mfma_f32_16x16x32_bf16 v[12:15], a[92:95], v[228:231], v[12:15]\n"
    "s_waitcnt vmcnt(3)\n"
    "buffer_load_dwordx4 v243, s[48:51], s85 offen offset:1024 nt lds\n"
    "v_add_f32 v184, v184, v186\n"
    "v_cvt_pk_bf16_f32 v224, v176, v177\n"
    "v_mfma_f32_16x16x32_bf16 v[16:19], a[96:99], v[228:231], v[16:19]\n"
    "v_cvt_pk_bf16_f32 v225, v178, v179\n"
    "v_cmp_lt_f32 vcc, s77, v184\n"
    "v_mfma_f32_16x16x32_bf16 v[20:23], a[100:103], v[228:231], v[20:23]\n"
    "s_waitcnt vmcnt(3)\n"
    "buffer_load_dwordx4 v242, s[48:51], s85 offen offset:2048 nt lds\n"
    "v_cvt_pk_bf16_f32 v226, v180, v181\n"
    "v_cvt_pk_bf16_f32 v227, v182, v183\n"
    "s_waitcnt lgkmcnt(2)\n"
    "v_mfma_f32_16x16x32_bf16 v[24:27], a[104:107], v[228:231], v[24:27]\n"
    "s_cbranch_vccnz L_rareBt_l0%=\n"
    "L_rareBt_l0_r%=:\n"
    "v_add_f32 v233, v233, v184\n"
    "s_waitcnt lgkmcnt(0)\n"
    "v_mfma_f32_16x16x32_bf16 v[28:31], a[108:111], v[228:231], v[28:31]\n"
    "s_waitcnt vmcnt(3)\n"
    "buffer_load_dwordx4 v243, s[48:51], s85 offen offset:3072 nt lds\n"
    "v_mfma_f32_16x16x32_bf16 v[160:163], a[144:147], a[48:51], v[204:207]\n"
    "v_exp_f32 v176, v168\n"
    "s_add_u32 m0, s87, 4096\n"
    "s_add_u32 s85, s85, 4096\n"
    "s_and_b32 s85, s85, 0x1ffff\n"
    "v_mfma_f32_16x16x32_bf16 v[164:167], a[160:163], a[48:51], v[204:207]\n"
    "s_waitcnt vmcnt(3)\n"
    "buffer_load_dwordx4 v242, s[48:51], s85 offen offset:0 nt lds\n"
    "v_exp_f32 v177, v169\n"
    "v_mfma_f32_16x16x32_bf16 v[160:163], a[148:151], a[52:55], v[160:163]\n"
    "v_exp_f32 v178, v170\n"
    "v_mfma_f32_16x16x32_bf16 v[164:167], a[164:167], a[52:55], v[164:167]\n"
    "s_waitcnt vmcnt(3)\n"
    "buffer_load_dwordx4 v243, s[48:51], s85 offen offset:1024 nt lds\n"
    "v_exp_f32 v179, v171\n"
    "v_mfma_f32_16x16x32_bf16 v[160:163], a[152:155], a[56:59], v[160:163]\n"
    "v_exp_f32 v180, v172\n"
    "v_mfma_f32_16x16x32_bf16 v[164:167], a[168:171], a[56:59], v[164:167]\n"
    "s_waitcnt vmcnt(3)\n"
    "buffer_load_dwordx4 v242, s[48:51], s85 offen offset:2048 nt lds\n"
    "v_exp_f32 v181, v173\n"
    "v_mfma_f32_16x16x32_bf16 v[160:163], a[156:159], a[60:63], v[160:163]\n"
    "v_exp_f32 v182, v174\n"
    "v_mfma_f32_16x16x32_bf16 v[164:167], a[172:175], a[60:63], v[164:167]\n"
    "s_waitcnt vmcnt(3)\n"
    "buffer_load_dwordx4 v243, s[48:51], s85 offen offset:3072 nt lds\n"
    "v_exp_f32 v183, v175\n"
    "v_mfma_f32_16x16x32_bf16 v[32:35], a[80:83], v[224:227], v[32:35]\n"
    "v_add_f32 v184, v176, v177\n"
    "v_add_f32 v185, v178, v179\n"
    "s_barrier\n"
    "s_mov_b32 m0, s89\n"
    "s_add_u32 s89, s89, 1024\n"
    "v_mfma_f32_16x16x32_bf16 v[36:39], a[84:87], v[224:227], v[36:39]\n"
    "buffer_load_dwordx4 v247, s[52:55], s88 offen offset:0 lds\n"
    "s_add_u32 s88, s88, 9216\n"
    "v_add_f32 v186, v180, v181\n"
    "v_add_f32 v187, v182, v183\n"
    "v_mfma_f32_16x16x32_bf16 v[40:43], a[88:91], v[224:227], v[40:43]\n"
    "v_add_f32 v184, v184, v185\n"
    "v_add_f32 v186, v186, v187\n"
    "s_mov_b32 m0, s89\n"
    "s_add_u32 s89, s89, 1024\n"
    "v_mfma_f32_16x16x32_bf16 v[44:47], a[92:95], v[224:227], v[44:47]\n"
    "buffer_load_dwordx4 v247, s[52:55], s88 offen offset:0 lds\n"
    "s_add_u32 s88, s88, 9216\n"
    "v_add_f32 v184, v184, v186\n"
    "v_cvt_pk_bf16_f32 v228, v176, v177\n"
    "v_mfma_f32_16x16x32_bf16 v[48:51], a[96:99], v[224:227], v[48:51]\n"
    "v_cvt_pk_bf16_f32 v229, v178, v179\n"
    "v_cmp_lt_f32 vcc, s77, v184\n"
    "s_mov_b32 m0, s89\n"
    "s_add_u32 s89, s89, 1024\n"
    "v_mfma_f32_16x16x32_bf16 v[52:55], a[100:103], v[224:227], v[52:55]\n"
    "buffer_load_dwordx4 v247, s[52:55], s88 offen offset:0 lds\n"
    "s_add_u32 s88, s88, 9216\n"
    "v_cvt_pk_bf16_f32 v230, v180, v181\n"
    "v_cvt_pk_bf16_f32 v231, v182, v183\n"
    "v_mfma_f32_16x16x32_bf16 v[56:59], a[104:107], v[224:227], v[56:59]\n"
    "s_cbranch_vccnz L_rareBt_l1%=\n"
    "L_rareBt_l1_r%=:\n"
    "v_add_f32 v234, v234, v184\n"
    "s_mov_b32 m0, s89\n"
    "s_add_u32 s89, s89, 1024\n"
    "v_mfma_f32_16x16x32_bf16 v[60:63], a[108:111], v[224:227], v[60:63]\n"
    "buffer_load_dwordx4 v247, s[52:55], s88 offen offset:0 lds\n"
    "s_add_u32 s88, s88, 9216\n"
    "v_mfma_f32_16x16x32_bf16 v[168:171], a[144:147], a[64:67], v[208:211]\n"
    "ds_read_b128 a[112:115], v220 offset:0\n"
    "v_exp_f32 v176, v160\n"
    "v_mfma_f32_16x16x32_bf16 v[172:175], a[160:163], a[64:67], v[208:211]\n"
    "ds_read_b128 a[128:131], v220 offset:4096\n"
    "v_exp_f32 v177, v161\n"
    "v_mfma_f32_16x16x32_bf16 v[168:171], a[148:151], a[68:71], v[168:171]\n"
    "ds_read_b128 a[116:119], v221 offset:0\n"
    "v_exp_f32 v178, v162\n"
    "v_mfma_f32_16x16x32_bf16 v[172:175], a[164:167], a[68:71], v[172:175]\n"
    "ds_read_b128 a[132:135], v221 offset:4096\n"
    "v_exp_f32 v179, v163\n"
    "v_mfma_f32_16x16x32_bf16 v[168:171], a[152:155], a[72:75], v[168:171]\n"
    "ds_read_b128 a[120:123], v222 offset:0\n"
    "v_exp_f32 v180, v164\n"
    "v_mfma_f32_16x16x32_bf16 v[172:175], a[168:171], a[72:75], v[172:175]\n"
    "ds_read_b128 a[136:139], v222 offset:4096\n"
    "v_exp_f32 v181, v165\n"
    "v_mfma_f32_16x16x32_bf16 v[168:171], a[156:159], a[76:79], v[168:171]\n"
    "ds_read_b128 a[124:127], v223 offset:0\n"
    "v_exp_f32 v182, v166\n"
    "v_mfma_f32_16x16x32_bf16 v[172:175], a[172:175], a[76:79], v[172:175]\n"
    "ds_read_b128 a[140:143], v223 offset:4096\n"
    "v_exp_f32 v183, v167\n"
    "v_mfma_f32_16x16x32_bf16 v[64:67], a[80:83], v[228:231], v[64:67]\n"
    "v_add_f32 v184, v176, v177\n"
    "v_add_f32 v185, v178, v179\n"
    "v_mfma_f32_16x16x32_bf16 v[68:71], a[84:87], v[228:231], v[68:71]\n"
    "v_add_f32 v186, v180, v181\n"
    "v_add_f32 v187, v182, v183\n"
    "v_mfma_f32_16x16x32_bf16 v[72:75], a[88:91], v[228:231], v[72:75]\n"
    "v_add_f32 v184, v184, v185\n"
    "v_add_f32 v186, v186, v187\n"
    "s_add_u32 m0, s74, 0\n"
    "s_add_u32 s71, s71, 4096\n"
    "s_and_b32 s71, s71, 0x1ffff\n"
    "s_waitcnt lgkmcnt(0)\n"
    "v_mfma_f32_16x16x32_bf16 v[76:79], a[92:95], v[228:231], v[76:79]\n"
    "s_waitcnt vmcnt(3)\n"
    "buffer_load_dwordx4 v188, s[44:47], s71 offen offset:0 nt lds\n"
    "v_add_f32 v184, v184, v186\n"
    "v_cvt_pk_bf16_f32 v224, v176, v177\n"
    "v_mfma_f32_16x16x32_bf16 v[80:83], a[96:99], v[228:231], v[80:83]\n"
    "s_waitcnt vmcnt(3)\n"
    "buffer_load_dwordx4 v189, s[44:47], s71 offen offset:1024 nt lds\n"
    "v_cvt_pk_bf16_f32 v225, v178, v179\n"
    "v_cmp_lt_f32 vcc, s77, v184\n"
    "v_mfma_f32_16x16x32_bf16 v[84:87], a[100:103], v[228:231], v[84:87]\n"
    "s_waitcnt vmcnt(3)\n"
    "buffer_load_dwordx4 v190, s[44:47], s71 offen offset:2048 nt lds\n"
    "v_cvt_pk_bf16_f32 v226, v180, v181\n"
    "v_cvt_pk_bf16_f32 v227, v182, v183\n"
    "v_mfma_f32_16x16x32_bf16 v[88:91], a[104:107], v[228:231], v[88:91]\n"
    "s_cbranch_vccnz L_rareBt_l2%=\n"
    "L_rareBt_l2_r%=:\n"
    "v_add_f32 v235, v235, v184\n"
    "v_mfma_f32_16x16x32_bf16 v[92:95], a[108:111], v[228:231], v[92:95]\n"
    "s_waitcnt vmcnt(3)\n"
    "buffer_load_dwordx4 v191, s[44:47], s71 offen offset:3072 nt lds\n"
    "v_mfma_f32_16x16x32_bf16 v[160:163], a[112:115], a[0:3], v[192:195]\n"
    "v_exp_f32 v176, v168\n"
    "s_add_u32 m0, s74, 4096\n"
    "s_add_u32 s71, s71, 4096\n"
    "s_and_b32 s71, s71, 0x1ffff\n"
    "v_mfma_f32_16x16x32_bf16 v[164:167], a[128:131], a[0:3], v[192:195]\n"
    "s_waitcnt vmcnt(3)\n"
    "buffer_load_dwordx4 v188, s[44:47], s71 offen offset:0 nt lds\n"
    "v_exp_f32 v177, v169\n"
    "v_mfma_f32_16x16x32_bf16 v[160:163], a[116:119], a[4:7], v[160:163]\n"
    "v_exp_f32 v178, v170\n"
    "v_mfma_f32_16x16x32_bf16 v[164:167], a[132:135], a[4:7], v[164:167]\n"
    "s_waitcnt vmcnt(3)\n"
    "buffer_load_dwordx4 v189, s[44:47], s71 offen offset:1024 nt lds\n"
    "v_exp_f32 v179, v171\n"
    "v_mfma_f32_16x16x32_bf16 v[160:163], a[120:123], a[8:11], v[160:163]\n"
    "v_exp_f32 v180, v172\n"
    "v_mfma_f32_16x16x32_bf16 v[164:167], a[136:139], a[8:11], v[164:167]\n"
    "s_waitcnt vmcnt(3)\n"
    "buffer_load_dwordx4 v190, s[44:47], s71 offen offset:2048 nt lds\n"
    "v_exp_f32 v181, v173\n"
    "v_mfma_f32_16x16x32_bf16 v[160:163], a[124:127], a[12:15], v[160:163]\n"
    "v_exp_f32 v182, v174\n"
    "v_mfma_f32_16x16x32_bf16 v[164:167], a[140:143], a[12:15], v[164:167]\n"
    "s_waitcnt vmcnt(3)\n"
    "buffer_load_dwordx4 v191, s[44:47], s71 offen offset:3072 nt lds\n"
    "v_exp_f32 v183, v175\n"
    "v_mfma_f32_16x16x32_bf16 v[96:99], a[80:83], v[224:227], v[96:99]\n"
    "v_add_f32 v184, v176, v177\n"
    "v_add_f32 v185, v178, v179\n"
    "s_mov_b32 m0, s89\n"
    "s_add_u32 s89, s89, 1024\n"
    "v_mfma_f32_16x16x32_bf16 v[100:103], a[84:87], v[224:227], v[100:103]\n"
    "buffer_load_dwordx4 v247, s[52:55], s88 offen offset:0 lds\n"
    "s_add_u32 s88, s88, 9216\n"
    "v_add_f32 v186, v180, v181\n"
    "v_add_f32 v187, v182, v183\n"
    "v_mfma_f32_16x16x32_bf16 v[104:107], a[88:91], v[224:227], v[104:107]\n"
    "v_add_f32 v184, v184, v185\n"
    "v_add_f32 v186, v186, v187\n"
    "s_mov_b32 m0, s89\n"
    "s_add_u32 s89, s89, 1024\n"
    "v_mfma_f32_16x16x32_bf16 v[108:111], a[92:95], v[224:227], v[108:111]\n"
    "buffer_load_dwordx4 v247, s[52:55], s88 offen offset:0 lds\n"
    "s_add_u32 s88, s88, 9216\n"
    "v_add_f32 v184, v184, v186\n"
    "v_cvt_pk_bf16_f32 v228, v176, v177\n"
    "v_mfma_f32_16x16x32_bf16 v[112:115], a[96:99], v[224:227], v[112:115]\n"
    "v_cvt_pk_bf16_f32 v229, v178, v179\n"
    "v_cmp_lt_f32 vcc, s77, v184\n"
    "s_mov_b32 m0, s89\n"
    "s_add_u32 s89, s89, 1024\n"
    "v_mfma_f32_16x16x32_bf16 v[116:119], a[100:103], v[224:227], v[116:119]\n"
    "buffer_load_dwordx4 v247, s[52:55], s88 offen offset:0 lds\n"
    "s_add_u32 s88, s88, 9216\n"
    "v_cvt_pk_bf16_f32 v230, v180, v181\n"
    "v_cvt_pk_bf16_f32 v231, v182, v183\n"
    "v_mfma_f32_16x16x32_bf16 v[120:123], a[104:107], v[224:227], v[120:123]\n"
    "s_cbranch_vccnz L_rareBt_l3%=\n"
    "L_rareBt_l3_r%=:\n"
    "v_add_f32 v236, v236, v184\n"
    "s_mov_b32 m0, s89\n"
    "s_add_u32 s89, s89, 1024\n"
    "v_mfma_f32_16x16x32_bf16 v[124:127], a[108:111], v[224:227], v[124:127]\n"
    "buffer_load_dwordx4 v247, s[52:55], s88 offen offset:0 lds\n"
    "s_add_u32 s88, s88, 9216\n"
    "v_mfma_f32_16x16x32_bf16 v[168:171], a[112:115], a[16:19], v[196:199]\n"
    "v_exp_f32 v176, v160\n"
    "v_mfma_f32_16x16x32_bf16 v[172:175], a[128:131], a[16:19], v[196:199]\n"
    "v_exp_f32 v177, v161\n"
    "v_mfma_f32_16x16x32_bf16 v[168:171], a[116:119], a[20:23], v[168:171]\n"
    "v_exp_f32 v178, v162\n"
    "v_mfma_f32_16x16x32_bf16 v[172:175], a[132:135], a[20:23], v[172:175]\n"
    "v_exp_f32 v179, v163\n"
    "v_mfma_f32_16x16x32_bf16 v[168:171], a[120:123], a[24:27], v[168:171]\n"
    "v_exp_f32 v180, v164\n"
    "v_mfma_f32_16x16x32_bf16 v[172:175], a[136:139], a[24:27], v[172:175]\n"
    "v_exp_f32 v181, v165\n"
    "v_mfma_f32_16x16x32_bf16 v[168:171], a[124:127], a[28:31], v[168:171]\n"
    "v_exp_f32 v182, v166\n"
    "v_mfma_f32_16x16x32_bf16 v[172:175], a[140:143], a[28:31], v[172:175]\n"
    "v_exp_f32 v183, v167\n"
    "v_mfma_f32_16x16x32_bf16 v[128:131], a[80:83], v[228:231], v[128:131]\n"
    "ds_read_b64_tr_b16 a[80:81], v212 offset:0\n"
    "v_add_f32 v184, v176, v177\n"
    "v_add_f32 v185, v178, v179\n"
    "v_mfma_f32_16x16x32_bf16 v[132:135], a[84:87], v[228:231], v[132:135]\n"
    "ds_read_b64_tr_b16 a[82:83], v212 offset:4096\n"
    "v_add_f32 v186, v180, v181\n"
    "v_add_f32 v187, v182, v183\n"
    "v_mfma_f32_16x16x32_bf16 v[136:139], a[88:91], v[228:231], v[136:139]\n"
    "ds_read_b64_tr_b16 a[84:85], v213 offset:0\n"
    "v_add_f32 v184, v184, v185\n"
    "v_add_f32 v186, v186, v187\n"
    "v_mfma_f32_16x16x32_bf16 v[140:143], a[92:95], v[228:231], v[140:143]\n"
    "ds_read_b64_tr_b16 a[86:87], v213 offset:4096\n"
    "v_add_f32 v184, v184, v186\n"
    "v_cvt_pk_bf16_f32 v224, v176, v177\n"
    "v_mfma_f32_16x16x32_bf16 v[144:147], a[96:99], v[228:231], v[144:147]\n"
    "ds_read_b64_tr_b16 a[88:89], v214 offset:0\n"
    "v_cvt_pk_bf16_f32 v225, v178, v179\n"
    "v_cmp_lt_f32 vcc, s77, v184\n"
    "v_mfma_f32_16x16x32_bf16 v[148:151], a[100:103], v[228:231], v[148:151]\n"
    "ds_read_b64_tr_b16 a[90:91], v214 offset:4096\n"
    "v_cvt_pk_bf16_f32 v226, v180, v181\n"
    "v_cvt_pk_bf16_f32 v227, v182, v183\n"
    "v_mfma_f32_16x16x32_bf16 v[152:155], a[104:107], v[228:231], v[152:155]\n"
    "ds_read_b64_tr_b16 a[92:93], v215 offset:0\n"
    "s_cbranch_vccnz L_rareBt_l4%=\n"
    "L_rareBt_l4_r%=:\n"
    "v_add_f32 v232, v232, v184\n"
    "v_mfma_f32_16x16x32_bf16 v[156:159], a[108:111], v[228:231], v[156:159]\n"
    "ds_read_b64_tr_b16 a[94:95], v215 offset:4096\n"
    "v_mfma_f32_16x16x32_bf16 v[160:163], a[112:115], a[32:35], v[200:203]\n"
    "ds_read_b64_tr_b16 a[96:97], v216 offset:0\n"
    "v_exp_f32 v176, v168\n"
    "v_mfma_f32_16x16x32_bf16 v[164:167], a[128:131], a[32:35], v[200:203]\n"
    "ds_read_b64_tr_b16 a[98:99], v216 offset:4096\n"
    "v_exp_f32 v177, v169\n"
    "v_mfma_f32_16x16x32_bf16 v[160:163], a[116:119], a[36:39], v[160:163]\n"
    "ds_read_b64_tr_b16 a[100:101], v217 offset:0\n"
    "v_exp_f32 v178, v170\n"
    "v_mfma_f32_16x16x32_bf16 v[164:167], a[132:135], a[36:39], v[164:167]\n"
    "ds_read_b64_tr_b16 a[102:103], v217 offset:4096\n"
    "v_exp_f32 v179, v171\n"
    "v_mfma_f32_16x16x32_bf16 v[160:163], a[120:123], a[40:43], v[160:163]\n"
    "ds_read_b64_tr_b16 a[104:105], v218 offset:0\n"
    "v_exp_f32 v180, v172\n"
    "v_mfma_f32_16x16x32_bf16 v[164:167], a[136:139], a[40:43], v[164:167]\n"
    "ds_read_b64_tr_b16 a[106:107], v218 offset:4096\n"
    "v_exp_f32 v181, v173\n"
    "v_mfma_f32_16x16x32_bf16 v[160:163], a[124:127], a[44:47], v[160:163]\n"
    "ds_read_b64_tr_b16 a[108:109], v219 offset:0\n"
    "v_exp_f32 v182, v174\n"
    "v_mfma_f32_16x16x32_bf16 v[164:167], a[140:143], a[44:47], v[164:167]\n"
    "ds_read_b64_tr_b16 a[110:111], v219 offset:4096\n"
    "v_exp_f32 v183, v175\n"
    "s_waitcnt lgkmcnt(4)\n"
    "v_mfma_f32_16x16x32_bf16 v[0:3], a[80:83], v[224:227], v[0:3]\n"
    "v_add_f32 v184, v176, v177\n"
    "v_add_f32 v185, v178, v179\n"
    "s_add_u32 m0, s87, 32768\n"
    "s_add_u32 s85, s85, 4096\n"
    "s_and_b32 s85, s85, 0x1ffff\n"
    "v_mfma_f32_16x16x32_bf16 v[4:7], a[84:87], v[224:227], v[4:7]\n"
    "s_waitcnt vmcnt(3)\n"
    "buffer_load_dwordx4 v242, s[48:51], s85 offen offset:0 nt lds\n"
    "v_add_f32 v186, v180, v181\n"
    "v_add_f32 v187, v182, v183\n"
    "v_mfma_f32_16x16x32_bf16 v[8:11], a[88:91], v[224:227], v[8:11]\n"
    "v_add_f32 v184, v184, v185\n"
    "v_add_f32 v186, v186, v187\n"
    "v_mfma_f32_16x16x32_bf16 v[12:15], a[92:95], v[224:227], v[12:15]\n"
    "s_waitcnt vmcnt(3)\n"
    "buffer_load_dwordx4 v243, s[48:51], s85 offen offset:1024 nt lds\n"
    "v_add_f32 v184, v184, v186\n"
    "v_cvt_pk_bf16_f32 v228, v176, v177\n"
    "v_mfma_f32_16x16x32_bf16 v[16:19], a[96:99], v[224:227], v[16:19]\n"
    "v_cvt_pk_bf16_f32 v229, v178, v179\n"
    "v_cmp_lt_f32 vcc, s77, v184\n"
    "v_mfma_f32_16x16x32_bf16 v[20:23], a[100:103], v[224:227], v[20:23]\n"
    "s_waitcnt vmcnt(3)\n"
    "buffer_load_dwordx4 v242, s[48:51], s85 offen offset:2048 nt lds\n"
    "v_cvt_pk_bf16_f32 v230, v180, v181\n"
    "v_cvt_pk_bf16_f32 v231, v182, v183\n"
    "s_waitcnt lgkmcnt(2)\n"
    "v_mfma_f32_16x16x32_bf16 v[24:27], a[104:107], v[224:227], v[24:27]\n"
    "s_cbranch_vccnz L_rareBt_l5%=\n"
    "L_rareBt_l5_r%=:\n"
    "v_add_f32 v233, v233, v184\n"
    "s_waitcnt lgkmcnt(0)\n"
    "v_mfma_f32_16x16x32_bf16 v[28:31], a[108:111], v[224:227], v[28:31]\n"
    "s_waitcnt vmcnt(3)\n"
    "buffer_load_dwordx4 v243, s[48:51], s85 offen offset:3072 nt lds\n"
    "v_mfma_f32_16x16x32_bf16 v[168:171], a[112:115], a[48:51], v[204:207]\n"
    "v_exp_f32 v176, v160\n"
    "s_add_u32 m0, s87, 36864\n"
    "s_add_u32 s85, s85, 4096\n"
    "s_and_b32 s85, s85, 0x1ffff\n"
    "v_mfma_f32_16x16x32_bf16 v[172:175], a[128:131], a[48:51], v[204:207]\n"
    "s_waitcnt vmcnt(3)\n"
    "buffer_load_dwordx4 v242, s[48:51], s85 offen offset:0 nt lds\n"
    "v_exp_f32 v177, v161\n"
    "v_mfma_f32_16x16x32_bf16 v[168:171], a[116:119], a[52:55], v[168:171]\n"
    "v_exp_f32 v178, v162\n"
    "v_mfma_f32_16x16x32_bf16 v[172:175], a[132:135], a[52:55], v[172:175]\n"
    "s_waitcnt vmcnt(3)\n"
    "buffer_load_dwordx4 v243, s[48:51], s85 offen offset:1024 nt lds\n"
    "v_exp_f32 v179, v163\n"
    "v_mfma_f32_16x16x32_bf16 v[168:171], a[120:123], a[56:59], v[168:171]\n"
    "v_exp_f32 v180, v164\n"
    "v_mfma_f32_16x16x32_bf16 v[172:175], a[136:139], a[56:59], v[172:175]\n"
    "s_waitcnt vmcnt(3)\n"
    "buffer_load_dwordx4 v242, s[48:51], s85 offen offset:2048 nt lds\n"
    "v_exp_f32 v181, v165\n"
    "v_mfma_f32_16x16x32_bf16 v[168:171], a[124:127], a[60:63], v[168:171]\n"
    "v_exp_f32 v182, v166\n"
    "v_mfma_f32_16x16x32_bf16 v[172:175], a[140:143], a[60:63], v[172:175]\n"
    "s_waitcnt vmcnt(3)\n"
    "buffer_load_dwordx4 v243, s[48:51], s85 offen offset:3072 nt lds\n"
    "v_exp_f32 v183, v167\n"
    "v_mfma_f32_16x16x32_bf16 v[32:35], a[80:83], v[228:231], v[32:35]\n"
    "v_add_f32 v184, v176, v177\n"
    "v_add_f32 v185, v178, v179\n"
    "s_mov_b32 m0, s89\n"
    "s_add_u32 s89, s89, 1024\n"
    "v_mfma_f32_16x16x32_bf16 v[36:39], a[84:87], v[228:231], v[36:39]\n"
    "buffer_load_dwordx4 v247, s[52:55], s88 offen offset:0 lds\n"
    "s_add_u32 s88, s88, 9216\n"
    "v_add_f32 v186, v180, v181\n"
    "v_add_f32 v187, v182, v183\n"
    "v_mfma_f32_16x16x32_bf16 v[40:43], a[88:91], v[228:231], v[40:43]\n"
    "v_add_f32 v184, v184, v185\n"
    "v_add_f32 v186, v186, v187\n"
    "s_mov_b32 m0, s89\n"
    "s_add_u32 s89, s89, 1024\n"
    "v_mfma_f32_16x16x32_bf16 v[44:47], a[92:95], v[228:231], v[44:47]\n"
    "buffer_load_dwordx4 v247, s[52:55], s88 offen offset:0 lds\n"
    "s_add_u32 s88, s88, 9216\n"
    "v_add_f32 v184, v184, v186\n"
    "v_cvt_pk_bf16_f32 v224, v176, v177\n"
    "v_mfma_f32_16x16x32_bf16 v[48:51], a[96:99], v[228:231], v[48:51]\n"
    "v_cvt_pk_bf16_f32 v225, v178, v179\n"
    "v_cmp_lt_f32 vcc, s77, v184\n"
    "s_mov_b32 m0, s89\n"
    "s_add_u32 s89, s89, 1024\n"
    "v_mfma_f32_16x16x32_bf16 v[52:55], a[100:103], v[228:231], v[52:55]\n"
    "buffer_load_dwordx4 v247, s[52:55], s88 offen offset:0 lds\n"
    "s_add_u32 s88, s88, 9216\n"
    "v_cvt_pk_bf16_f32 v226, v180, v181\n"
    "v_cvt_pk_bf16_f32 v227, v182, v183\n"
    "v_mfma_f32_16x16x32_bf16 v[56:59], a[104:107], v[228:231], v[56:59]\n"
    "s_cbranch_vccnz L_rareBt_l6%=\n"
    "L_rareBt_l6_r%=:\n"
    "v_add_f32 v234, v234, v184\n"
    "s_mov_b32 m0, s89\n"
    "s_add_u32 s89, s89, 1024\n"
    "v_mfma_f32_16x16x32_bf16 v[60:63], a[108:111], v[228:231], v[60:63]\n"
    "buffer_load_dwordx4 v247, s[52:55], s88 offen offset:0 lds\n"
    "s_add_u32 s88, s88, 9216\n"
    "v_mfma_f32_16x16x32_bf16 v[160:163], a[112:115], a[64:67], v[208:211]\n"
    "ds_read_b128 a[144:147], v220 offset:0\n"
    "v_exp_f32 v176, v168\n"
    "v_mfma_f32_16x16x32_bf16 v[164:167], a[128:131], a[64:67], v[208:211]\n"
    "ds_read_b128 a[160:163], v220 offset:4096\n"
    "v_exp_f32 v177, v169\n"
    "v_mfma_f32_16x16x32_bf16 v[160:163], a[116:119], a[68:71], v[160:163]\n"
    "ds_read_b128 a[148:151], v221 offset:0\n"
    "v_exp_f32 v178, v170\n"
    "v_mfma_f32_16x16x32_bf16 v[164:167], a[132:135], a[68:71], v[164:167]\n"
    "ds_read_b128 a[164:167], v221 offset:4096\n"
    "v_exp_f32 v179, v171\n"
    "v_mfma_f32_16x16x32_bf16 v[160:163], a[120:123], a[72:75], v[160:163]\n"
    "ds_read_b128 a[152:155], v222 offset:0\n"
    "v_exp_f32 v180, v172\n"
    "v_mfma_f32_16x16x32_bf16 v[164:167], a[136:139], a[72:75], v[164:167]\n"
    "ds_read_b128 a[168:171], v222 offset:4096\n"
    "v_exp_f32 v181, v173\n"
    "v_mfma_f32_16x16x32_bf16 v[160:163], a[124:127], a[76:79], v[160:163]\n"
    "ds_read_b128 a[156:159], v223 offset:0\n"
    "v_exp_f32 v182, v174\n"
    "v_mfma_f32_16x16x32_bf16 v[164:167], a[140:143], a[76:79], v[164:167]\n"
    "ds_read_b128 a[172:175], v223 offset:4096\n"
    "v_exp_f32 v183, v175\n"
    "v_mfma_f32_16x16x32_bf16 v[64:67], a[80:83], v[224:227], v[64:67]\n"
    "v_add_f32 v184, v176, v177\n"
    "v_add_f32 v185, v178, v179\n"
    "v_mfma_f32_16x16x32_bf16 v[68:71], a[84:87], v[224:227], v[68:71]\n"
    "v_add_f32 v186, v180, v181\n"
    "v_add_f32 v187, v182, v183\n"
    "v_mfma_f32_16x16x32_bf16 v[72:75], a[88:91], v[224:227], v[72:75]\n"
    "v_add_f32 v184, v184, v185\n"
    "v_add_f32 v186, v186, v187\n"
    "s_add_u32 m0, s74, 0\n"
    "s_add_u32 s71, s71, 4096\n"
    "s_and_b32 s71, s71, 0x1ffff\n"
    "s_waitcnt lgkmcnt(0)\n"
    "v_mfma_f32_16x16x32_bf16 v[76:79], a[92:95], v[224:227], v[76:79]\n"
    "s_waitcnt vmcnt(3)\n"
    "buffer_load_dwordx4 v188, s[44:47], s71 offen offset:0 nt lds\n"
    "v_add_f32 v184, v184, v186\n"
    "v_cvt_pk_bf16_f32 v228, v176, v177\n"
    "v_mfma_f32_16x16x32_bf16 v[80:83], a[96:99], v[224:227], v[80:83]\n"
    "s_waitcnt vmcnt(3)\n"
    "buffer_load_dwordx4 v189, s[44:47], s71 offen offset:1024 nt lds\n"
    "v_cvt_pk_bf16_f32 v229, v178, v179\n"
    "v_cmp_lt_f32 vcc, s77, v184\n"
    "v_mfma_f32_16x16x32_bf16 v[84:87], a[100:103], v[224:227], v[84:87]\n"
    "s_waitcnt vmcnt(3)\n"
    "buffer_load_dwordx4 v190, s[44:47], s71 offen offset:2048 nt lds\n"
    "v_cvt_pk_bf16_f32 v230, v180, v181\n"
    "v_cvt_pk_bf16_f32 v231, v182, v183\n"
    "v_mfma_f32_16x16x32_bf16 v[88:91], a[104:107], v[224:227], v[88:91]\n"
    "s_cbranch_vccnz L_rareBt_l7%=\n"
    "L_rareBt_l7_r%=:\n"
    "v_add_f32 v235, v235, v184\n"
    "v_mfma_f32_16x16x32_bf16 v[92:95], a[108:111], v[224:227], v[92:95]\n"
    "s_waitcnt vmcnt(3)\n"
    "buffer_load_dwordx4 v191, s[44:47], s71 offen offset:3072 nt lds\n"
    "v_mfma_f32_16x16x32_bf16 v[168:171], a[144:147], a[0:3], v[192:195]\n"
    "v_exp_f32 v176, v160\n"
    "s_add_u32 m0, s74, 4096\n"
    "s_add_u32 s71, s71, 4096\n"
    "s_and_b32 s71, s71, 0x1ffff\n"
    "v_mfma_f32_16x16x32_bf16 v[172:175], a[160:163], a[0:3], v[192:195]\n"
    "s_waitcnt vmcnt(3)\n"
    "buffer_load_dwordx4 v188, s[44:47], s71 offen offset:0 nt lds\n"
    "v_exp_f32 v177, v161\n"
    "v_mfma_f32_16x16x32_bf16 v[168:171], a[148:151], a[4:7], v[168:171]\n"
    "v_exp_f32 v178, v162\n"
    "v_mfma_f32_16x16x32_bf16 v[172:175], a[164:167], a[4:7], v[172:175]\n"
    "s_waitcnt vmcnt(3)\n"
    "buffer_load_dwordx4 v189, s[44:47], s71 offen offset:1024 nt lds\n"
    "v_exp_f32 v179, v163\n"
    "v_mfma_f32_16x16x32_bf16 v[168:171], a[152:155], a[8:11], v[168:171]\n"
    "v_exp_f32 v180, v164\n"
    "v_mfma_f32_16x16x32_bf16 v[172:175], a[168:171], a[8:11], v[172:175]\n"
    "s_waitcnt vmcnt(3)\n"
    "buffer_load_dwordx4 v190, s[44:47], s71 offen offset:2048 nt lds\n"
    "v_exp_f32 v181, v165\n"
    "v_mfma_f32_16x16x32_bf16 v[168:171], a[156:159], a[12:15], v[168:171]\n"
    "v_exp_f32 v182, v166\n"
    "v_mfma_f32_16x16x32_bf16 v[172:175], a[172:175], a[12:15], v[172:175]\n"
    "s_waitcnt vmcnt(3)\n"
    "buffer_load_dwordx4 v191, s[44:47], s71 offen offset:3072 nt lds\n"
    "v_exp_f32 v183, v167\n"
    "v_mfma_f32_16x16x32_bf16 v[96:99], a[80:83], v[228:231], v[96:99]\n"
    "v_add_f32 v184, v176, v177\n"
    "v_add_f32 v185, v178, v179\n"
    "s_mov_b32 m0, s89\n"
    "s_add_u32 s89, s89, 1024\n"
    "v_mfma_f32_16x16x32_bf16 v[100:103], a[84:87], v[228:231], v[100:103]\n"
    "buffer_load_dwordx4 v247, s[52:55], s88 offen offset:0 lds\n"
    "s_add_u32 s88, s88, 9216\n"
    "v_add_f32 v186, v180, v181\n"
    "v_add_f32 v187, v182, v183\n"
    "v_mfma_f32_16x16x32_bf16 v[104:107], a[88:91], v[228:231], v[104:107]\n"
    "v_add_f32 v184, v184, v185\n"
    "v_add_f32 v186, v186, v187\n"
    "s_mov_b32 m0, s89\n"
    "s_add_u32 s89, s89, 1024\n"
    "v_mfma_f32_16x16x32_bf16 v[108:111], a[92:95], v[228:231], v[108:111]\n"
    "buffer_load_dwordx4 v247, s[52:55], s88 offen offset:0 lds\n"
    "s_add_u32 s88, s88, 9216\n"
    "v_add_f32 v184, v184, v186\n"
    "v_cvt_pk_bf16_f32 v224, v176, v177\n"
    "v_mfma_f32_16x16x32_bf16 v[112:115], a[96:99], v[228:231], v[112:115]\n"
    "v_cvt_pk_bf16_f32 v225, v178, v179\n"
    "v_cmp_lt_f32 vcc, s77, v184\n"
    "s_mov_b32 m0, s89\n"
    "s_add_u32 s89, s89, 1024\n"
    "v_mfma_f32_16x16x32_bf16 v[116:119], a[100:103], v[228:231], v[116:119]\n"
    "buffer_load_dwordx4 v247, s[52:55], s88 offen offset:0 lds\n"
    "s_add_u32 s88, s88, 9216\n"
    "v_cvt_pk_bf16_f32 v226, v180, v181\n"
    "v_cvt_pk_bf16_f32 v227, v182, v183\n"
    "v_mfma_f32_16x16x32_bf16 v[120:123], a[104:107], v[228:231], v[120:123]\n"
    "s_cbranch_vccnz L_rareBt_l8%=\n"
    "L_rareBt_l8_r%=:\n"
    "v_add_f32 v236, v236, v184\n"
    "s_mov_b32 m0, s89\n"
    "s_add_u32 s89, s89, 1024\n"
    "v_mfma_f32_16x16x32_bf16 v[124:127], a[108:111], v[228:231], v[124:127]\n"
    "buffer_load_dwordx4 v247, s[52:55], s88 offen offset:0 lds\n"
    "s_add_u32 s88, s88, 9216\n"
    "v_mfma_f32_16x16x32_bf16 v[160:163], a[144:147], a[16:19], v[196:199]\n"
    "v_exp_f32 v176, v168\n"
    "v_mfma_f32_16x16x32_bf16 v[164:167], a[160:163], a[16:19], v[196:199]\n"
    "v_exp_f32 v177, v169\n"
    "v_mfma_f32_16x16x32_bf16 v[160:163], a[148:151], a[20:23], v[160:163]\n"
    "v_exp_f32 v178, v170\n"
    "v_mfma_f32_16x16x32_bf16 v[164:167], a[164:167], a[20:23], v[164:167]\n"
    "v_exp_f32 v179, v171\n"
    "v_mfma_f32_16x16x32_bf16 v[160:163], a[152:155], a[24:27], v[160:163]\n"
    "v_exp_f32 v180, v172\n"
    "v_mfma_f32_16x16x32_bf16 v[164:167], a[168:171], a[24:27], v[164:167]\n"
    "v_exp_f32 v181, v173\n"
    "v_mfma_f32_16x16x32_bf16 v[160:163], a[156:159], a[28:31], v[160:163]\n"
    "v_exp_f32 v182, v174\n"
    "v_mfma_f32_16x16x32_bf16 v[164:167], a[172:175], a[28:31], v[164:167]\n"
    "v_exp_f32 v183, v175\n"
    "v_mfma_f32_16x16x32_bf16 v[128:131], a[80:83], v[224:227], v[128:131]\n"
    "ds_read_b64_tr_b16 a[80:81], v212 offset:32768\n"
    "v_add_f32 v184, v176, v177\n"
    "v_add_f32 v185, v178, v179\n"
    "v_mfma_f32_16x16x32_bf16 v[132:135], a[84:87], v[224:227], v[132:135]\n"
    "ds_read_b64_tr_b16 a[82:83], v212 offset:36864\n"
    "v_add_f32 v186, v180, v181\n"
    "v_add_f32 v187, v182, v183\n"
    "v_mfma_f32_16x16x32_bf16 v[136:139], a[88:91], v[224:227], v[136:139]\n"
    "ds_read_b64_tr_b16 a[84:85], v213 offset:32768\n"
    "v_add_f32 v184, v184, v185\n"
    "v_add_f32 v186, v186, v187\n"
    "v_mfma_f32_16x16x32_bf16 v[140:143], a[92:95], v[224:227], v[140:143]\n"
    "ds_read_b64_tr_b16 a[86:87], v213 offset:36864\n"
    "v_add_f32 v184, v184, v186\n"
    "v_cvt_pk_bf16_f32 v228, v176, v177\n"
    "v_mfma_f32_16x16x32_bf16 v[144:147], a[96:99], v[224:227], v[144:147]\n"
    "ds_read_b64_tr_b16 a[88:89], v214 offset:32768\n"
    "v_cvt_pk_bf16_f32 v229, v178, v179\n"
    "v_cmp_lt_f32 vcc, s77, v184\n"
    "v_mfma_f32_16x16x32_bf16 v[148:151], a[100:103], v[224:227], v[148:151]\n"
    "ds_read_b64_tr_b16 a[90:91], v214 offset:36864\n"
    "v_cvt_pk_bf16_f32 v230, v180, v181\n"
    "v_cvt_pk_bf16_f32 v231, v182, v183\n"
    "v_mfma_f32_16x16x32_bf16 v[152:155], a[104:107], v[224:227], v[152:155]\n"
    "ds_read_b64_tr_b16 a[92:93], v215 offset:32768\n"
    "s_cbranch_vccnz L_rareBt_l9%=\n"
    "L_rareBt_l9_r%=:\n"
    "v_add_f32 v232, v232, v184\n"
    "s_sub_u32 s82, s82, 1\n"
    "v_mfma_f32_16x16x32_bf16 v[156:159], a[108:111], v[224:227], v[156:159]\n"
    "ds_read_b64_tr_b16 a[94:95], v215 offset:36864\n"
    "s_bitcmp1_b32 8, s82\n"
    "s_cbranch_scc1 L_attBt%=\n"
    "s_bitcmp1_b32 20, s82\n"
    "s_cbranch_scc1 L_attBu%=\n"
    "s_branch L_attB%=\n"
    "L_attxB%=:\n"
    "v_mfma_f32_16x16x32_bf16 v[168:171], a[144:147], a[32:35], v[200:203]\n"
    "ds_read_b64_tr_b16 a[96:97], v216 offset:32768\n"
    "v_exp_f32 v176, v160\n"
    "v_mfma_f32_16x16x32_bf16 v[172:175], a[160:163], a[32:35], v[200:203]\n"
    "ds_read_b64_tr_b16 a[98:99], v216 offset:36864\n"
    "v_exp_f32 v177, v161\n"
    "v_mfma_f32_16x16x32_bf16 v[168:171], a[148:151], a[36:39], v[168:171]\n"
    "ds_read_b64_tr_b16 a[100:101], v217 offset:32768\n"
    "v_exp_f32 v178, v162\n"
    "v_mfma_f32_16x16x32_bf16 v[172:175], a[164:167], a[36:39], v[172:175]\n"
    "ds_read_b64_tr_b16 a[102:103], v217 offset:36864\n"
    "v_exp_f32 v179, v163\n"
    "v_mfma_f32_16x16x32_bf16 v[168:171], a[152:155], a[40:43], v[168:171]\n"
    "ds_read_b64_tr_b16 a[104:105], v218 offset:32768\n"
    "v_exp_f32 v180, v164\n"
    "v_mfma_f32_16x16x32_bf16 v[172:175], a[168:171], a[40:43], v[172:175]\n"
    "ds_read_b64_tr_b16 a[106:107], v218 offset:36864\n"
    "v_exp_f32 v181, v165\n"
    "v_mfma_f32_16x16x32_bf16 v[168:171], a[156:159], a[44:47], v[168:171]\n"
    "ds_read_b64_tr_b16 a[108:109], v219 offset:32768\n"
    "v_exp_f32 v182, v166\n"
    "v_mfma_f32_16x16x32_bf16 v[172:175], a[172:175], a[44:47], v[172:175]\n"
    "ds_read_b64_tr_b16 a[110:111], v219 offset:36864\n"
    "v_exp_f32 v183, v167\n"
    "s_waitcnt lgkmcnt(4)\n"
    "v_mfma_f32_16x16x32_bf16 v[0:3], a[80:83], v[228:231], v[0:3]\n"
    "v_add_f32 v184, v176, v177\n"
    "v_add_f32 v185, v178, v179\n"
    "s_add_u32 m0, s87, 0\n"
    "s_add_u32 s85, s85, 4096\n"
    "s_and_b32 s85, s85, 0x1ffff\n"
    "v_mfma_f32_16x16x32_bf16 v[4:7], a[84:87], v[228:231], v[4:7]\n"
    "buffer_load_dwordx4 v242, s[48:51], s85 offen offset:0 nt lds\n"
    "v_add_f32 v186, v180, v181\n"
    "v_add_f32 v187, v182, v183\n"
    "v_mfma_f32_16x16x32_bf16 v[8:11], a[88:91], v[228:231], v[8:11]\n"
    "v_add_f32 v184, v184, v185\n"
    "v_add_f32 v186, v186, v187\n"
    "v_mfma_f32_16x16x32_bf16 v[12:15], a[92:95], v[228:231], v[12:15]\n"
    "buffer_load_dwordx4 v243, s[48:51], s85 offen offset:1024 nt lds\n"
    "v_add_f32 v184, v184, v186\n"
    "v_cvt_pk_bf16_f32 v224, v176, v177\n"
    "v_mfma_f32_16x16x32_bf16 v[16:19], a[96:99], v[228:231], v[16:19]\n"
    "v_cvt_pk_bf16_f32 v225, v178, v179\n"
    "v_cmp_lt_f32 vcc, s77, v184\n"
    "v_mfma_f32_16x16x32_bf16 v[20:23], a[100:103], v[228:231], v[20:23]\n"
    "buffer_load_dwordx4 v242, s[48:51], s85 offen offset:2048 nt lds\n"
    "v_cvt_pk_bf16_f32 v226, v180, v181\n"
    "v_cvt_pk_bf16_f32 v227, v182, v183\n"
    "s_waitcnt lgkmcnt(2)\n"
    "v_mfma_f32_16x16x32_bf16 v[24:27], a[104:107], v[228:231], v[24:27]\n"
    "s_cbranch_vccnz L_rareB_e66%=\n"
    "L_rareB_e66_r%=:\n"
    "v_add_f32 v233, v233, v184\n"
    "s_waitcnt lgkmcnt(0)\n"
    "v_mfma_f32_16x16x32_bf16 v[28:31], a[108:111], v[228:231], v[28:31]\n"
    "buffer_load_dwordx4 v243, s[48:51], s85 offen offset:3072 nt lds\n"
    "v_mfma_f32_16x16x32_bf16 v[160:163], a[144:147], a[48:51], v[204:207]\n"
    "v_exp_f32 v176, v168\n"
    "s_add_u32 m0, s87, 4096\n"
    "s_add_u32 s85, s85, 4096\n"
    "s_and_b32 s85, s85, 0x1ffff\n"
    "v_mfma_f32_16x16x32_bf16 v[164:167], a[160:163], a[48:51], v[204:207]\n"
    "buffer_load_dwordx4 v242, s[48:51], s85 offen offset:0 nt lds\n"
    "v_exp_f32 v177, v169\n"
    "v_mfma_f32_16x16x32_bf16 v[160:163], a[148:151], a[52:55], v[160:163]\n"
    "v_exp_f32 v178, v170\n"
    "v_mfma_f32_16x16x32_bf16 v[164:167], a[164:167], a[52:55], v[164:167]\n"
    "buffer_load_dwordx4 v243, s[48:51], s85 offen offset:1024 nt lds\n"
    "v_exp_f32 v179, v171\n"
    "v_mfma_f32_16x16x32_bf16 v[160:163], a[152:155], a[56:59], v[160:163]\n"
    "v_exp_f32 v180, v172\n"
    "v_mfma_f32_16x16x32_bf16 v[164:167], a[168:171], a[56:59], v[164:167]\n"
    "buffer_load_dwordx4 v242, s[48:51], s85 offen offset:2048 nt lds\n"
    "v_exp_f32 v181, v173\n"
    "v_mfma_f32_16x16x32_bf16 v[160:163], a[156:159], a[60:63], v[160:163]\n"
    "v_exp_f32 v182, v174\n"
    "v_mfma_f32_16x16x32_bf16 v[164:167], a[172:175], a[60:63], v[164:167]\n"
    "buffer_load_dwordx4 v243, s[48:51], s85 offen offset:3072 nt lds\n"
    "v_exp_f32 v183, v175\n"
    "v_mfma_f32_16x16x32_bf16 v[32:35], a[80:83], v[224:227], v[32:35]\n"
    "v_add_f32 v184, v176, v177\n"
    "v_add_f32 v185, v178, v179\n"
    "s_barrier\n"
    "v_mfma_f32_16x16x32_bf16 v[36:39], a[84:87], v[224:227], v[36:39]\n"
    "v_add_f32 v186, v180, v181\n"
    "v_add_f32 v187, v182, v183\n"
    "v_mfma_f32_16x16x32_bf16 v[40:43], a[88:91], v[224:227], v[40:43]\n"
    "v_add_f32 v184, v184, v185\n"
    "v_add_f32 v186, v186, v187\n"
    "v_mfma_f32_16x16x32_bf16 v[44:47], a[92:95], v[224:227], v[44:47]\n"
    "v_add_f32 v184, v184, v186\n"
    "v_cvt_pk_bf16_f32 v228, v176, v177\n"
    "v_mfma_f32_16x16x32_bf16 v[48:51], a[96:99], v[224:227], v[48:51]\n"
    "v_cvt_pk_bf16_f32 v229, v178, v179\n"
    "v_cmp_lt_f32 vcc, s77, v184\n"
    "v_mfma_f32_16x16x32_bf16 v[52:55], a[100:103], v[224:227], v[52:55]\n"
    "v_cvt_pk_bf16_f32 v230, v180, v181\n"
    "v_cvt_pk_bf16_f32 v231, v182, v183\n"
    "v_mfma_f32_16x16x32_bf16 v[56:59], a[104:107], v[224:227], v[56:59]\n"
    "s_cbranch_vccnz L_rareB_e67%=\n"
    "L_rareB_e67_r%=:\n"
    "v_add_f32 v234, v234, v184\n"
    "v_mfma_f32_16x16x32_bf16 v[60:63], a[108:111], v[224:227], v[60:63]\n"
    "v_mfma_f32_16x16x32_bf16 v[168:171], a[144:147], a[64:67], v[208:211]\n"
    "s_waitcnt vmcnt(12)\n"
    "ds_read_b128 a[112:115], v220 offset:0\n"
    "v_exp_f32 v176, v160\n"
    "v_mfma_f32_16x16x32_bf16 v[172:175], a[160:163], a[64:67], v[208:211]\n"
    "s_waitcnt vmcnt(8)\n"
    "ds_read_b128 a[128:131], v220 offset:4096\n"
    "v_exp_f32 v177, v161\n"
    "v_mfma_f32_16x16x32_bf16 v[168:171], a[148:151], a[68:71], v[168:171]\n"
    "ds_read_b128 a[116:119], v221 offset:0\n"
    "v_exp_f32 v178, v162\n"
    "v_mfma_f32_16x16x32_bf16 v[172:175], a[164:167], a[68:71], v[172:175]\n"
    "ds_read_b128 a[132:135], v221 offset:4096\n"
    "v_exp_f32 v179, v163\n"
    "v_mfma_f32_16x16x32_bf16 v[168:171], a[152:155], a[72:75], v[168:171]\n"
    "ds_read_b128 a[120:123], v222 offset:0\n"
    "v_exp_f32 v180, v164\n"
    "v_mfma_f32_16x16x32_bf16 v[172:175], a[168:171], a[72:75], v[172:175]\n"
    "ds_read_b128 a[136:139], v222 offset:4096\n"
    "v_exp_f32 v181, v165\n"
    "v_mfma_f32_16x16x32_bf16 v[168:171], a[156:159], a[76:79], v[168:171]\n"
    "ds_read_b128 a[124:127], v223 offset:0\n"
    "v_exp_f32 v182, v166\n"
    "v_mfma_f32_16x16x32_bf16 v[172:175], a[172:175], a[76:79], v[172:175]\n"
    "ds_read_b128 a[140:143], v223 offset:4096\n"
    "v_exp_f32 v183, v167\n"
    "v_mfma_f32_16x16x32_bf16 v[64:67], a[80:83], v[228:231], v[64:67]\n"
    "v_add_f32 v184, v176, v177\n"
    "v_add_f32 v185, v178, v179\n"
    "v_mfma_f32_16x16x32_bf16 v[68:71], a[84:87], v[228:231], v[68:71]\n"
    "v_add_f32 v186, v180, v181\n"
    "v_add_f32 v187, v182, v183\n"
    "v_mfma_f32_16x16x32_bf16 v[72:75], a[88:91], v[228:231], v[72:75]\n"
    "v_add_f32 v184, v184, v185\n"
    "v_add_f32 v186, v186, v187\n"
    "s_add_u32 m0, s74, 0\n"
    "s_add_u32 s71, s71, 4096\n"
    "s_and_b32 s71, s71, 0x1ffff\n"
    "s_waitcnt lgkmcnt(0)\n"
    "v_mfma_f32_16x16x32_bf16 v[76:79], a[92:95], v[228:231], v[76:79]\n"
    "buffer_load_dwordx4 v188, s[44:47], s71 offen offset:0 nt lds\n"
    "v_add_f32 v184, v184, v186\n"
    "v_cvt_pk_bf16_f32 v224, v176, v177\n"
    "v_mfma_f32_16x16x32_bf16 v[80:83], a[96:99], v[228:231], v[80:83]\n"
    "buffer_load_dwordx4 v189, s[44:47], s71 offen offset:1024 nt lds\n"
    "v_cvt_pk_bf16_f32 v225, v178, v179\n"
    "v_cmp_lt_f32 vcc, s77, v184\n"
    "v_mfma_f32_16x16x32_bf16 v[84:87], a[100:103], v[228:231], v[84:87]\n"
    "buffer_load_dwordx4 v190, s[44:47], s71 offen offset:2048 nt lds\n"
    "v_cvt_pk_bf16_f32 v226, v180, v181\n"
    "v_cvt_pk_bf16_f32 v227, v182, v183\n"
    "v_mfma_f32_16x16x32_bf16 v[88:91], a[104:107], v[228:231], v[88:91]\n"
    "s_cbranch_vccnz L_rareB_e68%=\n"
    "L_rareB_e68_r%=:\n"
    "v_add_f32 v235, v235, v184\n"
    "v_mfma_f32_16x16x32_bf16 v[92:95], a[108:111], v[228:231], v[92:95]\n"
    "buffer_load_dwordx4 v191, s[44:47], s71 offen offset:3072 nt lds\n"
    "v_mfma_f32_16x16x32_bf16 v[160:163], a[112:115], a[0:3], v[192:195]\n"
    "v_exp_f32 v176, v168\n"
    "s_add_u32 m0, s74, 4096\n"
    "s_add_u32 s71, s71, 4096\n"
    "s_and_b32 s71, s71, 0x1ffff\n"
    "v_mfma_f32_16x16x32_bf16 v[164:167], a[128:131], a[0:3], v[192:195]\n"
    "buffer_load_dwordx4 v188, s[44:47], s71 offen offset:0 nt lds\n"
    "v_exp_f32 v177, v169\n"
    "v_mfma_f32_16x16x32_bf16 v[160:163], a[116:119], a[4:7], v[160:163]\n"
    "v_exp_f32 v178, v170\n"
    "v_mfma_f32_16x16x32_bf16 v[164:167], a[132:135], a[4:7], v[164:167]\n"
    "buffer_load_dwordx4 v189, s[44:47], s71 offen offset:1024 nt lds\n"
    "v_exp_f32 v179, v171\n"
    "v_mfma_f32_16x16x32_bf16 v[160:163], a[120:123], a[8:11], v[160:163]\n"
    "v_exp_f32 v180, v172\n"
    "v_mfma_f32_16x16x32_bf16 v[164:167], a[136:139], a[8:11], v[164:167]\n"
    "buffer_load_dwordx4 v190, s[44:47], s71 offen offset:2048 nt lds\n"
    "v_exp_f32 v181, v173\n"
    "v_mfma_f32_16x16x32_bf16 v[160:163], a[124:127], a[12:15], v[160:163]\n"
    "v_exp_f32 v182, v174\n"
    "v_mfma_f32_16x16x32_bf16 v[164:167], a[140:143], a[12:15], v[164:167]\n"
    "buffer_load_dwordx4 v191, s[44:47], s71 offen offset:3072 nt lds\n"
    "v_exp_f32 v183, v175\n"
    "v_mfma_f32_16x16x32_bf16 v[96:99], a[80:83], v[224:227], v[96:99]\n"
    "v_add_f32 v184, v176, v177\n"
    "v_add_f32 v185, v178, v179\n"
    "v_mfma_f32_16x16x32_bf16 v[100:103], a[84:87], v[224:227], v[100:103]\n"
    "v_add_f32 v186, v180, v181\n"
    "v_add_f32 v187, v182, v183\n"
    "v_mfma_f32_16x16x32_bf16 v[104:107], a[88:91], v[224:227], v[104:107]\n"
    "v_add_f32 v184, v184, v185\n"
    "v_add_f32 v186, v186, v187\n"
    "v_mfma_f32_16x16x32_bf16 v[108:111], a[92:95], v[224:227], v[108:111]\n"
    "v_add_f32 v184, v184, v186\n"
    "v_cvt_pk_bf16_f32 v228, v176, v177\n"
    "v_mfma_f32_16x16x32_bf16 v[112:115], a[96:99], v[224:227], v[112:115]\n"
    "v_cvt_pk_bf16_f32 v229, v178, v179\n"
    "v_cmp_lt_f32 vcc, s77, v184\n"
    "v_mfma_f32_16x16x32_bf16 v[116:119], a[100:103], v[224:227], v[116:119]\n"
    "v_cvt_pk_bf16_f32 v230, v180, v181\n"
    "v_cvt_pk_bf16_f32 v231, v182, v183\n"
    "v_mfma_f32_16x16x32_bf16 v[120:123], a[104:107], v[224:227], v[120:123]\n"
    "s_cbranch_vccnz L_rareB_e69%=\n"
    "L_rareB_e69_r%=:\n"
    "v_add_f32 v236, v236, v184\n"
    "v_mfma_f32_16x16x32_bf16 v[124:127], a[108:111], v[224:227], v[124:127]\n"
    "v_mfma_f32_16x16x32_bf16 v[168:171], a[112:115], a[16:19], v[196:199]\n"
    "v_exp_f32 v176, v160\n"
    "v_mfma_f32_16x16x32_bf16 v[172:175], a[128:131], a[16:19], v[196:199]\n"
    "v_exp_f32 v177, v161\n"
    "v_mfma_f32_16x16x32_bf16 v[168:171], a[116:119], a[20:23], v[168:171]\n"
    "v_exp_f32 v178, v162\n"
    "v_mfma_f32_16x16x32_bf16 v[172:175], a[132:135], a[20:23], v[172:175]\n"
    "v_exp_f32 v179, v163\n"
    "v_mfma_f32_16x16x32_bf16 v[168:171], a[120:123], a[24:27], v[168:171]\n"
    "v_exp_f32 v180, v164\n"
    "v_mfma_f32_16x16x32_bf16 v[172:175], a[136:139], a[24:27], v[172:175]\n"
    "v_exp_f32 v181, v165\n"
    "v_mfma_f32_16x16x32_bf16 v[168:171], a[124:127], a[28:31], v[168:171]\n"
    "v_exp_f32 v182, v166\n"
    "v_mfma_f32_16x16x32_bf16 v[172:175], a[140:143], a[28:31], v[172:175]\n"
    "v_exp_f32 v183, v167\n"
    "v_mfma_f32_16x16x32_bf16 v[128:131], a[80:83], v[228:231], v[128:131]\n"
    "s_waitcnt vmcnt(12)\n"
    "ds_read_b64_tr_b16 a[80:81], v212 offset:0\n"
    "v_add_f32 v184, v176, v177\n"
    "v_add_f32 v185, v178, v179\n"
    "v_mfma_f32_16x16x32_bf16 v[132:135], a[84:87], v[228:231], v[132:135]\n"
    "s_waitcnt vmcnt(8)\n"
    "ds_read_b64_tr_b16 a[82:83], v212 offset:4096\n"
    "v_add_f32 v186, v180, v181\n"
    "v_add_f32 v187, v182, v183\n"
    "v_mfma_f32_16x16x32_bf16 v[136:139], a[88:91], v[228:231], v[136:139]\n"
    "ds_read_b64_tr_b16 a[84:85], v213 offset:0\n"
    "v_add_f32 v184, v184, v185\n"
    "v_add_f32 v186, v186, v187\n"
    "v_mfma_f32_16x16x32_bf16 v[140:143], a[92:95], v[228:231], v[140:143]\n"
    "ds_read_b64_tr_b16 a[86:87], v213 offset:4096\n"
    "v_add_f32 v184, v184, v186\n"
    "v_cvt_pk_bf16_f32 v224, v176, v177\n"
    "v_mfma_f32_16x16x32_bf16 v[144:147], a[96:99], v[228:231], v[144:147]\n"
    "ds_read_b64_tr_b16 a[88:89], v214 offset:0\n"
    "v_cvt_pk_bf16_f32 v225, v178, v179\n"
    "v_cmp_lt_f32 vcc, s77, v184\n"
    "v_mfma_f32_16x16x32_bf16 v[148:151], a[100:103], v[228:231], v[148:151]\n"
    "ds_read_b64_tr_b16 a[90:91], v214 offset:4096\n"
    "v_cvt_pk_bf16_f32 v226, v180, v181\n"
    "v_cvt_pk_bf16_f32 v227, v182, v183\n"
    "v_mfma_f32_16x16x32_bf16 v[152:155], a[104:107], v[228:231], v[152:155]\n"
    "ds_read_b64_tr_b16 a[92:93], v215 offset:0\n"
    "s_cbranch_vccnz L_rareB_e70%=\n"
    "L_rareB_e70_r%=:\n"
    "v_add_f32 v232, v232, v184\n"
    "v_mfma_f32_16x16x32_bf16 v[156:159], a[108:111], v[228:231], v[156:159]\n"
    "ds_read_b64_tr_b16 a[94:95], v215 offset:4096\n"
    "v_mfma_f32_16x16x32_bf16 v[160:163], a[112:115], a[32:35], v[200:203]\n"
    "ds_read_b64_tr_b16 a[96:97], v216 offset:0\n"
    "v_exp_f32 v176, v168\n"
    "v_mfma_f32_16x16x32_bf16 v[164:167], a[128:131], a[32:35], v[200:203]\n"
    "ds_read_b64_tr_b16 a[98:99], v216 offset:4096\n"
    "v_exp_f32 v177, v169\n"
    "v_mfma_f32_16x16x32_bf16 v[160:163], a[116:119], a[36:39], v[160:163]\n"
    "ds_read_b64_tr_b16 a[100:101], v217 offset:0\n"
    "v_exp_f32 v178, v170\n"
    "v_mfma_f32_16x16x32_bf16 v[164:167], a[132:135], a[36:39], v[164:167]\n"
    "ds_read_b64_tr_b16 a[102:103], v217 offset:4096\n"
    "v_exp_f32 v179, v171\n"
    "v_mfma_f32_16x16x32_bf16 v[160:163], a[120:123], a[40:43], v[160:163]\n"
    "ds_read_b64_tr_b16 a[104:105], v218 offset:0\n"
    "v_exp_f32 v180, v172\n"
    "v_mfma_f32_16x16x32_bf16 v[164:167], a[136:139], a[40:43], v[164:167]\n"
    "ds_read_b64_tr_b16 a[106:107], v218 offset:4096\n"
    "v_exp_f32 v181, v173\n"
    "v_mfma_f32_16x16x32_bf16 v[160:163], a[124:127], a[44:47], v[160:163]\n"
    "ds_read_b64_tr_b16 a[108:109], v219 offset:0\n"
    "v_exp_f32 v182, v174\n"
    "v_mfma_f32_16x16x32_bf16 v[164:167], a[140:143], a[44:47], v[164:167]\n"
    "ds_read_b64_tr_b16 a[110:111], v219 offset:4096\n"
    "v_exp_f32 v183, v175\n"
    "s_waitcnt lgkmcnt(4)\n"
    "v_mfma_f32_16x16x32_bf16 v[0:3], a[80:83], v[224:227], v[0:3]\n"
    "v_add_f32 v184, v176, v177\n"
    "v_add_f32 v185, v178, v179\n"
    "s_add_u32 m0, s87, 32768\n"
    "s_add_u32 s85, s85, 4096\n"
    "s_and_b32 s85, s85, 0x1ffff\n"
    "v_mfma_f32_16x16x32_bf16 v[4:7], a[84:87], v[224:227], v[4:7]\n"
    "buffer_load_dwordx4 v242, s[48:51], s85 offen offset:0 nt lds\n"
    "v_add_f32 v186, v180, v181\n"
    "v_add_f32 v187, v182, v183\n"
    "v_mfma_f32_16x16x32_bf16 v[8:11], a[88:91], v[224:227], v[8:11]\n"
    "v_add_f32 v184, v184, v185\n"
    "v_add_f32 v186, v186, v187\n"
    "v_mfma_f32_16x16x32_bf16 v[12:15], a[92:95], v[224:227], v[12:15]\n"
    "buffer_load_dwordx4 v243, s[48:51], s85 offen offset:1024 nt lds\n"
    "v_add_f32 v184, v184, v186\n"
    "v_cvt_pk_bf16_f32 v228, v176, v177\n"
    "v_mfma_f32_16x16x32_bf16 v[16:19], a[96:99], v[224:227], v[16:19]\n"
    "v_cvt_pk_bf16_f32 v229, v178, v179\n"
    "v_cmp_lt_f32 vcc, s77, v184\n"
    "v_mfma_f32_16x16x32_bf16 v[20:23], a[100:103], v[224:227], v[20:23]\n"
    "buffer_load_dwordx4 v242, s[48:51], s85 offen offset:2048 nt lds\n"
    "v_cvt_pk_bf16_f32 v230, v180, v181\n"
    "v_cvt_pk_bf16_f32 v231, v182, v183\n"
    "s_waitcnt lgkmcnt(2)\n"
    "v_mfma_f32_16x16x32_bf16 v[24:27], a[104:107], v[224:227], v[24:27]\n"
    "s_cbranch_vccnz L_rareB_e71%=\n"
    "L_rareB_e71_r%=:\n"
    "v_add_f32 v233, v233, v184\n"
    "s_waitcnt lgkmcnt(0)\n"
    "v_mfma_f32_16x16x32_bf16 v[28:31], a[108:111], v[224:227], v[28:31]\n"
    "buffer_load_dwordx4 v243, s[48:51], s85 offen offset:3072 nt lds\n"
    "v_mfma_f32_16x16x32_bf16 v[168:171], a[112:115], a[48:51], v[204:207]\n"
    "v_exp_f32 v176, v160\n"
    "s_add_u32 m0, s87, 36864\n"
    "s_add_u32 s85, s85, 4096\n"
    "s_and_b32 s85, s85, 0x1ffff\n"
    "v_mfma_f32_16x16x32_bf16 v[172:175], a[128:131], a[48:51], v[204:207]\n"
    "buffer_load_dwordx4 v242, s[48:51], s85 offen offset:0 nt lds\n"
    "v_exp_f32 v177, v161\n"
    "v_mfma_f32_16x16x32_bf16 v[168:171], a[116:119], a[52:55], v[168:171]\n"
    "v_exp_f32 v178, v162\n"
    "v_mfma_f32_16x16x32_bf16 v[172:175], a[132:135], a[52:55], v[172:175]\n"
    "buffer_load_dwordx4 v243, s[48:51], s85 offen offset:1024 nt lds\n"
    "v_exp_f32 v179, v163\n"
    "v_mfma_f32_16x16x32_bf16 v[168:171], a[120:123], a[56:59], v[168:171]\n"
    "v_exp_f32 v180, v164\n"
    "v_mfma_f32_16x16x32_bf16 v[172:175], a[136:139], a[56:59], v[172:175]\n"
    "buffer_load_dwordx4 v242, s[48:51], s85 offen offset:2048 nt lds\n"
    "v_exp_f32 v181, v165\n"
    "v_mfma_f32_16x16x32_bf16 v[168:171], a[124:127], a[60:63], v[168:171]\n"
    "v_exp_f32 v182, v166\n"
    "v_mfma_f32_16x16x32_bf16 v[172:175], a[140:143], a[60:63], v[172:175]\n"
    "buffer_load_dwordx4 v243, s[48:51], s85 offen offset:3072 nt lds\n"
    "v_exp_f32 v183, v167\n"
    "v_mfma_f32_16x16x32_bf16 v[32:35], a[80:83], v[228:231], v[32:35]\n"
    "v_add_f32 v184, v176, v177\n"
    "v_add_f32 v185, v178, v179\n"
    "v_mfma_f32_16x16x32_bf16 v[36:39], a[84:87], v[228:231], v[36:39]\n"
    "v_add_f32 v186, v180, v181\n"
    "v_add_f32 v187, v182, v183\n"
    "v_mfma_f32_16x16x32_bf16 v[40:43], a[88:91], v[228:231], v[40:43]\n"
    "v_add_f32 v184, v184, v185\n"
    "v_add_f32 v186, v186, v187\n"
    "v_mfma_f32_16x16x32_bf16 v[44:47], a[92:95], v[228:231], v[44:47]\n"
    "v_add_f32 v184, v184, v186\n"
    "v_cvt_pk_bf16_f32 v224, v176, v177\n"
    "v_mfma_f32_16x16x32_bf16 v[48:51], a[96:99], v[228:231], v[48:51]\n"
    "v_cvt_pk_bf16_f32 v225, v178, v179\n"
    "v_cmp_lt_f32 vcc, s77, v184\n"
    "v_mfma_f32_16x16x32_bf16 v[52:55], a[100:103], v[228:231], v[52:55]\n"
    "v_cvt_pk_bf16_f32 v226, v180, v181\n"
    "v_cvt_pk_bf16_f32 v227, v182, v183\n"
    "v_mfma_f32_16x16x32_bf16 v[56:59], a[104:107], v[228:231], v[56:59]\n"
    "s_cbranch_vccnz L_rareB_e72%=\n"
    "L_rareB_e72_r%=:\n"
    "v_add_f32 v234, v234, v184\n"
    "v_mfma_f32_16x16x32_bf16 v[60:63], a[108:111], v[228:231], v[60:63]\n"
    "v_mfma_f32_16x16x32_bf16 v[160:163], a[112:115], a[64:67], v[208:211]\n"
    "v_exp_f32 v176, v168\n"
    "v_mfma_f32_16x16x32_bf16 v[164:167], a[128:131], a[64:67], v[208:211]\n"
    "v_exp_f32 v177, v169\n"
    "v_mfma_f32_16x16x32_bf16 v[160:163], a[116:119], a[68:71], v[160:163]\n"
    "v_exp_f32 v178, v170\n"
    "v_mfma_f32_16x16x32_bf16 v[164:167], a[132:135], a[68:71], v[164:167]\n"
    "v_exp_f32 v179, v171\n"
    "v_mfma_f32_16x16x32_bf16 v[160:163], a[120:123], a[72:75], v[160:163]\n"
    "v_exp_f32 v180, v172\n"
    "v_mfma_f32_16x16x32_bf16 v[164:167], a[136:139], a[72:75], v[164:167]\n"
    "v_exp_f32 v181, v173\n"
    "v_mfma_f32_16x16x32_bf16 v[160:163], a[124:127], a[76:79], v[160:163]\n"
    "v_exp_f32 v182, v174\n"
    "v_mfma_f32_16x16x32_bf16 v[164:167], a[140:143], a[76:79], v[164:167]\n"
    "v_exp_f32 v183, v175\n"
    "v_mfma_f32_16x16x32_bf16 v[64:67], a[80:83], v[224:227], v[64:67]\n"
    "v_add_f32 v184, v176, v177\n"
    "v_add_f32 v185, v178, v179\n"
    "v_mfma_f32_16x16x32_bf16 v[68:71], a[84:87], v[224:227], v[68:71]\n"
    "v_add_f32 v186, v180, v181\n"
    "v_add_f32 v187, v182, v183\n"
    "v_mfma_f32_16x16x32_bf16 v[72:75], a[88:91], v[224:227], v[72:75]\n"
    "v_add_f32 v184, v184, v185\n"
    "v_add_f32 v186, v186, v187\n"
    "v_mfma_f32_16x16x32_bf16 v[76:79], a[92:95], v[224:227], v[76:79]\n"
    "v_add_f32 v184, v184, v186\n"
    "v_cvt_pk_bf16_f32 v228, v176, v177\n"
    "v_mfma_f32_16x16x32_bf16 v[80:83], a[96:99], v[224:227], v[80:83]\n"
    "v_cvt_pk_bf16_f32 v229, v178, v179\n"
    "v_cmp_lt_f32 vcc, s77, v184\n"
    "v_mfma_f32_16x16x32_bf16 v[84:87], a[100:103], v[224:227], v[84:87]\n"
    "v_cvt_pk_bf16_f32 v230, v180, v181\n"
    "v_cvt_pk_bf16_f32 v231, v182, v183\n"
    "v_mfma_f32_16x16x32_bf16 v[88:91], a[104:107], v[224:227], v[88:91]\n"
    "s_cbranch_vccnz L_rareB_e73%=\n"
    "L_rareB_e73_r%=:\n"
    "v_add_f32 v235, v235, v184\n"
    "v_mfma_f32_16x16x32_bf16 v[92:95], a[108:111], v[224:227], v[92:95]\n"
    "v_mfma_f32_16x16x32_bf16 v[96:99], a[80:83], v[228:231], v[96:99]\n"
    "v_exp_f32 v176, v160\n"
    "v_mfma_f32_16x16x32_bf16 v[100:103], a[84:87], v[228:231], v[100:103]\n"
    "v_exp_f32 v177, v161\n"
    "v_mfma_f32_16x16x32_bf16 v[104:107], a[88:91], v[228:231], v[104:107]\n"
    "v_exp_f32 v178, v162\n"
    "v_mfma_f32_16x16x32_bf16 v[108:111], a[92:95], v[228:231], v[108:111]\n"
    "v_exp_f32 v179, v163\n"
    "v_mfma_f32_16x16x32_bf16 v[112:115], a[96:99], v[228:231], v[112:115]\n"
    "v_exp_f32 v180, v164\n"
    "v_mfma_f32_16x16x32_bf16 v[116:119], a[100:103], v[228:231], v[116:119]\n"
    "v_exp_f32 v181, v165\n"
    "v_mfma_f32_16x16x32_bf16 v[120:123], a[104:107], v[228:231], v[120:123]\n"
    "v_exp_f32 v182, v166\n"
    "v_mfma_f32_16x16x32_bf16 v[124:127], a[108:111], v[228:231], v[124:127]\n"
    "v_exp_f32 v183, v167\n"
    "v_add_f32 v184, v176, v177\n"
    "v_add_f32 v185, v178, v179\n"
    "v_add_f32 v186, v180, v181\n"
    "v_add_f32 v187, v182, v183\n"
    "v_add_f32 v184, v184, v185\n"
    "v_add_f32 v186, v186, v187\n"
    "v_add_f32 v184, v184, v186\n"
    "v_cvt_pk_bf16_f32 v224, v176, v177\n"
    "v_cvt_pk_bf16_f32 v225, v178, v179\n"
    "v_cmp_lt_f32 vcc, s77, v184\n"
    "v_cvt_pk_bf16_f32 v226, v180, v181\n"
    "v_cvt_pk_bf16_f32 v227, v182, v183\n"
    "s_cbranch_vccnz L_rareB_e74%=\n"
    "L_rareB_e74_r%=:\n"
    "v_add_f32 v236, v236, v184\n"
    "v_mfma_f32_16x16x32_bf16 v[128:131], a[80:83], v[224:227], v[128:131]\n"
    "v_add_u32 v212, 32768, v212\n"
    "v_mfma_f32_16x16x32_bf16 v[132:135], a[84:87], v[224:227], v[132:135]\n"
    "v_add_u32 v213, 32768, v213\n"
    "v_mfma_f32_16x16x32_bf16 v[136:139], a[88:91], v[224:227], v[136:139]\n"
    "v_add_u32 v214, 32768, v214\n"
    "v_mfma_f32_16x16x32_bf16 v[140:143], a[92:95], v[224:227], v[140:143]\n"
    "v_add_u32 v215, 32768, v215\n"
    "v_mfma_f32_16x16x32_bf16 v[144:147], a[96:99], v[224:227], v[144:147]\n"
    "v_add_u32 v216, 32768, v216\n"
    "v_mfma_f32_16x16x32_bf16 v[148:151], a[100:103], v[224:227], v[148:151]\n"
    "v_add_u32 v217, 32768, v217\n"
    "v_mfma_f32_16x16x32_bf16 v[152:155], a[104:107], v[224:227], v[152:155]\n"
    "v_add_u32 v218, 32768, v218\n"
    "v_mfma_f32_16x16x32_bf16 v[156:159], a[108:111], v[224:227], v[156:159]\n"
    "v_add_u32 v219, 32768, v219\n"
    "s_waitcnt vmcnt(12)\n"
    "ds_read_b128 a[144:147], v220 offset:0\n"
    "s_waitcnt vmcnt(8)\n"
    "ds_read_b128 a[160:163], v220 offset:4096\n"
    "ds_read_b128 a[148:151], v221 offset:0\n"
    "ds_read_b128 a[164:167], v221 offset:4096\n"
    "ds_read_b128 a[152:155], v222 offset:0\n"
    "ds_read_b128 a[168:171], v222 offset:4096\n"
    "ds_read_b128 a[156:159], v223 offset:0\n"
    "ds_read_b128 a[172:175], v223 offset:4096\n"
    "s_waitcnt lgkmcnt(7)\n"
    "v_mfma_f32_16x16x32_bf16 v[168:171], a[144:147], a[0:3], v[192:195]\n"
    "s_waitcnt lgkmcnt(6)\n"
    "v_mfma_f32_16x16x32_bf16 v[172:175], a[160:163], a[0:3], v[192:195]\n"
    "s_waitcnt lgkmcnt(5)\n"
    "v_mfma_f32_16x16x32_bf16 v[168:171], a[148:151], a[4:7], v[168:171]\n"
    "s_waitcnt lgkmcnt(4)\n"
    "v_mfma_f32_16x16x32_bf16 v[172:175], a[164:167], a[4:7], v[172:175]\n"
    "s_waitcnt lgkmcnt(3)\n"
    "v_mfma_f32_16x16x32_bf16 v[168:171], a[152:155], a[8:11], v[168:171]\n"
    "s_waitcnt lgkmcnt(2)\n"
    "v_mfma_f32_16x16x32_bf16 v[172:175], a[168:171], a[8:11], v[172:175]\n"
    "s_waitcnt lgkmcnt(1)\n"
    "v_mfma_f32_16x16x32_bf16 v[168:171], a[156:159], a[12:15], v[168:171]\n"
    "s_waitcnt lgkmcnt(0)\n"
    "v_mfma_f32_16x16x32_bf16 v[172:175], a[172:175], a[12:15], v[172:175]\n"
    "v_mfma_f32_16x16x32_bf16 v[160:163], a[144:147], a[16:19], v[196:199]\n"
    "s_nop 4\n"
    "v_exp_f32 v176, v168\n"
    "v_mfma_f32_16x16x32_bf16 v[164:167], a[160:163], a[16:19], v[196:199]\n"
    "v_exp_f32 v177, v169\n"
    "v_mfma_f32_16x16x32_bf16 v[160:163], a[148:151], a[20:23], v[160:163]\n"
    "v_exp_f32 v178, v170\n"
    "v_mfma_f32_16x16x32_bf16 v[164:167], a[164:167], a[20:23], v[164:167]\n"
    "v_exp_f32 v179, v171\n"
    "v_mfma_f32_16x16x32_bf16 v[160:163], a[152:155], a[24:27], v[160:163]\n"
    "v_exp_f32 v180, v172\n"
    "v_mfma_f32_16x16x32_bf16 v[164:167], a[168:171], a[24:27], v[164:167]\n"
    "v_exp_f32 v181, v173\n"
    "v_mfma_f32_16x16x32_bf16 v[160:163], a[156:159], a[28:31], v[160:163]\n"
    "v_exp_f32 v182, v174\n"
    "v_mfma_f32_16x16x32_bf16 v[164:167], a[172:175], a[28:31], v[164:167]\n"
    "v_exp_f32 v183, v175\n"
    "v_add_f32 v184, v176, v177\n"
    "v_add_f32 v185, v178, v179\n"
    "v_add_f32 v186, v180, v181\n"
    "v_add_f32 v187, v182, v183\n"
    "v_add_f32 v184, v184, v185\n"
    "v_add_f32 v186, v186, v187\n"
    "v_add_f32 v184, v184, v186\n"
    "v_cvt_pk_bf16_f32 v228, v176, v177\n"
    "v_cvt_pk_bf16_f32 v229, v178, v179\n"
    "v_cmp_lt_f32 vcc, s77, v184\n"
    "v_cvt_pk_bf16_f32 v230, v180, v181\n"
    "v_cvt_pk_bf16_f32 v231, v182, v183\n"
    "s_cbranch_vccnz L_rareB_e75%=\n"
    "L_rareB_e75_r%=:\n"
    "v_add_f32 v232, v232, v184\n"
    "L_tail%=:\n"
    "v_mfma_f32_16x16x32_bf16 v[168:171], a[144:147], a[32:35], v[200:203]\n"
    "v_exp_f32 v176, v160\n"
    "v_mfma_f32_16x16x32_bf16 v[172:175], a[160:163], a[32:35], v[200:203]\n"
    "v_exp_f32 v177, v161\n"
    "v_mfma_f32_16x16x32_bf16 v[168:171], a[148:151], a[36:39], v[168:171]\n"
    "v_exp_f32 v178, v162\n"
    "v_mfma_f32_16x16x32_bf16 v[172:175], a[164:167], a[36:39], v[172:175]\n"
    "v_exp_f32 v179, v163\n"
    "v_mfma_f32_16x16x32_bf16 v[168:171], a[152:155], a[40:43], v[168:171]\n"
    "v_exp_f32 v180, v164\n"
    "v_mfma_f32_16x16x32_bf16 v[172:175], a[168:171], a[40:43], v[172:175]\n"
    "v_exp_f32 v181, v165\n"
    "v_mfma_f32_16x16x32_bf16 v[168:171], a[156:159], a[44:47], v[168:171]\n"
    "v_exp_f32 v182, v166\n"
    "v_mfma_f32_16x16x32_bf16 v[172:175], a[172:175], a[44:47], v[172:175]\n"
    "v_exp_f32 v183, v167\n"
    "v_add_f32 v184, v176, v177\n"
    "v_add_f32 v185, v178, v179\n"
    "v_add_f32 v186, v180, v181\n"
    "v_add_f32 v187, v182, v183\n"
    "v_add_f32 v184, v184, v185\n"
    "v_add_f32 v186, v186, v187\n"
    "v_add_f32 v184, v184, v186\n"
    "v_cvt_pk_bf16_f32 v224, v176, v177\n"
    "v_cvt_pk_bf16_f32 v225, v178, v179\n"
    "v_cmp_lt_f32 vcc, s77, v184\n"
    "v_cvt_pk_bf16_f32 v226, v180, v181\n"
    "v_cvt_pk_bf16_f32 v227, v182, v183\n"
    "s_cbranch_vccnz L_rareT_76%=\n"
    "L_rareT_76_r%=:\n"
    "v_add_f32 v233, v233, v184\n"
    "v_mfma_f32_16x16x32_bf16 v[160:163], a[144:147], a[48:51], v[204:207]\n"
    "v_exp_f32 v176, v168\n"
    "v_mfma_f32_16x16x32_bf16 v[164:167], a[160:163], a[48:51], v[204:207]\n"
    "v_exp_f32 v177, v169\n"
    "v_mfma_f32_16x16x32_bf16 v[160:163], a[148:151], a[52:55], v[160:163]\n"
    "v_exp_f32 v178, v170\n"
    "v_mfma_f32_16x16x32_bf16 v[164:167], a[164:167], a[52:55], v[164:167]\n"
    "v_exp_f32 v179, v171\n"
    "v_mfma_f32_16x16x32_bf16 v[160:163], a[152:155], a[56:59], v[160:163]\n"
    "v_exp_f32 v180, v172\n"
    "v_mfma_f32_16x16x32_bf16 v[164:167], a[168:171], a[56:59], v[164:167]\n"
    "v_exp_f32 v181, v173\n"
    "v_mfma_f32_16x16x32_bf16 v[160:163], a[156:159], a[60:63], v[160:163]\n"
    "v_exp_f32 v182, v174\n"
    "v_mfma_f32_16x16x32_bf16 v[164:167], a[172:175], a[60:63], v[164:167]\n"
    "v_exp_f32 v183, v175\n"
    "v_add_f32 v184, v176, v177\n"
    "v_add_f32 v185, v178, v179\n"
    "v_add_f32 v186, v180, v181\n"
    "v_add_f32 v187, v182, v183\n"
    "v_add_f32 v184, v184, v185\n"
    "v_add_f32 v186, v186, v187\n"
    "v_add_f32 v184, v184, v186\n"
    "v_cvt_pk_bf16_f32 v188, v176, v177\n"
    "v_cvt_pk_bf16_f32 v189, v178, v179\n"
    "v_cmp_lt_f32 vcc, s77, v184\n"
    "v_cvt_pk_bf16_f32 v190, v180, v181\n"
    "v_cvt_pk_bf16_f32 v191, v182, v183\n"
    "s_cbranch_vccnz L_rareT_77%=\n"
    "L_rareT_77_r%=:\n"
    "v_add_f32 v234, v234, v184\n"
    "v_mfma_f32_16x16x32_bf16 v[168:171], a[144:147], a[64:67], v[208:211]\n"
    "v_exp_f32 v176, v160\n"
    "v_mfma_f32_16x16x32_bf16 v[172:175], a[160:163], a[64:67], v[208:211]\n"
    "v_exp_f32 v177, v161\n"
    "v_mfma_f32_16x16x32_bf16 v[168:171], a[148:151], a[68:71], v[168:171]\n"
    "v_exp_f32 v178, v162\n"
    "v_mfma_f32_16x16x32_bf16 v[172:175], a[164:167], a[68:71], v[172:175]\n"
    "v_exp_f32 v179, v163\n"
    "v_mfma_f32_16x16x32_bf16 v[168:171], a[152:155], a[72:75], v[168:171]\n"
    "v_exp_f32 v180, v164\n"
    "v_mfma_f32_16x16x32_bf16 v[172:175], a[168:171], a[72:75], v[172:175]\n"
    "v_exp_f32 v181, v165\n"
    "v_mfma_f32_16x16x32_bf16 v[168:171], a[156:159], a[76:79], v[168:171]\n"
    "v_exp_f32 v182, v166\n"
    "v_mfma_f32_16x16x32_bf16 v[172:175], a[172:175], a[76:79], v[172:175]\n"
    "v_exp_f32 v183, v167\n"
    "v_add_f32 v184, v176, v177\n"
    "v_add_f32 v185, v178, v179\n"
    "v_add_f32 v186, v180, v181\n"
    "v_add_f32 v187, v182, v183\n"
    "v_add_f32 v184, v184, v185\n"
    "v_add_f32 v186, v186, v187\n"
    "v_add_f32 v184, v184, v186\n"
    "v_cvt_pk_bf16_f32 v242, v176, v177\n"
    "v_cvt_pk_bf16_f32 v243, v178, v179\n"
    "v_cmp_lt_f32 vcc, s77, v184\n"
    "v_cvt_pk_bf16_f32 v244, v180, v181\n"
    "v_cvt_pk_bf16_f32 v245, v182, v183\n"
    "s_cbranch_vccnz L_rareT_78%=\n"
    "L_rareT_78_r%=:\n"
    "v_add_f32 v235, v235, v184\n"
    "v_exp_f32 v176, v168\n"
    "v_exp_f32 v177, v169\n"
    "v_exp_f32 v178, v170\n"
    "v_exp_f32 v179, v171\n"
    "v_exp_f32 v180, v172\n"
    "v_exp_f32 v181, v173\n"
    "v_exp_f32 v182, v174\n"
    "v_exp_f32 v183, v175\n"
    "v_add_f32 v184, v176, v177\n"
    "v_add_f32 v185, v178, v179\n"
    "v_add_f32 v186, v180, v181\n"
    "v_add_f32 v187, v182, v183\n"
    "v_add_f32 v184, v184, v185\n"
    "v_add_f32 v186, v186, v187\n"
    "v_add_f32 v184, v184, v186\n"
    "v_cvt_pk_bf16_f32 v164, v176, v177\n"
    "v_cvt_pk_bf16_f32 v165, v178, v179\n"
    "v_cmp_lt_f32 vcc, s77, v184\n"
    "v_cvt_pk_bf16_f32 v166, v180, v181\n"
    "v_cvt_pk_bf16_f32 v167, v182, v183\n"
    "s_cbranch_vccnz L_rareT_79%=\n"
    "L_rareT_79_r%=:\n"
    "v_add_f32 v236, v236, v184\n"
    "s_lshl_b32 s95, %2, 2\n"
    "s_mul_i32 s68, s95, 36864\n"
    "s_add_u32 s69, s68, 18432\n"
    "buffer_load_dwordx4 v[192:195], v248, s[52:55], s68 offen offset:0\n"
    "v_mov_b32 v176, v232\n"
    "v_mov_b32 v177, v233\n"
    "v_mov_b32 v178, v234\n"
    "v_mov_b32 v179, v235\n"
    "v_mov_b32 v180, v236\n"
    "buffer_load_dwordx4 v[196:199], v248, s[52:55], s68 offen offset:1152\n"
    "v_permlane16_swap_b32 v232, v176\n"
    "v_permlane16_swap_b32 v233, v177\n"
    "v_permlane16_swap_b32 v234, v178\n"
    "v_permlane16_swap_b32 v235, v179\n"
    "v_permlane16_swap_b32 v236, v180\n"
    "buffer_load_dwordx4 v[200:203], v248, s[52:55], s68 offen offset:2304\n"
    "v_add_f32 v232, v232, v176\n"
    "v_add_f32 v233, v233, v177\n"
    "v_add_f32 v234, v234, v178\n"
    "v_add_f32 v235, v235, v179\n"
    "v_add_f32 v236, v236, v180\n"
    "buffer_load_dwordx4 v[204:207], v248, s[52:55], s68 offen offset:3456\n"
    "v_mov_b32 v176, v232\n"
    "v_mov_b32 v177, v233\n"
    "v_mov_b32 v178, v234\n"
    "v_mov_b32 v179, v235\n"
    "v_mov_b32 v180, v236\n"
    "buffer_load_dwordx4 v[208:211], v248, s[52:55], s69 offen offset:0\n"
    "v_permlane32_swap_b32 v232, v176\n"
    "v_permlane32_swap_b32 v233, v177\n"
    "v_permlane32_swap_b32 v234, v178\n"
    "v_permlane32_swap_b32 v235, v179\n"
    "v_permlane32_swap_b32 v236, v180\n"
    "buffer_load_dwordx4 v[220:223], v248, s[52:55], s69 offen offset:3456\n"
    "v_add_f32 v232, v232, v176\n"
    "v_add_f32 v233, v233, v177\n"
    "v_add_f32 v234, v234, v178\n"
    "v_add_f32 v235, v235, v179\n"
    "v_add_f32 v236, v236, v180\n"
    "s_waitcnt vmcnt(6)\n"
    "ds_read_b64_tr_b16 a[80:81], v212 offset:0\n"
    "ds_read_b64_tr_b16 a[82:83], v212 offset:4096\n"
    "ds_read_b64_tr_b16 a[84:85], v213 offset:0\n"
    "ds_read_b64_tr_b16 a[86:87], v213 offset:4096\n"
    "ds_read_b64_tr_b16 a[88:89], v214 offset:0\n"
    "ds_read_b64_tr_b16 a[90:91], v214 offset:4096\n"
    "ds_read_b64_tr_b16 a[92:93], v215 offset:0\n"
    "ds_read_b64_tr_b16 a[94:95], v215 offset:4096\n"
    "ds_read_b64_tr_b16 a[96:97], v216 offset:0\n"
    "ds_read_b64_tr_b16 a[98:99], v216 offset:4096\n"
    "ds_read_b64_tr_b16 a[100:101], v217 offset:0\n"
    "ds_read_b64_tr_b16 a[102:103], v217 offset:4096\n"
    "ds_read_b64_tr_b16 a[104:105], v218 offset:0\n"
    "ds_read_b64_tr_b16 a[106:107], v218 offset:4096\n"
    "ds_read_b64_tr_b16 a[108:109], v219 offset:0\n"
    "ds_read_b64_tr_b16 a[110:111], v219 offset:4096\n"
    "v_rcp_f32 v232, v232\n"
    "v_rcp_f32 v233, v233\n"
    "v_rcp_f32 v234, v234\n"
    "v_rcp_f32 v235, v235\n"
    "v_rcp_f32 v236, v236\n"
    "v_lshlrev_b32 v246, 4, v240\n"
    "v_add_u32 v250, s87, v246\n"
    "v_add_u32 v251, s99, v246\n"
    "v_add_u32 v246, 65536, v246\n"
    "s_waitcnt lgkmcnt(14)\n"
    "v_mfma_f32_16x16x32_bf16 v[0:3], a[80:83], v[228:231], v[0:3]\n"
    "s_waitcnt lgkmcnt(12)\n"
    "v_mfma_f32_16x16x32_bf16 v[4:7], a[84:87], v[228:231], v[4:7]\n"
    "buffer_load_dwordx4 v[212:215], v248, s[52:55], s69 offen offset:1152\n"
    "s_waitcnt lgkmcnt(10)\n"
    "v_mfma_f32_16x16x32_bf16 v[8:11], a[88:91], v[228:231], v[8:11]\n"
    "s_waitcnt lgkmcnt(8)\n"
    "v_mfma_f32_16x16x32_bf16 v[12:15], a[92:95], v[228:231], v[12:15]\n"
    "s_waitcnt lgkmcnt(6)\n"
    "v_mfma_f32_16x16x32_bf16 v[16:19], a[96:99], v[228:231], v[16:19]\n"
    "buffer_load_dwordx4 v[216:219], v248, s[52:55], s69 offen offset:2304\n"
    "s_waitcnt lgkmcnt(4)\n"
    "v_mfma_f32_16x16x32_bf16 v[20:23], a[100:103], v[228:231], v[20:23]\n"
    "s_add_u32 s95, s95, 1\n"
    "s_and_b32 s95, s95, 15\n"
    "s_waitcnt lgkmcnt(2)\n"
    "v_mfma_f32_16x16x32_bf16 v[24:27], a[104:107], v[228:231], v[24:27]\n"
    "s_mul_i32 s68, s95, 36864\n"
    "s_add_u32 s69, s68, 18432\n"
    "s_waitcnt lgkmcnt(0)\n"
    "v_mfma_f32_16x16x32_bf16 v[28:31], a[108:111], v[228:231], v[28:31]\n"
    "buffer_load_dwordx4 v[160:163], v248, s[52:55], s68 offen offset:0\n"
    "v_pk_mul_f32 v[0:1], v[0:1], v[232:233] op_sel_hi:[1,0]\n"
    "v_pk_mul_f32 v[2:3], v[2:3], v[232:233] op_sel_hi:[1,0]\n"
    "v_pk_mul_f32 v[4:5], v[4:5], v[232:233] op_sel_hi:[1,0]\n"
    "v_pk_mul_f32 v[6:7], v[6:7], v[232:233] op_sel_hi:[1,0]\n"
    "v_pk_mul_f32 v[8:9], v[8:9], v[232:233] op_sel_hi:[1,0]\n"
    "v_pk_mul_f32 v[10:11], v[10:11], v[232:233] op_sel_hi:[1,0]\n"
    "v_pk_mul_f32 v[12:13], v[12:13], v[232:233] op_sel_hi:[1,0]\n"
    "v_pk_mul_f32 v[14:15], v[14:15], v[232:233] op_sel_hi:[1,0]\n"
    "v_pk_mul_f32 v[16:17], v[16:17], v[232:233] op_sel_hi:[1,0]\n"
    "v_pk_mul_f32 v[18:19], v[18:19], v[232:233] op_sel_hi:[1,0]\n"
    "v_pk_mul_f32 v[20:21], v[20:21], v[232:233] op_sel_hi:[1,0]\n"
    "v_pk_mul_f32 v[22:23], v[22:23], v[232:233] op_sel_hi:[1,0]\n"
    "v_pk_mul_f32 v[24:25], v[24:25], v[232:233] op_sel_hi:[1,0]\n"
    "v_pk_mul_f32 v[26:27], v[26:27], v[232:233] op_sel_hi:[1,0]\n"
    "v_pk_mul_f32 v[28:29], v[28:29], v[232:233] op_sel_hi:[1,0]\n"
    "v_pk_mul_f32 v[30:31], v[30:31], v[232:233] op_sel_hi:[1,0]\n"
    "s_cmp_lg_u32 s7, 0\n"
    "s_cbranch_scc1 L_nob%=\n"
    "s_barrier\n"
    "L_nob%=:\n"
    "v_mfma_f32_16x16x32_bf16 v[32:35], a[80:83], v[224:227], v[32:35]\n"
    "buffer_load_dwordx4 v[168:171], v248, s[52:55], s68 offen offset:2304\n"
    "v_cvt_pk_bf16_f32 v0, v0, v1\n"
    "v_cvt_pk_bf16_f32 v1, v2, v3\n"
    "v_mfma_f32_16x16x32_bf16 v[36:39], a[84:87], v[224:227], v[36:39]\n"
    "v_cvt_pk_bf16_f32 v2, v4, v5\n"
    "v_cvt_pk_bf16_f32 v3, v6, v7\n"
    "v_mfma_f32_16x16x32_bf16 v[40:43], a[88:91], v[224:227], v[40:43]\n"
    "ds_write_b128 v250, v[0:3] offset:0\n"
    "v_cvt_pk_bf16_f32 v8, v8, v9\n"
    "v_cvt_pk_bf16_f32 v9, v10, v11\n"
    "v_mfma_f32_16x16x32_bf16 v[44:47], a[92:95], v[224:227], v[44:47]\n"
    "v_cvt_pk_bf16_f32 v10, v12, v13\n"
    "v_cvt_pk_bf16_f32 v11, v14, v15\n"
    "v_mfma_f32_16x16x32_bf16 v[48:51], a[96:99], v[224:227], v[48:51]\n"
    "ds_write_b128 v250, v[8:11] offset:2048\n"
    "v_cvt_pk_bf16_f32 v16, v16, v17\n"
    "v_cvt_pk_bf16_f32 v17, v18, v19\n"
    "v_mfma_f32_16x16x32_bf16 v[52:55], a[100:103], v[224:227], v[52:55]\n"
    "buffer_load_dwordx4 v[172:175], v248, s[52:55], s68 offen offset:3456\n"
    "v_cvt_pk_bf16_f32 v18, v20, v21\n"
    "v_cvt_pk_bf16_f32 v19, v22, v23\n"
    "v_mfma_f32_16x16x32_bf16 v[56:59], a[104:107], v[224:227], v[56:59]\n"
    "ds_write_b128 v250, v[16:19] offset:4096\n"
    "v_cvt_pk_bf16_f32 v24, v24, v25\n"
    "v_cvt_pk_bf16_f32 v25, v26, v27\n"
    "v_mfma_f32_16x16x32_bf16 v[60:63], a[108:111], v[224:227], v[60:63]\n"
    "v_cvt_pk_bf16_f32 v26, v28, v29\n"
    "v_cvt_pk_bf16_f32 v27, v30, v31\n"
    "v_pk_mul_f32 v[32:33], v[32:33], v[232:233] op_sel:[0,1] op_sel_hi:[1,1]\n"
    "v_pk_mul_f32 v[34:35], v[34:35], v[232:233] op_sel:[0,1] op_sel_hi:[1,1]\n"
    "v_pk_mul_f32 v[36:37], v[36:37], v[232:233] op_sel:[0,1] op_sel_hi:[1,1]\n"
    "v_pk_mul_f32 v[38:39], v[38:39], v[232:233] op_sel:[0,1] op_sel_hi:[1,1]\n"
    "ds_write_b128 v250, v[24:27] offset:6144\n"
    "v_pk_mul_f32 v[40:41], v[40:41], v[232:233] op_sel:[0,1] op_sel_hi:[1,1]\n"
    "v_pk_mul_f32 v[42:43], v[42:43], v[232:233] op_sel:[0,1] op_sel_hi:[1,1]\n"
    "v_pk_mul_f32 v[44:45], v[44:45], v[232:233] op_sel:[0,1] op_sel_hi:[1,1]\n"
    "v_pk_mul_f32 v[46:47], v[46:47], v[232:233] op_sel:[0,1] op_sel_hi:[1,1]\n"
    "v_pk_mul_f32 v[48:49], v[48:49], v[232:233] op_sel:[0,1] op_sel_hi:[1,1]\n"
    "v_pk_mul_f32 v[50:51], v[50:51], v[232:233] op_sel:[0,1] op_sel_hi:[1,1]\n"
    "v_pk_mul_f32 v[52:53], v[52:53], v[232:233] op_sel:[0,1] op_sel_hi:[1,1]\n"
    "v_pk_mul_f32 v[54:55], v[54:55], v[232:233] op_sel:[0,1] op_sel_hi:[1,1]\n"
    "v_pk_mul_f32 v[56:57], v[56:57], v[232:233] op_sel:[0,1] op_sel_hi:[1,1]\n"
    "v_pk_mul_f32 v[58:59], v[58:59], v[232:233] op_sel:[0,1] op_sel_hi:[1,1]\n"
    "v_pk_mul_f32 v[60:61], v[60:61], v[232:233] op_sel:[0,1] op_sel_hi:[1,1]\n"
    "v_pk_mul_f32 v[62:63], v[62:63], v[232:233] op_sel:[0,1] op_sel_hi:[1,1]\n"
    "v_mfma_f32_16x16x32_bf16 v[64:67], a[80:83], v[188:191], v[64:67]\n"
    "buffer_load_dwordx4 v[176:179], v248, s[52:55], s69 offen offset:0\n"
    "v_cvt_pk_bf16_f32 v32, v32, v33\n"
    "v_cvt_pk_bf16_f32 v33, v34, v35\n"
    "v_mfma_f32_16x16x32_bf16 v[68:71], a[84:87], v[188:191], v[68:71]\n"
    "v_cvt_pk_bf16_f32 v34, v36, v37\n"
    "v_cvt_pk_bf16_f32 v35, v38, v39\n"
    "v_mfma_f32_16x16x32_bf16 v[72:75], a[88:91], v[188:191], v[72:75]\n"
    "ds_write_b128 v250, v[32:35] offset:1024\n"
    "v_cvt_pk_bf16_f32 v40, v40, v41\n"
    "v_cvt_pk_bf16_f32 v41, v42, v43\n"
    "v_mfma_f32_16x16x32_bf16 v[76:79], a[92:95], v[188:191], v[76:79]\n"
    "v_cvt_pk_bf16_f32 v42, v44, v45\n"
    "v_cvt_pk_bf16_f32 v43, v46, v47\n"
    "v_mfma_f32_16x16x32_bf16 v[80:83], a[96:99], v[188:191], v[80:83]\n"
    "ds_write_b128 v250, v[40:43] offset:3072\n"
    "v_cvt_pk_bf16_f32 v48, v48, v49\n"
    "v_cvt_pk_bf16_f32 v49, v50, v51\n"
    "v_mfma_f32_16x16x32_bf16 v[84:87], a[100:103], v[188:191], v[84:87]\n"
    "buffer_load_dwordx4 v[180:183], v248, s[52:55], s69 offen offset:1152\n"
    "v_cvt_pk_bf16_f32 v50, v52, v53\n"
    "v_cvt_pk_bf16_f32 v51, v54, v55\n"
    "v_mfma_f32_16x16x32_bf16 v[88:91], a[104:107], v[188:191], v[88:91]\n"
    "ds_write_b128 v250, v[48:51] offset:5120\n"
    "v_cvt_pk_bf16_f32 v56, v56, v57\n"
    "v_cvt_pk_bf16_f32 v57, v58, v59\n"
    "v_mfma_f32_16x16x32_bf16 v[92:95], a[108:111], v[188:191], v[92:95]\n"
    "v_cvt_pk_bf16_f32 v58, v60, v61\n"
    "v_cvt_pk_bf16_f32 v59, v62, v63\n"
    "v_pk_mul_f32 v[64:65], v[64:65], v[234:235] op_sel_hi:[1,0]\n"
    "v_pk_mul_f32 v[66:67], v[66:67], v[234:235] op_sel_hi:[1,0]\n"
    "v_pk_mul_f32 v[68:69], v[68:69], v[234:235] op_sel_hi:[1,0]\n"
    "v_pk_mul_f32 v[70:71], v[70:71], v[234:235] op_sel_hi:[1,0]\n"
    "ds_write_b128 v250, v[56:59] offset:7168\n"
    "v_pk_mul_f32 v[72:73], v[72:73], v[234:235] op_sel_hi:[1,0]\n"
    "v_pk_mul_f32 v[74:75], v[74:75], v[234:235] op_sel_hi:[1,0]\n"
    "v_pk_mul_f32 v[76:77], v[76:77], v[234:235] op_sel_hi:[1,0]\n"
    "v_pk_mul_f32 v[78:79], v[78:79], v[234:235] op_sel_hi:[1,0]\n"
    "v_pk_mul_f32 v[80:81], v[80:81], v[234:235] op_sel_hi:[1,0]\n"
    "v_pk_mul_f32 v[82:83], v[82:83], v[234:235] op_sel_hi:[1,0]\n"
    "v_pk_mul_f32 v[84:85], v[84:85], v[234:235] op_sel_hi:[1,0]\n"
    "v_pk_mul_f32 v[86:87], v[86:87], v[234:235] op_sel_hi:[1,0]\n"
    "v_pk_mul_f32 v[88:89], v[88:89], v[234:235] op_sel_hi:[1,0]\n"
    "v_pk_mul_f32 v[90:91], v[90:91], v[234:235] op_sel_hi:[1,0]\n"
    "v_pk_mul_f32 v[92:93], v[92:93], v[234:235] op_sel_hi:[1,0]\n"
    "v_pk_mul_f32 v[94:95], v[94:95], v[234:235] op_sel_hi:[1,0]\n"
    "v_mfma_f32_16x16x32_bf16 v[96:99], a[80:83], v[242:245], v[96:99]\n"
    "buffer_load_dwordx4 v[184:187], v248, s[52:55], s69 offen offset:2304\n"
    "v_cvt_pk_bf16_f32 v64, v64, v65\n"
    "v_cvt_pk_bf16_f32 v65, v66, v67\n"
    "v_mfma_f32_16x16x32_bf16 v[100:103], a[84:87], v[242:245], v[100:103]\n"
    "v_cvt_pk_bf16_f32 v66, v68, v69\n"
    "v_cvt_pk_bf16_f32 v67, v70, v71\n"
    "v_mfma_f32_16x16x32_bf16 v[104:107], a[88:91], v[242:245], v[104:107]\n"
    "ds_write_b128 v250, v[64:67] offset:32768\n"
    "v_cvt_pk_bf16_f32 v72, v72, v73\n"
    "v_cvt_pk_bf16_f32 v73, v74, v75\n"
    "v_mfma_f32_16x16x32_bf16 v[108:111], a[92:95], v[242:245], v[108:111]\n"
    "v_cvt_pk_bf16_f32 v74, v76, v77\n"
    "v_cvt_pk_bf16_f32 v75, v78, v79\n"
    "v_mfma_f32_16x16x32_bf16 v[112:115], a[96:99], v[242:245], v[112:115]\n"
    "ds_write_b128 v250, v[72:75] offset:34816\n"
    "v_cvt_pk_bf16_f32 v80, v80, v81\n"
    "v_cvt_pk_bf16_f32 v81, v82, v83\n"
    "v_mfma_f32_16x16x32_bf16 v[116:119], a[100:103], v[242:245], v[116:119]\n"
    "v_cvt_pk_bf16_f32 v82, v84, v85\n"
    "v_cvt_pk_bf16_f32 v83, v86, v87\n"
    "v_mfma_f32_16x16x32_bf16 v[120:123], a[104:107], v[242:245], v[120:123]\n"
    "ds_write_b128 v250, v[80:83] offset:36864\n"
    "v_cvt_pk_bf16_f32 v88, v88, v89\n"
    "v_cvt_pk_bf16_f32 v89, v90, v91\n"
    "v_mfma_f32_16x16x32_bf16 v[124:127], a[108:111], v[242:245], v[124:127]\n"
    "v_cvt_pk_bf16_f32 v90, v92, v93\n"
    "v_cvt_pk_bf16_f32 v91, v94, v95\n"
    "v_pk_mul_f32 v[96:97], v[96:97], v[234:235] op_sel:[0,1] op_sel_hi:[1,1]\n"
    "v_pk_mul_f32 v[98:99], v[98:99], v[234:235] op_sel:[0,1] op_sel_hi:[1,1]\n"
    "v_pk_mul_f32 v[100:101], v[100:101], v[234:235] op_sel:[0,1] op_sel_hi:[1,1]\n"
    "v_pk_mul_f32 v[102:103], v[102:103], v[234:235] op_sel:[0,1] op_sel_hi:[1,1]\n"
    "ds_write_b128 v250, v[88:91] offset:38912\n"
    "v_pk_mul_f32 v[104:105], v[104:105], v[234:235] op_sel:[0,1] op_sel_hi:[1,1]\n"
    "v_pk_mul_f32 v[106:107], v[106:107], v[234:235] op_sel:[0,1] op_sel_hi:[1,1]\n"
    "v_pk_mul_f32 v[108:109], v[108:109], v[234:235] op_sel:[0,1] op_sel_hi:[1,1]\n"
    "v_pk_mul_f32 v[110:111], v[110:111], v[234:235] op_sel:[0,1] op_sel_hi:[1,1]\n"
    "v_pk_mul_f32 v[112:113], v[112:113], v[234:235] op_sel:[0,1] op_sel_hi:[1,1]\n"
    "v_pk_mul_f32 v[114:115], v[114:115], v[234:235] op_sel:[0,1] op_sel_hi:[1,1]\n"
    "v_pk_mul_f32 v[116:117], v[116:117], v[234:235] op_sel:[0,1] op_sel_hi:[1,1]\n"
    "v_pk_mul_f32 v[118:119], v[118:119], v[234:235] op_sel:[0,1] op_sel_hi:[1,1]\n"
    "v_pk_mul_f32 v[120:121], v[120:121], v[234:235] op_sel:[0,1] op_sel_hi:[1,1]\n"
    "v_pk_mul_f32 v[122:123], v[122:123], v[234:235] op_sel:[0,1] op_sel_hi:[1,1]\n"
    "v_pk_mul_f32 v[124:125], v[124:125], v[234:235] op_sel:[0,1] op_sel_hi:[1,1]\n"
    "v_pk_mul_f32 v[126:127], v[126:127], v[234:235] op_sel:[0,1] op_sel_hi:[1,1]\n"
    "v_mfma_f32_16x16x32_bf16 v[128:131], a[80:83], v[164:167], v[128:131]\n"
    "buffer_load_dwordx4 v[188:191], v248, s[52:55], s69 offen offset:3456\n"
    "v_cvt_pk_bf16_f32 v96, v96, v97\n"
    "v_cvt_pk_bf16_f32 v97, v98, v99\n"
    "v_mfma_f32_16x16x32_bf16 v[132:135], a[84:87], v[164:167], v[132:135]\n"
    "v_cvt_pk_bf16_f32 v98, v100, v101\n"
    "v_cvt_pk_bf16_f32 v99, v102, v103\n"
    "v_mfma_f32_16x16x32_bf16 v[136:139], a[88:91], v[164:167], v[136:139]\n"
    "ds_write_b128 v250, v[96:99] offset:33792\n"
    "v_cvt_pk_bf16_f32 v104, v104, v105\n"
    "v_cvt_pk_bf16_f32 v105, v106, v107\n"
    "v_mfma_f32_16x16x32_bf16 v[140:143], a[92:95], v[164:167], v[140:143]\n"
    "v_cvt_pk_bf16_f32 v106, v108, v109\n"
    "v_cvt_pk_bf16_f32 v107, v110, v111\n"
    "v_mfma_f32_16x16x32_bf16 v[144:147], a[96:99], v[164:167], v[144:147]\n"
    "ds_write_b128 v250, v[104:107] offset:35840\n"
    "v_cvt_pk_bf16_f32 v112, v112, v113\n"
    "v_cvt_pk_bf16_f32 v113, v114, v115\n"
    "v_mfma_f32_16x16x32_bf16 v[148:151], a[100:103], v[164:167], v[148:151]\n"
    "v_cvt_pk_bf16_f32 v114, v116, v117\n"
    "v_cvt_pk_bf16_f32 v115, v118, v119\n"
    "v_mfma_f32_16x16x32_bf16 v[152:155], a[104:107], v[164:167], v[152:155]\n"
    "ds_write_b128 v250, v[112:115] offset:37888\n"
    "v_cvt_pk_bf16_f32 v120, v120, v121\n"
    "v_cvt_pk_bf16_f32 v121, v122, v123\n"
    "v_mfma_f32_16x16x32_bf16 v[156:159], a[108:111], v[164:167], v[156:159]\n"
    "v_cvt_pk_bf16_f32 v122, v124, v125\n"
    "v_cvt_pk_bf16_f32 v123, v126, v127\n"
    "v_pk_mul_f32 v[128:129], v[128:129], v[236:237] op_sel_hi:[1,0]\n"
    "v_pk_mul_f32 v[130:131], v[130:131], v[236:237] op_sel_hi:[1,0]\n"
    "v_pk_mul_f32 v[132:133], v[132:133], v[236:237] op_sel_hi:[1,0]\n"
    "v_pk_mul_f32 v[134:135], v[134:135], v[236:237] op_sel_hi:[1,0]\n"
    "ds_write_b128 v250, v[120:123] offset:39936\n"
    "v_pk_mul_f32 v[136:137], v[136:137], v[236:237] op_sel_hi:[1,0]\n"
    "v_pk_mul_f32 v[138:139], v[138:139], v[236:237] op_sel_hi:[1,0]\n"
    "v_pk_mul_f32 v[140:141], v[140:141], v[236:237] op_sel_hi:[1,0]\n"
    "v_pk_mul_f32 v[142:143], v[142:143], v[236:237] op_sel_hi:[1,0]\n"
    "v_pk_mul_f32 v[144:145], v[144:145], v[236:237] op_sel_hi:[1,0]\n"
    "v_pk_mul_f32 v[146:147], v[146:147], v[236:237] op_sel_hi:[1,0]\n"
    "v_pk_mul_f32 v[148:149], v[148:149], v[236:237] op_sel_hi:[1,0]\n"
    "v_pk_mul_f32 v[150:151], v[150:151], v[236:237] op_sel_hi:[1,0]\n"
    "v_pk_mul_f32 v[152:153], v[152:153], v[236:237] op_sel_hi:[1,0]\n"
    "v_pk_mul_f32 v[154:155], v[154:155], v[236:237] op_sel_hi:[1,0]\n"
    "v_pk_mul_f32 v[156:157], v[156:157], v[236:237] op_sel_hi:[1,0]\n"
    "v_pk_mul_f32 v[158:159], v[158:159], v[236:237] op_sel_hi:[1,0]\n"
    "buffer_load_dwordx4 v[164:167], v248, s[52:55], s68 offen offset:1152\n"
    "v_cvt_pk_bf16_f32 v128, v128, v129\n"
    "v_cvt_pk_bf16_f32 v129, v130, v131\n"
    "v_cvt_pk_bf16_f32 v130, v132, v133\n"
    "v_cvt_pk_bf16_f32 v131, v134, v135\n"
    "ds_write_b128 v251, v[128:131] offset:0\n"
    "v_cvt_pk_bf16_f32 v136, v136, v137\n"
    "v_cvt_pk_bf16_f32 v137, v138, v139\n"
    "v_cvt_pk_bf16_f32 v138, v140, v141\n"
    "v_cvt_pk_bf16_f32 v139, v142, v143\n"
    "ds_write_b128 v251, v[136:139] offset:2048\n"
    "v_cvt_pk_bf16_f32 v144, v144, v145\n"
    "v_cvt_pk_bf16_f32 v145, v146, v147\n"
    "v_cvt_pk_bf16_f32 v146, v148, v149\n"
    "v_cvt_pk_bf16_f32 v147, v150, v151\n"
    "ds_write_b128 v251, v[144:147] offset:4096\n"
    "v_cvt_pk_bf16_f32 v152, v152, v153\n"
    "v_cvt_pk_bf16_f32 v153, v154, v155\n"
    "v_cvt_pk_bf16_f32 v154, v156, v157\n"
    "v_cvt_pk_bf16_f32 v155, v158, v159\n"
    "ds_write_b128 v251, v[152:155] offset:6144\n"
    "s_add_u32 s95, s95, 1\n"
    "s_and_b32 s95, s95, 15\n"
    "s_mul_i32 s68, s95, 36864\n"
    "s_add_u32 s69, s68, 18432\n"
    "buffer_load_dwordx4 v[128:131], v248, s[52:55], s68 offen offset:0\n"
    "buffer_load_dwordx4 v[132:135], v248, s[52:55], s68 offen offset:1152\n"
    "s_lshl_b32 s94, %2, 2\n"
    "s_lshl_b32 s90, s94, 12\n"
    "v_add_u32 v252, s90, v249\n"
    "s_lshl_b32 s90, s94, 11\n"
    "v_add_u32 v250, s90, v246\n"
    "v_add_u32 v251, 65536, v250\n"
    "s_add_u32 s94, s94, 1\n"
    "s_and_b32 s94, s94, 15\n"
    "s_waitcnt vmcnt(10)\n"
    "ds_read_b128 a[0:3], v250 offset:0\n"
    "ds_read_b128 a[4:7], v250 offset:1024\n"
    "ds_read_b128 a[8:11], v250 offset:32768\n"
    "ds_read_b128 a[12:15], v250 offset:33792\n"
    "ds_read_b128 a[16:19], v251\n"
    "ds_read_b64_tr_b16 a[60:61], v252 offset:0\n"
    "ds_read_b64_tr_b16 a[62:63], v252 offset:2048\n"
    "s_lshl_b32 s90, s94, 12\n"
    "v_add_u32 v252, s90, v249\n"
    "s_lshl_b32 s90, s94, 11\n"
    "v_add_u32 v250, s90, v246\n"
    "v_add_u32 v251, 65536, v250\n"
    "s_add_u32 s94, s94, 1\n"
    "s_and_b32 s94, s94, 15\n"
    "v_perm_b32 v224, v196, v192, s75\n"
    "v_perm_b32 v225, v204, v200, s75\n"
    "v_perm_b32 v226, v212, v208, s75\n"
    "v_perm_b32 v227, v220, v216, s75\n"
    "v_perm_b32 v228, v196, v192, s76\n"
    "v_perm_b32 v229, v204, v200, s76\n"
    "v_perm_b32 v230, v212, v208, s76\n"
    "v_perm_b32 v231, v220, v216, s76\n"
    "s_mov_b32 s82, 4\n"
    "s_and_b32 s77, s33, 3\n"
    "s_lshl_b32 s77, s77, 2\n"
    "s_lshr_b32 s77, 12819, s77\n"
    "s_and_b32 s77, s77, 15\n"
    "s_cmp_lg_u32 s6, 0\n"
    "s_cselect_b32 s77, 99, s77\n"
    "s_waitcnt lgkmcnt(6)\n"
    "v_mfma_f32_16x16x32_bf16 v[0:3], a[0:3], v[224:227], 0\n"
    "v_perm_b32 v232, v197, v193, s75\n"
    "v_perm_b32 v233, v205, v201, s75\n"
    "s_waitcnt lgkmcnt(5)\n"
    "v_mfma_f32_16x16x32_bf16 v[32:35], a[4:7], v[224:227], 0\n"
    "v_perm_b32 v234, v213, v209, s75\n"
    "v_perm_b32 v235, v221, v217, s75\n"
    "s_waitcnt lgkmcnt(4)\n"
    "v_mfma_f32_16x16x32_bf16 v[64:67], a[8:11], v[224:227], 0\n"
    "buffer_load_dwordx4 v[136:139], v248, s[52:55], s68 offen offset:2304\n"
    "s_mul_i32 s70, s5, 92160\n"
    "s_waitcnt lgkmcnt(3)\n"
    "v_mfma_f32_16x16x32_bf16 v[96:99], a[12:15], v[224:227], 0\n"
    "v_perm_b32 v236, v197, v193, s76\n"
    "v_perm_b32 v237, v205, v201, s76\n"
    "s_waitcnt lgkmcnt(2)\n"
    "v_mfma_f32_16x16x32_bf16 a[176:179], a[16:19], v[224:227], 0\n"
    "v_perm_b32 v238, v213, v209, s76\n"
    "v_perm_b32 v239, v221, v217, s76\n"
    "v_mfma_f32_16x16x32_bf16 v[4:7], a[0:3], v[228:231], 0\n"
    "v_and_b32 v245, 15, v240\n"
    "v_lshrrev_b32 v253, 4, v240\n"
    "v_mfma_f32_16x16x32_bf16 v[36:39], a[4:7], v[228:231], 0\n"
    "ds_read_b128 a[20:23], v250 offset:0\n"
    "v_mfma_f32_16x16x32_bf16 v[68:71], a[8:11], v[228:231], 0\n"
    "ds_read_b128 a[24:27], v250 offset:1024\n"
    "v_mfma_f32_16x16x32_bf16 v[100:103], a[12:15], v[228:231], 0\n"
    "buffer_load_dwordx4 v[140:143], v248, s[52:55], s68 offen offset:3456\n"
    "s_mul_i32 s71, %2, 2560\n"
    "v_mfma_f32_16x16x32_bf16 a[180:183], a[16:19], v[228:231], 0\n"
    "ds_read_b128 a[28:31], v250 offset:32768\n"
    "v_mfma_f32_16x16x32_bf16 v[8:11], a[0:3], v[232:235], 0\n"
    "v_perm_b32 v224, v198, v194, s75\n"
    "v_perm_b32 v225, v206, v202, s75\n"
    "v_mfma_f32_16x16x32_bf16 v[40:43], a[4:7], v[232:235], 0\n"
    "v_perm_b32 v226, v214, v210, s75\n"
    "v_perm_b32 v227, v222, v218, s75\n"
    "v_mfma_f32_16x16x32_bf16 v[72:75], a[8:11], v[232:235], 0\n"
    "v_perm_b32 v228, v198, v194, s76\n"
    "v_perm_b32 v229, v206, v202, s76\n"
    "v_mfma_f32_16x16x32_bf16 v[104:107], a[12:15], v[232:235], 0\n"
    "buffer_load_dwordx4 v[144:147], v248, s[52:55], s69 offen offset:0\n"
    "s_add_u32 s71, s71, 81920\n"
    "v_mfma_f32_16x16x32_bf16 a[184:187], a[16:19], v[232:235], 0\n"
    "v_perm_b32 v230, v214, v210, s76\n"
    "v_perm_b32 v231, v222, v218, s76\n"
    "v_mfma_f32_16x16x32_bf16 v[12:15], a[0:3], v[236:239], 0\n"
    "ds_read_b128 a[32:35], v250 offset:33792\n"
    "v_mfma_f32_16x16x32_bf16 v[44:47], a[4:7], v[236:239], 0\n"
    "ds_read_b128 a[36:39], v251\n"
    "v_mfma_f32_16x16x32_bf16 v[76:79], a[8:11], v[236:239], 0\n"
    "ds_read_b64_tr_b16 a[64:65], v252 offset:0\n"
    "v_mfma_f32_16x16x32_bf16 v[108:111], a[12:15], v[236:239], 0\n"
    "ds_read_b64_tr_b16 a[66:67], v252 offset:2048\n"
    "v_mfma_f32_16x16x32_bf16 a[188:191], a[16:19], v[236:239], 0\n"
    "buffer_load_dwordx4 v[148:151], v248, s[52:55], s69 offen offset:1152\n"
    "s_add_u32 s71, s71, s70\n"
    "v_mfma_f32_16x16x32_bf16 v[16:19], a[0:3], v[224:227], 0\n"
    "v_perm_b32 v232, v199, v195, s75\n"
    "v_perm_b32 v233, v207, v203, s75\n"
    "v_mfma_f32_16x16x32_bf16 v[48:51], a[4:7], v[224:227], 0\n"
    "v_perm_b32 v234, v215, v211, s75\n"
    "v_perm_b32 v235, v223, v219, s75\n"
    "v_mfma_f32_16x16x32_bf16 v[80:83], a[8:11], v[224:227], 0\n"
    "v_perm_b32 v236, v199, v195, s76\n"
    "v_perm_b32 v237, v207, v203, s76\n"
    "v_mfma_f32_16x16x32_bf16 v[112:115], a[12:15], v[224:227], 0\n"
    "v_perm_b32 v238, v215, v211, s76\n"
    "v_perm_b32 v239, v223, v219, s76\n"
    "v_mfma_f32_16x16x32_bf16 a[192:195], a[16:19], v[224:227], 0\n"
    "buffer_load_dwordx4 v[152:155], v248, s[52:55], s69 offen offset:2304\n"
    "s_lshl_b32 s72, %2, 8\n"
    "v_mfma_f32_16x16x32_bf16 v[20:23], a[0:3], v[228:231], 0\n"
    "s_lshl_b32 s90, s94, 12\n"
    "v_add_u32 v252, s90, v249\n"
    "v_mfma_f32_16x16x32_bf16 v[52:55], a[4:7], v[228:231], 0\n"
    "s_lshl_b32 s90, s94, 11\n"
    "v_add_u32 v250, s90, v246\n"
    "v_mfma_f32_16x16x32_bf16 v[84:87], a[8:11], v[228:231], 0\n"
    "v_add_u32 v251, 65536, v250\n"
    "s_add_u32 s94, s94, 1\n"
    "v_mfma_f32_16x16x32_bf16 v[116:119], a[12:15], v[228:231], 0\n"
    "s_and_b32 s94, s94, 15\n"
    "v_mfma_f32_16x16x32_bf16 a[196:199], a[16:19], v[228:231], 0\n"
    "v_mfma_f32_16x16x32_bf16 v[24:27], a[0:3], v[232:235], 0\n"
    "buffer_load_dwordx4 v[156:159], v248, s[52:55], s69 offen offset:3456\n"
    "s_add_u32 s95, s95, 1\n"
    "v_mfma_f32_16x16x32_bf16 v[56:59], a[4:7], v[232:235], 0\n"
    "s_and_b32 s95, s95, 15\n"
    "s_mul_i32 s68, s95, 36864\n"
    "v_mfma_f32_16x16x32_bf16 v[88:91], a[8:11], v[232:235], 0\n"
    "v_lshlrev_b32 v244, 4, v245\n"
    "v_lshl_add_u32 v244, v253, 12, v244\n"
    "v_mfma_f32_16x16x32_bf16 v[120:123], a[12:15], v[232:235], 0\n"
    "s_add_u32 s69, s68, 18432\n"
    "v_mfma_f32_16x16x32_bf16 a[200:203], a[16:19], v[232:235], 0\n"
    "s_waitcnt vmcnt(8)\n"
    "v_mfma_f32_16x16x32_bf16 v[28:31], a[0:3], v[236:239], 0\n"
    "v_perm_b32 v224, v164, v160, s75\n"
    "v_perm_b32 v225, v172, v168, s75\n"
    "v_mfma_f32_16x16x32_bf16 v[60:63], a[4:7], v[236:239], 0\n"
    "buffer_load_dwordx4 v[192:195], v248, s[52:55], s68 offen offset:0\n"
    "s_add_u32 s70, s70, s72\n"
    "v_mfma_f32_16x16x32_bf16 v[92:95], a[8:11], v[236:239], 0\n"
    "v_perm_b32 v226, v180, v176, s75\n"
    "v_perm_b32 v227, v188, v184, s75\n"
    "v_mfma_f32_16x16x32_bf16 v[124:127], a[12:15], v[236:239], 0\n"
    "v_perm_b32 v228, v164, v160, s76\n"
    "v_perm_b32 v229, v172, v168, s76\n"
    "v_mfma_f32_16x16x32_bf16 a[204:207], a[16:19], v[236:239], 0\n"
    "v_perm_b32 v230, v180, v176, s76\n"
    "v_perm_b32 v231, v188, v184, s76\n"
    "s_waitcnt lgkmcnt(7)\n"
    "v_mfma_f32_16x16x32_bf16 a[112:115], a[60:63], a[0:3], 0\n"
    "v_lshlrev_b32 v245, 5, v245\n"
    "v_lshl_add_u32 v245, v253, 3, v245\n"
    "v_mfma_f32_16x16x32_bf16 a[116:119], a[60:63], a[4:7], 0\n"
    "buffer_load_dwordx4 v[196:199], v248, s[52:55], s68 offen offset:1152\n"
    "s_lshl_b32 s73, s5, 7\n"
    "v_mfma_f32_16x16x32_bf16 a[120:123], a[60:63], a[8:11], 0\n"
    "s_waitcnt lgkmcnt(0)\n"
    "v_mfma_f32_16x16x32_bf16 a[124:127], a[60:63], a[12:15], 0\n"
    "v_and_b32 v241, 7, v240\n"
    "v_lshl_add_u32 v241, v241, 3, s73\n"
    "v_mfma_f32_16x16x32_bf16 a[128:131], a[60:63], a[16:19], 0\n"
    "L_p3%=:\n"
    "v_mfma_f32_16x16x32_bf16 v[0:3], a[20:23], v[224:227], v[0:3]\n"
    "v_perm_b32 v232, v165, v161, s75\n"
    "v_perm_b32 v233, v173, v169, s75\n"
    "v_mfma_f32_16x16x32_bf16 v[32:35], a[24:27], v[224:227], v[32:35]\n"
    "v_perm_b32 v234, v181, v177, s75\n"
    "v_perm_b32 v235, v189, v185, s75\n"
    "v_mfma_f32_16x16x32_bf16 v[64:67], a[28:31], v[224:227], v[64:67]\n"
    "buffer_load_dwordx4 v[200:203], v248, s[52:55], s68 offen offset:2304\n"
    "v_mfma_f32_16x16x32_bf16 v[96:99], a[32:35], v[224:227], v[96:99]\n"
    "v_perm_b32 v236, v165, v161, s76\n"
    "v_perm_b32 v237, v173, v169, s76\n"
    "v_mfma_f32_16x16x32_bf16 a[176:179], a[36:39], v[224:227], a[176:179]\n"
    "v_perm_b32 v238, v181, v177, s76\n"
    "v_perm_b32 v239, v189, v185, s76\n"
    "v_mfma_f32_16x16x32_bf16 v[4:7], a[20:23], v[228:231], v[4:7]\n"
    "v_mfma_f32_16x16x32_bf16 v[36:39], a[24:27], v[228:231], v[36:39]\n"
    "ds_read_b128 a[40:43], v250 offset:0\n"
    "v_mfma_f32_16x16x32_bf16 v[68:71], a[28:31], v[228:231], v[68:71]\n"
    "ds_read_b128 a[44:47], v250 offset:1024\n"
    "v_mfma_f32_16x16x32_bf16 v[100:103], a[32:35], v[228:231], v[100:103]\n"
    "buffer_load_dwordx4 v[204:207], v248, s[52:55], s68 offen offset:3456\n"
    "v_mfma_f32_16x16x32_bf16 a[180:183], a[36:39], v[228:231], a[180:183]\n"
    "ds_read_b128 a[48:51], v250 offset:32768\n"
    "v_mfma_f32_16x16x32_bf16 v[8:11], a[20:23], v[232:235], v[8:11]\n"
    "v_perm_b32 v224, v166, v162, s75\n"
    "v_perm_b32 v225, v174, v170, s75\n"
    "v_mfma_f32_16x16x32_bf16 v[40:43], a[24:27], v[232:235], v[40:43]\n"
    "v_perm_b32 v226, v182, v178, s75\n"
    "v_perm_b32 v227, v190, v186, s75\n"
    "v_mfma_f32_16x16x32_bf16 v[72:75], a[28:31], v[232:235], v[72:75]\n"
    "v_perm_b32 v228, v166, v162, s76\n"
    "v_perm_b32 v229, v174, v170, s76\n"
    "v_mfma_f32_16x16x32_bf16 v[104:107], a[32:35], v[232:235], v[104:107]\n"
    "buffer_load_dwordx4 v[208:211], v248, s[52:55], s69 offen offset:0\n"
    "v_mfma_f32_16x16x32_bf16 a[184:187], a[36:39], v[232:235], a[184:187]\n"
    "v_perm_b32 v230, v182, v178, s76\n"
    "v_perm_b32 v231, v190, v186, s76\n"
    "v_mfma_f32_16x16x32_bf16 v[12:15], a[20:23], v[236:239], v[12:15]\n"
    "ds_read_b128 a[52:55], v250 offset:33792\n"
    "v_mfma_f32_16x16x32_bf16 v[44:47], a[24:27], v[236:239], v[44:47]\n"
    "ds_read_b128 a[56:59], v251\n"
    "v_mfma_f32_16x16x32_bf16 v[76:79], a[28:31], v[236:239], v[76:79]\n"
    "ds_read_b64_tr_b16 a[68:69], v252 offset:0\n"
    "v_mfma_f32_16x16x32_bf16 v[108:111], a[32:35], v[236:239], v[108:111]\n"
    "ds_read_b64_tr_b16 a[70:71], v252 offset:2048\n"
    "v_mfma_f32_16x16x32_bf16 a[188:191], a[36:39], v[236:239], a[188:191]\n"
    "buffer_load_dwordx4 v[212:215], v248, s[52:55], s69 offen offset:1152\n"
    "v_mfma_f32_16x16x32_bf16 v[16:19], a[20:23], v[224:227], v[16:19]\n"
    "v_perm_b32 v232, v167, v163, s75\n"
    "v_perm_b32 v233, v175, v171, s75\n"
    "v_mfma_f32_16x16x32_bf16 v[48:51], a[24:27], v[224:227], v[48:51]\n"
    "v_perm_b32 v234, v183, v179, s75\n"
    "v_perm_b32 v235, v191, v187, s75\n"
    "v_mfma_f32_16x16x32_bf16 v[80:83], a[28:31], v[224:227], v[80:83]\n"
    "v_perm_b32 v236, v167, v163, s76\n"
    "v_perm_b32 v237, v175, v171, s76\n"
    "v_mfma_f32_16x16x32_bf16 v[112:115], a[32:35], v[224:227], v[112:115]\n"
    "v_perm_b32 v238, v183, v179, s76\n"
    "v_perm_b32 v239, v191, v187, s76\n"
    "v_mfma_f32_16x16x32_bf16 a[192:195], a[36:39], v[224:227], a[192:195]\n"
    "buffer_load_dwordx4 v[216:219], v248, s[52:55], s69 offen offset:2304\n"
    "v_mfma_f32_16x16x32_bf16 v[20:23], a[20:23], v[228:231], v[20:23]\n"
    "s_lshl_b32 s90, s94, 12\n"
    "v_add_u32 v252, s90, v249\n"
    "v_mfma_f32_16x16x32_bf16 v[52:55], a[24:27], v[228:231], v[52:55]\n"
    "s_lshl_b32 s90, s94, 11\n"
    "v_add_u32 v250, s90, v246\n"
    "v_mfma_f32_16x16x32_bf16 v[84:87], a[28:31], v[228:231], v[84:87]\n"
    "v_add_u32 v251, 65536, v250\n"
    "s_add_u32 s94, s94, 1\n"
    "v_mfma_f32_16x16x32_bf16 v[116:119], a[32:35], v[228:231], v[116:119]\n"
    "s_and_b32 s94, s94, 15\n"
    "v_mfma_f32_16x16x32_bf16 a[196:199], a[36:39], v[228:231], a[196:199]\n"
    "v_mfma_f32_16x16x32_bf16 v[24:27], a[20:23], v[232:235], v[24:27]\n"
    "buffer_load_dwordx4 v[220:223], v248, s[52:55], s69 offen offset:3456\n"
    "s_add_u32 s95, s95, 1\n"
    "v_mfma_f32_16x16x32_bf16 v[56:59], a[24:27], v[232:235], v[56:59]\n"
    "s_and_b32 s95, s95, 15\n"
    "s_mul_i32 s68, s95, 36864\n"
    "v_mfma_f32_16x16x32_bf16 v[88:91], a[28:31], v[232:235], v[88:91]\n"
    "v_mfma_f32_16x16x32_bf16 v[120:123], a[32:35], v[232:235], v[120:123]\n"
    "s_add_u32 s69, s68, 18432\n"
    "v_mfma_f32_16x16x32_bf16 a[200:203], a[36:39], v[232:235], a[200:203]\n"
    "s_waitcnt vmcnt(8)\n"
    "v_mfma_f32_16x16x32_bf16 v[28:31], a[20:23], v[236:239], v[28:31]\n"
    "v_perm_b32 v224, v132, v128, s75\n"
    "v_perm_b32 v225, v140, v136, s75\n"
    "v_mfma_f32_16x16x32_bf16 v[60:63], a[24:27], v[236:239], v[60:63]\n"
    "buffer_load_dwordx4 v[160:163], v248, s[52:55], s68 offen offset:0\n"
    "s_cmp_ge_u32 s82, s77\n"
    "v_mfma_f32_16x16x32_bf16 v[92:95], a[28:31], v[236:239], v[92:95]\n"
    "v_perm_b32 v226, v148, v144, s75\n"
    "v_perm_b32 v227, v156, v152, s75\n"
    "v_mfma_f32_16x16x32_bf16 v[124:127], a[32:35], v[236:239], v[124:127]\n"
    "v_perm_b32 v228, v132, v128, s76\n"
    "v_perm_b32 v229, v140, v136, s76\n"
    "v_mfma_f32_16x16x32_bf16 a[204:207], a[36:39], v[236:239], a[204:207]\n"
    "v_perm_b32 v230, v148, v144, s76\n"
    "v_perm_b32 v231, v156, v152, s76\n"
    "v_mfma_f32_16x16x32_bf16 a[112:115], a[64:67], a[20:23], a[112:115]\n"
    "s_cbranch_scc1 L_no1_%=\n"
    "L_no1r_%=:\n"
    "v_mfma_f32_16x16x32_bf16 a[116:119], a[64:67], a[24:27], a[116:119]\n"
    "buffer_load_dwordx4 v[164:167], v248, s[52:55], s68 offen offset:1152\n"
    "v_mfma_f32_16x16x32_bf16 a[120:123], a[64:67], a[28:31], a[120:123]\n"
    "s_waitcnt lgkmcnt(0)\n"
    "v_mfma_f32_16x16x32_bf16 a[124:127], a[64:67], a[32:35], a[124:127]\n"
    "v_mfma_f32_16x16x32_bf16 a[128:131], a[64:67], a[36:39], a[128:131]\n"
    "v_mfma_f32_16x16x32_bf16 v[0:3], a[40:43], v[224:227], v[0:3]\n"
    "v_perm_b32 v232, v133, v129, s75\n"
    "v_perm_b32 v233, v141, v137, s75\n"
    "v_mfma_f32_16x16x32_bf16 v[32:35], a[44:47], v[224:227], v[32:35]\n"
    "v_perm_b32 v234, v149, v145, s75\n"
    "v_perm_b32 v235, v157, v153, s75\n"
    "v_mfma_f32_16x16x32_bf16 v[64:67], a[48:51], v[224:227], v[64:67]\n"
    "buffer_load_dwordx4 v[168:171], v248, s[52:55], s68 offen offset:2304\n"
    "v_mfma_f32_16x16x32_bf16 v[96:99], a[52:55], v[224:227], v[96:99]\n"
    "v_perm_b32 v236, v133, v129, s76\n"
    "v_perm_b32 v237, v141, v137, s76\n"
    "v_mfma_f32_16x16x32_bf16 a[176:179], a[56:59], v[224:227], a[176:179]\n"
    "v_perm_b32 v238, v149, v145, s76\n"
    "v_perm_b32 v239, v157, v153, s76\n"
    "v_mfma_f32_16x16x32_bf16 v[4:7], a[40:43], v[228:231], v[4:7]\n"
    "v_mfma_f32_16x16x32_bf16 v[36:39], a[44:47], v[228:231], v[36:39]\n"
    "ds_read_b128 a[0:3], v250 offset:0\n"
    "v_mfma_f32_16x16x32_bf16 v[68:71], a[48:51], v[228:231], v[68:71]\n"
    "ds_read_b128 a[4:7], v250 offset:1024\n"
    "v_mfma_f32_16x16x32_bf16 v[100:103], a[52:55], v[228:231], v[100:103]\n"
    "buffer_load_dwordx4 v[172:175], v248, s[52:55], s68 offen offset:3456\n"
    "v_mfma_f32_16x16x32_bf16 a[180:183], a[56:59], v[228:231], a[180:183]\n"
    "ds_read_b128 a[8:11], v250 offset:32768\n"
    "v_mfma_f32_16x16x32_bf16 v[8:11], a[40:43], v[232:235], v[8:11]\n"
    "v_perm_b32 v224, v134, v130, s75\n"
    "v_perm_b32 v225, v142, v138, s75\n"
    "v_mfma_f32_16x16x32_bf16 v[40:43], a[44:47], v[232:235], v[40:43]\n"
    "v_perm_b32 v226, v150, v146, s75\n"
    "v_perm_b32 v227, v158, v154, s75\n"
    "v_mfma_f32_16x16x32_bf16 v[72:75], a[48:51], v[232:235], v[72:75]\n"
    "v_perm_b32 v228, v134, v130, s76\n"
    "v_perm_b32 v229, v142, v138, s76\n"
    "v_mfma_f32_16x16x32_bf16 v[104:107], a[52:55], v[232:235], v[104:107]\n"
    "buffer_load_dwordx4 v[176:179], v248, s[52:55], s69 offen offset:0\n"
    "v_mfma_f32_16x16x32_bf16 a[184:187], a[56:59], v[232:235], a[184:187]\n"
    "v_perm_b32 v230, v150, v146, s76\n"
    "v_perm_b32 v231, v158, v154, s76\n"
    "v_mfma_f32_16x16x32_bf16 v[12:15], a[40:43], v[236:239], v[12:15]\n"
    "ds_read_b128 a[12:15], v250 offset:33792\n"
    "v_mfma_f32_16x16x32_bf16 v[44:47], a[44:47], v[236:239], v[44:47]\n"
    "ds_read_b128 a[16:19], v251\n"
    "v_mfma_f32_16x16x32_bf16 v[76:79], a[48:51], v[236:239], v[76:79]\n"
    "ds_read_b64_tr_b16 a[60:61], v252 offset:0\n"
    "v_mfma_f32_16x16x32_bf16 v[108:111], a[52:55], v[236:239], v[108:111]\n"
    "ds_read_b64_tr_b16 a[62:63], v252 offset:2048\n"
    "v_mfma_f32_16x16x32_bf16 a[188:191], a[56:59], v[236:239], a[188:191]\n"
    "buffer_load_dwordx4 v[180:183], v248, s[52:55], s69 offen offset:1152\n"
    "v_mfma_f32_16x16x32_bf16 v[16:19], a[40:43], v[224:227], v[16:19]\n"
    "v_perm_b32 v232, v135, v131, s75\n"
    "v_perm_b32 v233, v143, v139, s75\n"
    "v_mfma_f32_16x16x32_bf16 v[48:51], a[44:47], v[224:227], v[48:51]\n"
    "v_perm_b32 v234, v151, v147, s75\n"
    "v_perm_b32 v235, v159, v155, s75\n"
    "v_mfma_f32_16x16x32_bf16 v[80:83], a[48:51], v[224:227], v[80:83]\n"
    "v_perm_b32 v236, v135, v131, s76\n"
    "v_perm_b32 v237, v143, v139, s76\n"
    "v_mfma_f32_16x16x32_bf16 v[112:115], a[52:55], v[224:227], v[112:115]\n"
    "v_perm_b32 v238, v151, v147, s76\n"
    "v_perm_b32 v239, v159, v155, s76\n"
    "v_mfma_f32_16x16x32_bf16 a[192:195], a[56:59], v[224:227], a[192:195]\n"
    "buffer_load_dwordx4 v[184:187], v248, s[52:55], s69 offen offset:2304\n"
    "v_mfma_f32_16x16x32_bf16 v[20:23], a[40:43], v[228:231], v[20:23]\n"
    "s_lshl_b32 s90, s94, 12\n"
    "v_add_u32 v252, s90, v249\n"
    "v_mfma_f32_16x16x32_bf16 v[52:55], a[44:47], v[228:231], v[52:55]\n"
    "s_lshl_b32 s90, s94, 11\n"
    "v_add_u32 v250, s90, v246\n"
    "v_mfma_f32_16x16x32_bf16 v[84:87], a[48:51], v[228:231], v[84:87]\n"
    "v_add_u32 v251, 65536, v250\n"
    "s_add_u32 s94, s94, 1\n"
    "v_mfma_f32_16x16x32_bf16 v[116:119], a[52:55], v[228:231], v[116:119]\n"
    "s_and_b32 s94, s94, 15\n"
    "v_mfma_f32_16x16x32_bf16 a[196:199], a[56:59], v[228:231], a[196:199]\n"
    "v_mfma_f32_16x16x32_bf16 v[24:27], a[40:43], v[232:235], v[24:27]\n"
    "buffer_load_dwordx4 v[188:191], v248, s[52:55], s69 offen offset:3456\n"
    "s_add_u32 s95, s95, 1\n"
    "v_mfma_f32_16x16x32_bf16 v[56:59], a[44:47], v[232:235], v[56:59]\n"
    "s_and_b32 s95, s95, 15\n"
    "s_mul_i32 s68, s95, 36864\n"
    "v_mfma_f32_16x16x32_bf16 v[88:91], a[48:51], v[232:235], v[88:91]\n"
    "v_mfma_f32_16x16x32_bf16 v[120:123], a[52:55], v[232:235], v[120:123]\n"
    "s_add_u32 s69, s68, 18432\n"
    "v_mfma_f32_16x16x32_bf16 a[200:203], a[56:59], v[232:235], a[200:203]\n"
    "s_waitcnt vmcnt(8)\n"
    "v_mfma_f32_16x16x32_bf16 v[28:31], a[40:43], v[236:239], v[28:31]\n"
    "v_perm_b32 v224, v196, v192, s75\n"
    "v_perm_b32 v225, v204, v200, s75\n"
    "v_mfma_f32_16x16x32_bf16 v[60:63], a[44:47], v[236:239], v[60:63]\n"
    "buffer_load_dwordx4 v[128:131], v248, s[52:55], s68 offen offset:0\n"
    "s_cmp_ge_u32 s82, s77\n"
    "v_mfma_f32_16x16x32_bf16 v[92:95], a[48:51], v[236:239], v[92:95]\n"
    "v_perm_b32 v226, v212, v208, s75\n"
    "v_perm_b32 v227, v220, v216, s75\n"
    "v_mfma_f32_16x16x32_bf16 v[124:127], a[52:55], v[236:239], v[124:127]\n"
    "v_perm_b32 v228, v196, v192, s76\n"
    "v_perm_b32 v229, v204, v200, s76\n"
    "v_mfma_f32_16x16x32_bf16 a[204:207], a[56:59], v[236:239], a[204:207]\n"
    "v_perm_b32 v230, v212, v208, s76\n"
    "v_perm_b32 v231, v220, v216, s76\n"
    "v_mfma_f32_16x16x32_bf16 a[112:115], a[68:71], a[40:43], a[112:115]\n"
    "s_cbranch_scc1 L_no2_%=\n"
    "L_no2r_%=:\n"
    "v_mfma_f32_16x16x32_bf16 a[116:119], a[68:71], a[44:47], a[116:119]\n"
    "buffer_load_dwordx4 v[132:135], v248, s[52:55], s68 offen offset:1152\n"
    "v_mfma_f32_16x16x32_bf16 a[120:123], a[68:71], a[48:51], a[120:123]\n"
    "s_waitcnt lgkmcnt(0)\n"
    "v_mfma_f32_16x16x32_bf16 a[124:127], a[68:71], a[52:55], a[124:127]\n"
    "v_mfma_f32_16x16x32_bf16 a[128:131], a[68:71], a[56:59], a[128:131]\n"
    "v_mfma_f32_16x16x32_bf16 v[0:3], a[0:3], v[224:227], v[0:3]\n"
    "v_perm_b32 v232, v197, v193, s75\n"
    "v_perm_b32 v233, v205, v201, s75\n"
    "v_mfma_f32_16x16x32_bf16 v[32:35], a[4:7], v[224:227], v[32:35]\n"
    "v_perm_b32 v234, v213, v209, s75\n"
    "v_perm_b32 v235, v221, v217, s75\n"
    "v_mfma_f32_16x16x32_bf16 v[64:67], a[8:11], v[224:227], v[64:67]\n"
    "buffer_load_dwordx4 v[136:139], v248, s[52:55], s68 offen offset:2304\n"
    "s_cmp_eq_u32 s82, 4\n"
    "v_mfma_f32_16x16x32_bf16 v[96:99], a[12:15], v[224:227], v[96:99]\n"
    "v_perm_b32 v236, v197, v193, s76\n"
    "v_perm_b32 v237, v205, v201, s76\n"
    "v_mfma_f32_16x16x32_bf16 a[176:179], a[16:19], v[224:227], a[176:179]\n"
    "v_perm_b32 v238, v213, v209, s76\n"
    "v_perm_b32 v239, v221, v217, s76\n"
    "v_mfma_f32_16x16x32_bf16 v[4:7], a[0:3], v[228:231], v[4:7]\n"
    "s_cbranch_scc1 L_p3b%=\n"
    "L_p3br%=:\n"
    "v_mfma_f32_16x16x32_bf16 v[36:39], a[4:7], v[228:231], v[36:39]\n"
    "ds_read_b128 a[20:23], v250 offset:0\n"
    "v_mfma_f32_16x16x32_bf16 v[68:71], a[8:11], v[228:231], v[68:71]\n"
    "ds_read_b128 a[24:27], v250 offset:1024\n"
    "v_mfma_f32_16x16x32_bf16 v[100:103], a[12:15], v[228:231], v[100:103]\n"
    "buffer_load_dwordx4 v[140:143], v248, s[52:55], s68 offen offset:3456\n"
    "v_mfma_f32_16x16x32_bf16 a[180:183], a[16:19], v[228:231], a[180:183]\n"
    "ds_read_b128 a[28:31], v250 offset:32768\n"
    "v_mfma_f32_16x16x32_bf16 v[8:11], a[0:3], v[232:235], v[8:11]\n"
    "v_perm_b32 v224, v198, v194, s75\n"
    "v_perm_b32 v225, v206, v202, s75\n"
    "v_mfma_f32_16x16x32_bf16 v[40:43], a[4:7], v[232:235], v[40:43]\n"
    "v_perm_b32 v226, v214, v210, s75\n"
    "v_perm_b32 v227, v222, v218, s75\n"
    "v_mfma_f32_16x16x32_bf16 v[72:75], a[8:11], v[232:235], v[72:75]\n"
    "v_perm_b32 v228, v198, v194, s76\n"
    "v_perm_b32 v229, v206, v202, s76\n"
    "v_mfma_f32_16x16x32_bf16 v[104:107], a[12:15], v[232:235], v[104:107]\n"
    "buffer_load_dwordx4 v[144:147], v248, s[52:55], s69 offen offset:0\n"
    "v_mfma_f32_16x16x32_bf16 a[184:187], a[16:19], v[232:235], a[184:187]\n"
    "v_perm_b32 v230, v214, v210, s76\n"
    "v_perm_b32 v231, v222, v218, s76\n"
    "v_mfma_f32_16x16x32_bf16 v[12:15], a[0:3], v[236:239], v[12:15]\n"
    "ds_read_b128 a[32:35], v250 offset:33792\n"
    "v_mfma_f32_16x16x32_bf16 v[44:47], a[4:7], v[236:239], v[44:47]\n"
    "ds_read_b128 a[36:39], v251\n"
    "v_mfma_f32_16x16x32_bf16 v[76:79], a[8:11], v[236:239], v[76:79]\n"
    "ds_read_b64_tr_b16 a[64:65], v252 offset:0\n"
    "v_mfma_f32_16x16x32_bf16 v[108:111], a[12:15], v[236:239], v[108:111]\n"
    "ds_read_b64_tr_b16 a[66:67], v252 offset:2048\n"
    "v_mfma_f32_16x16x32_bf16 a[188:191], a[16:19], v[236:239], a[188:191]\n"
    "buffer_load_dwordx4 v[148:151], v248, s[52:55], s69 offen offset:1152\n"
    "v_mfma_f32_16x16x32_bf16 v[16:19], a[0:3], v[224:227], v[16:19]\n"
    "v_perm_b32 v232, v199, v195, s75\n"
    "v_perm_b32 v233, v207, v203, s75\n"
    "v_mfma_f32_16x16x32_bf16 v[48:51], a[4:7], v[224:227], v[48:51]\n"
    "v_perm_b32 v234, v215, v211, s75\n"
    "v_perm_b32 v235, v223, v219, s75\n"
    "v_mfma_f32_16x16x32_bf16 v[80:83], a[8:11], v[224:227], v[80:83]\n"
    "v_perm_b32 v236, v199, v195, s76\n"
    "v_perm_b32 v237, v207, v203, s76\n"
    "v_mfma_f32_16x16x32_bf16 v[112:115], a[12:15], v[224:227], v[112:115]\n"
    "v_perm_b32 v238, v215, v211, s76\n"
    "v_perm_b32 v239, v223, v219, s76\n"
    "v_mfma_f32_16x16x32_bf16 a[192:195], a[16:19], v[224:227], a[192:195]\n"
    "buffer_load_dwordx4 v[152:155], v248, s[52:55], s69 offen offset:2304\n"
    "v_mfma_f32_16x16x32_bf16 v[20:23], a[0:3], v[228:231], v[20:23]\n"
    "s_lshl_b32 s90, s94, 12\n"
    "v_add_u32 v252, s90, v249\n"
    "v_mfma_f32_16x16x32_bf16 v[52:55], a[4:7], v[228:231], v[52:55]\n"
    "s_lshl_b32 s90, s94, 11\n"
    "v_add_u32 v250, s90, v246\n"
    "v_mfma_f32_16x16x32_bf16 v[84:87], a[8:11], v[228:231], v[84:87]\n"
    "v_add_u32 v251, 65536, v250\n"
    "s_add_u32 s94, s94, 1\n"
    "v_mfma_f32_16x16x32_bf16 v[116:119], a[12:15], v[228:231], v[116:119]\n"
    "s_and_b32 s94, s94, 15\n"
    "v_mfma_f32_16x16x32_bf16 a[196:199], a[16:19], v[228:231], a[196:199]\n"
    "v_mfma_f32_16x16x32_bf16 v[24:27], a[0:3], v[232:235], v[24:27]\n"
    "buffer_load_dwordx4 v[156:159], v248, s[52:55], s69 offen offset:3456\n"
    "s_add_u32 s95, s95, 1\n"
    "v_mfma_f32_16x16x32_bf16 v[56:59], a[4:7], v[232:235], v[56:59]\n"
    "s_and_b32 s95, s95, 15\n"
    "s_mul_i32 s68, s95, 36864\n"
    "v_mfma_f32_16x16x32_bf16 v[88:91], a[8:11], v[232:235], v[88:91]\n"
    "v_mfma_f32_16x16x32_bf16 v[120:123], a[12:15], v[232:235], v[120:123]\n"
    "s_add_u32 s69, s68, 18432\n"
    "v_mfma_f32_16x16x32_bf16 a[200:203], a[16:19], v[232:235], a[200:203]\n"
    "s_waitcnt vmcnt(8)\n"
    "v_mfma_f32_16x16x32_bf16 v[28:31], a[0:3], v[236:239], v[28:31]\n"
    "v_perm_b32 v224, v164, v160, s75\n"
    "v_perm_b32 v225, v172, v168, s75\n"
    "v_mfma_f32_16x16x32_bf16 v[60:63], a[4:7], v[236:239], v[60:63]\n"
    "buffer_load_dwordx4 v[192:195], v248, s[52:55], s68 offen offset:0\n"
    "s_cmp_ge_u32 s82, s77\n"
    "v_mfma_f32_16x16x32_bf16 v[92:95], a[8:11], v[236:239], v[92:95]\n"
    "v_perm_b32 v226, v180, v176, s75\n"
    "v_perm_b32 v227, v188, v184, s75\n"
    "v_mfma_f32_16x16x32_bf16 v[124:127], a[12:15], v[236:239], v[124:127]\n"
    "v_perm_b32 v228, v164, v160, s76\n"
    "v_perm_b32 v229, v172, v168, s76\n"
    "v_mfma_f32_16x16x32_bf16 a[204:207], a[16:19], v[236:239], a[204:207]\n"
    "v_perm_b32 v230, v180, v176, s76\n"
    "v_perm_b32 v231, v188, v184, s76\n"
    "v_mfma_f32_16x16x32_bf16 a[112:115], a[60:63], a[0:3], a[112:115]\n"
    "s_cbranch_scc1 L_no0_%=\n"
    "L_no0r_%=:\n"
    "v_mfma_f32_16x16x32_bf16 a[116:119], a[60:63], a[4:7], a[116:119]\n"
    "buffer_load_dwordx4 v[196:199], v248, s[52:55], s68 offen offset:1152\n"
    "v_mfma_f32_16x16x32_bf16 a[120:123], a[60:63], a[8:11], a[120:123]\n"
    "s_waitcnt lgkmcnt(0)\n"
    "v_mfma_f32_16x16x32_bf16 a[124:127], a[60:63], a[12:15], a[124:127]\n"
    "s_sub_u32 s82, s82, 1\n"
    "s_cmp_lg_u32 s82, 0\n"
    "v_mfma_f32_16x16x32_bf16 a[128:131], a[60:63], a[16:19], a[128:131]\n"
    "s_cbranch_scc1 L_p3%=\n"
    "v_mfma_f32_16x16x32_bf16 v[0:3], a[20:23], v[224:227], v[0:3]\n"
    "v_perm_b32 v232, v165, v161, s75\n"
    "v_perm_b32 v233, v173, v169, s75\n"
    "v_mfma_f32_16x16x32_bf16 v[32:35], a[24:27], v[224:227], v[32:35]\n"
    "v_perm_b32 v234, v181, v177, s75\n"
    "v_perm_b32 v235, v189, v185, s75\n"
    "v_mfma_f32_16x16x32_bf16 v[64:67], a[28:31], v[224:227], v[64:67]\n"
    "buffer_load_dwordx4 v[200:203], v248, s[52:55], s68 offen offset:2304\n"
    "v_mfma_f32_16x16x32_bf16 v[96:99], a[32:35], v[224:227], v[96:99]\n"
    "v_perm_b32 v236, v165, v161, s76\n"
    "v_perm_b32 v237, v173, v169, s76\n"
    "v_mfma_f32_16x16x32_bf16 a[176:179], a[36:39], v[224:227], a[176:179]\n"
    "v_perm_b32 v238, v181, v177, s76\n"
    "v_perm_b32 v239, v189, v185, s76\n"
    "v_mfma_f32_16x16x32_bf16 v[4:7], a[20:23], v[228:231], v[4:7]\n"
    "buffer_load_dwordx4 v[204:207], v248, s[52:55], s68 offen offset:3456\n"
    "v_mfma_f32_16x16x32_bf16 v[36:39], a[24:27], v[228:231], v[36:39]\n"
    "ds_read_b128 a[40:43], v250 offset:0\n"
    "v_mfma_f32_16x16x32_bf16 v[68:71], a[28:31], v[228:231], v[68:71]\n"
    "ds_read_b128 a[44:47], v250 offset:1024\n"
    "v_mfma_f32_16x16x32_bf16 v[100:103], a[32:35], v[228:231], v[100:103]\n"
    "ds_read_b128 a[48:51], v250 offset:32768\n"
    "v_mfma_f32_16x16x32_bf16 a[180:183], a[36:39], v[228:231], a[180:183]\n"
    "buffer_load_dwordx4 v[208:211], v248, s[52:55], s69 offen offset:0\n"
    "v_mfma_f32_16x16x32_bf16 v[8:11], a[20:23], v[232:235], v[8:11]\n"
    "v_perm_b32 v224, v166, v162, s75\n"
    "v_perm_b32 v225, v174, v170, s75\n"
    "v_mfma_f32_16x16x32_bf16 v[40:43], a[24:27], v[232:235], v[40:43]\n"
    "v_perm_b32 v226, v182, v178, s75\n"
    "v_perm_b32 v227, v190, v186, s75\n"
    "v_mfma_f32_16x16x32_bf16 v[72:75], a[28:31], v[232:235], v[72:75]\n"
    "buffer_load_dwordx4 v[212:215], v248, s[52:55], s69 offen offset:1152\n"
    "v_mfma_f32_16x16x32_bf16 v[104:107], a[32:35], v[232:235], v[104:107]\n"
    "v_perm_b32 v228, v166, v162, s76\n"
    "v_perm_b32 v229, v174, v170, s76\n"
    "v_mfma_f32_16x16x32_bf16 a[184:187], a[36:39], v[232:235], a[184:187]\n"
    "v_perm_b32 v230, v182, v178, s76\n"
    "v_perm_b32 v231, v190, v186, s76\n"
    "v_mfma_f32_16x16x32_bf16 v[12:15], a[20:23], v[236:239], v[12:15]\n"
    "ds_read_b128 a[52:55], v250 offset:33792\n"
    "v_mfma_f32_16x16x32_bf16 v[44:47], a[24:27], v[236:239], v[44:47]\n"
    "buffer_load_dwordx4 v[216:219], v248, s[52:55], s69 offen offset:2304\n"
    "v_mfma_f32_16x16x32_bf16 v[76:79], a[28:31], v[236:239], v[76:79]\n"
    "ds_read_b128 a[56:59], v251\n"
    "v_mfma_f32_16x16x32_bf16 v[108:111], a[32:35], v[236:239], v[108:111]\n"
    "ds_read_b64_tr_b16 a[68:69], v252 offset:0\n"
    "v_mfma_f32_16x16x32_bf16 a[188:191], a[36:39], v[236:239], a[188:191]\n"
    "buffer_load_dwordx4 v[220:223], v248, s[52:55], s69 offen offset:3456\n"
    "v_mfma_f32_16x16x32_bf16 v[16:19], a[20:23], v[224:227], v[16:19]\n"
    "v_perm_b32 v232, v167, v163, s75\n"
    "v_perm_b32 v233, v175, v171, s75\n"
    "v_mfma_f32_16x16x32_bf16 v[48:51], a[24:27], v[224:227], v[48:51]\n"
    "v_perm_b32 v234, v183, v179, s75\n"
    "v_perm_b32 v235, v191, v187, s75\n"
    "v_mfma_f32_16x16x32_bf16 v[80:83], a[28:31], v[224:227], v[80:83]\n"
    "v_perm_b32 v236, v167, v163, s76\n"
    "v_perm_b32 v237, v175, v171, s76\n"
    "v_mfma_f32_16x16x32_bf16 v[112:115], a[32:35], v[224:227], v[112:115]\n"
    "v_perm_b32 v238, v183, v179, s76\n"
    "v_perm_b32 v239, v191, v187, s76\n"
    "v_mfma_f32_16x16x32_bf16 a[192:195], a[36:39], v[224:227], a[192:195]\n"
    "ds_read_b64_tr_b16 a[70:71], v252 offset:2048\n"
    "s_waitcnt vmcnt(8)\n"
    "v_mfma_f32_16x16x32_bf16 v[20:23], a[20:23], v[228:231], v[20:23]\n"
    "s_lshl_b32 s90, s94, 12\n"
    "v_add_u32 v252, s90, v249\n"
    "v_mfma_f32_16x16x32_bf16 v[52:55], a[24:27], v[228:231], v[52:55]\n"
    "s_lshl_b32 s90, s94, 11\n"
    "v_add_u32 v250, s90, v246\n"
    "v_add_u32 v251, 65536, v250\n"
    "v_mfma_f32_16x16x32_bf16 v[84:87], a[28:31], v[228:231], v[84:87]\n"
    "ds_read_b128 a[0:3], v250 offset:0\n"
    "v_mfma_f32_16x16x32_bf16 v[116:119], a[32:35], v[228:231], v[116:119]\n"
    "v_perm_b32 v160, v132, v128, s75\n"
    "v_perm_b32 v161, v140, v136, s75\n"
    "v_mfma_f32_16x16x32_bf16 a[196:199], a[36:39], v[228:231], a[196:199]\n"
    "ds_read_b128 a[4:7], v250 offset:1024\n"
    "v_perm_b32 v162, v148, v144, s75\n"
    "v_perm_b32 v163, v156, v152, s75\n"
    "v_mfma_f32_16x16x32_bf16 v[24:27], a[20:23], v[232:235], v[24:27]\n"
    "v_perm_b32 v164, v132, v128, s76\n"
    "v_perm_b32 v165, v140, v136, s76\n"
    "v_mfma_f32_16x16x32_bf16 v[56:59], a[24:27], v[232:235], v[56:59]\n"
    "ds_read_b128 a[8:11], v250 offset:32768\n"
    "v_perm_b32 v166, v148, v144, s76\n"
    "v_perm_b32 v167, v156, v152, s76\n"
    "v_mfma_f32_16x16x32_bf16 v[88:91], a[28:31], v[232:235], v[88:91]\n"
    "v_perm_b32 v168, v133, v129, s75\n"
    "v_perm_b32 v169, v141, v137, s75\n"
    "v_mfma_f32_16x16x32_bf16 v[120:123], a[32:35], v[232:235], v[120:123]\n"
    "ds_read_b128 a[12:15], v250 offset:33792\n"
    "v_perm_b32 v170, v149, v145, s75\n"
    "v_perm_b32 v171, v157, v153, s75\n"
    "v_mfma_f32_16x16x32_bf16 a[200:203], a[36:39], v[232:235], a[200:203]\n"
    "v_perm_b32 v172, v133, v129, s76\n"
    "v_perm_b32 v173, v141, v137, s76\n"
    "v_mfma_f32_16x16x32_bf16 v[28:31], a[20:23], v[236:239], v[28:31]\n"
    "ds_read_b128 a[16:19], v251\n"
    "v_perm_b32 v174, v149, v145, s76\n"
    "v_perm_b32 v175, v157, v153, s76\n"
    "v_mfma_f32_16x16x32_bf16 v[60:63], a[24:27], v[236:239], v[60:63]\n"
    "v_perm_b32 v176, v134, v130, s75\n"
    "v_perm_b32 v177, v142, v138, s75\n"
    "v_mfma_f32_16x16x32_bf16 v[92:95], a[28:31], v[236:239], v[92:95]\n"
    "ds_read_b64_tr_b16 a[60:61], v252 offset:0\n"
    "v_perm_b32 v178, v150, v146, s75\n"
    "v_perm_b32 v179, v158, v154, s75\n"
    "v_mfma_f32_16x16x32_bf16 v[124:127], a[32:35], v[236:239], v[124:127]\n"
    "v_perm_b32 v180, v134, v130, s76\n"
    "v_perm_b32 v181, v142, v138, s76\n"
    "v_mfma_f32_16x16x32_bf16 a[204:207], a[36:39], v[236:239], a[204:207]\n"
    "ds_read_b64_tr_b16 a[62:63], v252 offset:2048\n"
    "v_perm_b32 v182, v150, v146, s76\n"
    "v_perm_b32 v183, v158, v154, s76\n"
    "v_mfma_f32_16x16x32_bf16 a[112:115], a[64:67], a[20:23], a[112:115]\n"
    "v_perm_b32 v184, v135, v131, s75\n"
    "v_perm_b32 v185, v143, v139, s75\n"
    "v_mfma_f32_16x16x32_bf16 a[116:119], a[64:67], a[24:27], a[116:119]\n"
    "v_perm_b32 v186, v151, v147, s75\n"
    "v_perm_b32 v187, v159, v155, s75\n"
    "v_mfma_f32_16x16x32_bf16 a[120:123], a[64:67], a[28:31], a[120:123]\n"
    "v_perm_b32 v188, v135, v131, s76\n"
    "v_perm_b32 v189, v143, v139, s76\n"
    "v_mfma_f32_16x16x32_bf16 a[124:127], a[64:67], a[32:35], a[124:127]\n"
    "v_perm_b32 v190, v151, v147, s76\n"
    "v_perm_b32 v191, v159, v155, s76\n"
    "v_mfma_f32_16x16x32_bf16 a[128:131], a[64:67], a[36:39], a[128:131]\n"
    "s_waitcnt lgkmcnt(0)\n"
    "s_waitcnt vmcnt(0)\n"
    "v_mfma_f32_16x16x32_bf16 v[0:3], a[40:43], v[160:163], v[0:3]\n"
    "v_mfma_f32_16x16x32_bf16 v[4:7], a[40:43], v[164:167], v[4:7]\n"
    "v_perm_b32 v128, v196, v192, s75\n"
    "v_perm_b32 v129, v204, v200, s75\n"
    "v_mfma_f32_16x16x32_bf16 v[8:11], a[40:43], v[168:171], v[8:11]\n"
    "v_perm_b32 v130, v212, v208, s75\n"
    "v_perm_b32 v131, v220, v216, s75\n"
    "v_mfma_f32_16x16x32_bf16 v[12:15], a[40:43], v[172:175], v[12:15]\n"
    "v_perm_b32 v132, v196, v192, s76\n"
    "v_perm_b32 v133, v204, v200, s76\n"
    "v_mfma_f32_16x16x32_bf16 v[16:19], a[40:43], v[176:179], v[16:19]\n"
    "v_perm_b32 v134, v212, v208, s76\n"
    "v_perm_b32 v135, v220, v216, s76\n"
    "v_mfma_f32_16x16x32_bf16 v[20:23], a[40:43], v[180:183], v[20:23]\n"
    "v_perm_b32 v136, v197, v193, s75\n"
    "v_perm_b32 v137, v205, v201, s75\n"
    "v_mfma_f32_16x16x32_bf16 v[24:27], a[40:43], v[184:187], v[24:27]\n"
    "v_perm_b32 v138, v213, v209, s75\n"
    "v_perm_b32 v139, v221, v217, s75\n"
    "v_mfma_f32_16x16x32_bf16 v[28:31], a[40:43], v[188:191], v[28:31]\n"
    "v_perm_b32 v140, v197, v193, s76\n"
    "v_perm_b32 v141, v205, v201, s76\n"
    "v_mfma_f32_16x16x32_bf16 a[112:115], a[68:71], a[40:43], a[112:115]\n"
    "v_perm_b32 v142, v213, v209, s76\n"
    "v_perm_b32 v143, v221, v217, s76\n"
    "v_mfma_f32_16x16x32_bf16 v[32:35], a[44:47], v[160:163], v[32:35]\n"
    "v_perm_b32 v144, v198, v194, s75\n"
    "v_perm_b32 v145, v206, v202, s75\n"
    "v_mfma_f32_16x16x32_bf16 v[36:39], a[44:47], v[164:167], v[36:39]\n"
    "v_perm_b32 v146, v214, v210, s75\n"
    "v_perm_b32 v147, v222, v218, s75\n"
    "v_mfma_f32_16x16x32_bf16 v[40:43], a[44:47], v[168:171], v[40:43]\n"
    "v_perm_b32 v148, v198, v194, s76\n"
    "v_perm_b32 v149, v206, v202, s76\n"
    "v_mfma_f32_16x16x32_bf16 v[44:47], a[44:47], v[172:175], v[44:47]\n"
    "v_perm_b32 v150, v214, v210, s76\n"
    "v_perm_b32 v151, v222, v218, s76\n"
    "v_mfma_f32_16x16x32_bf16 v[48:51], a[44:47], v[176:179], v[48:51]\n"
    "v_perm_b32 v152, v199, v195, s75\n"
    "v_perm_b32 v153, v207, v203, s75\n"
    "v_mfma_f32_16x16x32_bf16 v[52:55], a[44:47], v[180:183], v[52:55]\n"
    "v_perm_b32 v154, v215, v211, s75\n"
    "v_perm_b32 v155, v223, v219, s75\n"
    "v_mfma_f32_16x16x32_bf16 v[56:59], a[44:47], v[184:187], v[56:59]\n"
    "v_perm_b32 v156, v199, v195, s76\n"
    "v_perm_b32 v157, v207, v203, s76\n"
    "v_mfma_f32_16x16x32_bf16 v[60:63], a[44:47], v[188:191], v[60:63]\n"
    "v_perm_b32 v158, v215, v211, s76\n"
    "v_perm_b32 v159, v223, v219, s76\n"
    "v_mfma_f32_16x16x32_bf16 a[116:119], a[68:71], a[44:47], a[116:119]\n"
    "v_mfma_f32_16x16x32_bf16 v[0:3], a[0:3], v[128:131], v[0:3]\n"
    "v_mfma_f32_16x16x32_bf16 v[4:7], a[0:3], v[132:135], v[4:7]\n"
    "v_mfma_f32_16x16x32_bf16 v[8:11], a[0:3], v[136:139], v[8:11]\n"
    "v_mfma_f32_16x16x32_bf16 v[12:15], a[0:3], v[140:143], v[12:15]\n"
    "v_mfma_f32_16x16x32_bf16 v[16:19], a[0:3], v[144:147], v[16:19]\n"
    "v_mfma_f32_16x16x32_bf16 v[20:23], a[0:3], v[148:151], v[20:23]\n"
    "v_mfma_f32_16x16x32_bf16 v[24:27], a[0:3], v[152:155], v[24:27]\n"
    "v_mfma_f32_16x16x32_bf16 v[28:31], a[0:3], v[156:159], v[28:31]\n"
    "v_mfma_f32_16x16x32_bf16 a[112:115], a[60:63], a[0:3], a[112:115]\n"
    "v_mfma_f32_16x16x32_bf16 v[32:35], a[4:7], v[128:131], v[32:35]\n"
    "v_mfma_f32_16x16x32_bf16 v[36:39], a[4:7], v[132:135], v[36:39]\n"
    "s_add_u32 s72, s70, 0\n"
    "v_cvt_pk_bf16_f32 v192, v0, v4\n"
    "v_mfma_f32_16x16x32_bf16 v[40:43], a[4:7], v[136:139], v[40:43]\n"
    "v_cvt_pk_bf16_f32 v193, v8, v12\n"
    "v_cvt_pk_bf16_f32 v194, v16, v20\n"
    "v_mfma_f32_16x16x32_bf16 v[44:47], a[4:7], v[140:143], v[44:47]\n"
    "v_cvt_pk_bf16_f32 v195, v24, v28\n"
    "v_mfma_f32_16x16x32_bf16 v[48:51], a[4:7], v[144:147], v[48:51]\n"
    "buffer_store_dwordx4 v[192:195], v244, s[56:59], s72 offen offset:0 sc1\n"
    "v_cvt_pk_bf16_f32 v196, v1, v5\n"
    "v_mfma_f32_16x16x32_bf16 v[52:55], a[4:7], v[148:151], v[52:55]\n"
    "v_cvt_pk_bf16_f32 v197, v9, v13\n"
    "v_cvt_pk_bf16_f32 v198, v17, v21\n"
    "v_mfma_f32_16x16x32_bf16 v[56:59], a[4:7], v[152:155], v[56:59]\n"
    "v_cvt_pk_bf16_f32 v199, v25, v29\n"
    "v_mfma_f32_16x16x32_bf16 v[60:63], a[4:7], v[156:159], v[60:63]\n"
    "buffer_store_dwordx4 v[196:199], v244, s[56:59], s72 offen offset:1024 sc1\n"
    "v_cvt_pk_bf16_f32 v200, v2, v6\n"
    "v_mfma_f32_16x16x32_bf16 a[116:119], a[60:63], a[4:7], a[116:119]\n"
    "v_cvt_pk_bf16_f32 v201, v10, v14\n"
    "v_cvt_pk_bf16_f32 v202, v18, v22\n"
    "v_mfma_f32_16x16x32_bf16 a[176:179], a[56:59], v[160:163], a[176:179]\n"
    "v_cvt_pk_bf16_f32 v203, v26, v30\n"
    "v_mfma_f32_16x16x32_bf16 a[180:183], a[56:59], v[164:167], a[180:183]\n"
    "buffer_store_dwordx4 v[200:203], v244, s[56:59], s72 offen offset:2048 sc1\n"
    "v_cvt_pk_bf16_f32 v204, v3, v7\n"
    "v_mfma_f32_16x16x32_bf16 a[184:187], a[56:59], v[168:171], a[184:187]\n"
    "v_cvt_pk_bf16_f32 v205, v11, v15\n"
    "v_cvt_pk_bf16_f32 v206, v19, v23\n"
    "v_mfma_f32_16x16x32_bf16 a[188:191], a[56:59], v[172:175], a[188:191]\n"
    "v_cvt_pk_bf16_f32 v207, v27, v31\n"
    "v_mfma_f32_16x16x32_bf16 a[192:195], a[56:59], v[176:179], a[192:195]\n"
    "buffer_store_dwordx4 v[204:207], v244, s[56:59], s72 offen offset:3072 sc1\n"
    "v_accvgpr_read_b32 v232, a112\n"
    "v_mfma_f32_16x16x32_bf16 a[196:199], a[56:59], v[180:183], a[196:199]\n"
    "v_accvgpr_read_b32 v233, a113\n"
    "v_accvgpr_read_b32 v234, a114\n"
    "v_mfma_f32_16x16x32_bf16 a[200:203], a[56:59], v[184:187], a[200:203]\n"
    "v_accvgpr_read_b32 v235, a115\n"
    "v_cvt_pk_bf16_f32 v232, v232, v233\n"
    "v_mfma_f32_16x16x32_bf16 a[204:207], a[56:59], v[188:191], a[204:207]\n"
    "v_cvt_pk_bf16_f32 v233, v234, v235\n"
    "v_mfma_f32_16x16x32_bf16 a[128:131], a[68:71], a[56:59], a[128:131]\n"
    "buffer_store_dwordx2 v[232:233], v245, s[56:59], s71 offen offset:0 sc1\n"
    "s_add_u32 s72, s70, 16384\n"
    "v_mfma_f32_16x16x32_bf16 a[176:179], a[16:19], v[128:131], a[176:179]\n"
    "v_cvt_pk_bf16_f32 v208, v32, v36\n"
    "v_cvt_pk_bf16_f32 v209, v40, v44\n"
    "v_mfma_f32_16x16x32_bf16 a[180:183], a[16:19], v[132:135], a[180:183]\n"
    "v_cvt_pk_bf16_f32 v210, v48, v52\n"
    "v_cvt_pk_bf16_f32 v211, v56, v60\n"
    "v_mfma_f32_16x16x32_bf16 a[184:187], a[16:19], v[136:139], a[184:187]\n"
    "buffer_store_dwordx4 v[208:211], v244, s[56:59], s72 offen offset:0 sc1\n"
    "v_cvt_pk_bf16_f32 v212, v33, v37\n"
    "v_mfma_f32_16x16x32_bf16 a[188:191], a[16:19], v[140:143], a[188:191]\n"
    "v_cvt_pk_bf16_f32 v213, v41, v45\n"
    "v_cvt_pk_bf16_f32 v214, v49, v53\n"
    "v_mfma_f32_16x16x32_bf16 a[192:195], a[16:19], v[144:147], a[192:195]\n"
    "v_cvt_pk_bf16_f32 v215, v57, v61\n"
    "v_mfma_f32_16x16x32_bf16 a[196:199], a[16:19], v[148:151], a[196:199]\n"
    "buffer_store_dwordx4 v[212:215], v244, s[56:59], s72 offen offset:1024 sc1\n"
    "v_cvt_pk_bf16_f32 v216, v34, v38\n"
    "v_mfma_f32_16x16x32_bf16 a[200:203], a[16:19], v[152:155], a[200:203]\n"
    "v_cvt_pk_bf16_f32 v217, v42, v46\n"
    "v_cvt_pk_bf16_f32 v218, v50, v54\n"
    "v_mfma_f32_16x16x32_bf16 a[204:207], a[16:19], v[156:159], a[204:207]\n"
    "v_cvt_pk_bf16_f32 v219, v58, v62\n"
    "v_mfma_f32_16x16x32_bf16 a[128:131], a[60:63], a[16:19], a[128:131]\n"
    "buffer_store_dwordx4 v[216:219], v244, s[56:59], s72 offen offset:2048 sc1\n"
    "v_cvt_pk_bf16_f32 v220, v35, v39\n"
    "v_mfma_f32_16x16x32_bf16 v[64:67], a[48:51], v[160:163], v[64:67]\n"
    "v_cvt_pk_bf16_f32 v221, v43, v47\n"
    "v_cvt_pk_bf16_f32 v222, v51, v55\n"
    "v_mfma_f32_16x16x32_bf16 v[68:71], a[48:51], v[164:167], v[68:71]\n"
    "v_cvt_pk_bf16_f32 v223, v59, v63\n"
    "v_mfma_f32_16x16x32_bf16 v[72:75], a[48:51], v[168:171], v[72:75]\n"
    "buffer_store_dwordx4 v[220:223], v244, s[56:59], s72 offen offset:3072 sc1\n"
    "v_accvgpr_read_b32 v232, a116\n"
    "v_mfma_f32_16x16x32_bf16 v[76:79], a[48:51], v[172:175], v[76:79]\n"
    "v_accvgpr_read_b32 v233, a117\n"
    "v_accvgpr_read_b32 v234, a118\n"
    "v_mfma_f32_16x16x32_bf16 v[80:83], a[48:51], v[176:179], v[80:83]\n"
    "v_accvgpr_read_b32 v235, a119\n"
    "v_cvt_pk_bf16_f32 v232, v232, v233\n"
    "v_mfma_f32_16x16x32_bf16 v[84:87], a[48:51], v[180:183], v[84:87]\n"
    "v_cvt_pk_bf16_f32 v233, v234, v235\n"
    "v_mfma_f32_16x16x32_bf16 v[88:91], a[48:51], v[184:187], v[88:91]\n"
    "buffer_store_dwordx2 v[232:233], v245, s[56:59], s71 offen offset:512 sc1\n"
    "s_add_u32 s72, s70, 65536\n"
    "v_mfma_f32_16x16x32_bf16 v[92:95], a[48:51], v[188:191], v[92:95]\n"
    "v_accvgpr_read_b32 v0, a176\n"
    "v_accvgpr_read_b32 v4, a180\n"
    "v_mfma_f32_16x16x32_bf16 a[120:123], a[68:71], a[48:51], a[120:123]\n"
    "v_accvgpr_read_b32 v8, a184\n"
    "v_accvgpr_read_b32 v12, a188\n"
    "v_mfma_f32_16x16x32_bf16 v[64:67], a[8:11], v[128:131], v[64:67]\n"
    "v_accvgpr_read_b32 v16, a192\n"
    "v_accvgpr_read_b32 v20, a196\n"
    "v_mfma_f32_16x16x32_bf16 v[68:71], a[8:11], v[132:135], v[68:71]\n"
    "v_accvgpr_read_b32 v24, a200\n"
    "v_accvgpr_read_b32 v28, a204\n"
    "v_mfma_f32_16x16x32_bf16 v[72:75], a[8:11], v[136:139], v[72:75]\n"
    "v_cvt_pk_bf16_f32 v192, v0, v4\n"
    "v_cvt_pk_bf16_f32 v193, v8, v12\n"
    "v_mfma_f32_16x16x32_bf16 v[76:79], a[8:11], v[140:143], v[76:79]\n"
    "v_cvt_pk_bf16_f32 v194, v16, v20\n"
    "v_cvt_pk_bf16_f32 v195, v24, v28\n"
    "v_mfma_f32_16x16x32_bf16 v[80:83], a[8:11], v[144:147], v[80:83]\n"
    "buffer_store_dwordx4 v[192:195], v244, s[56:59], s72 offen offset:0 sc1\n"
    "v_accvgpr_read_b32 v1, a177\n"
    "v_mfma_f32_16x16x32_bf16 v[84:87], a[8:11], v[148:151], v[84:87]\n"
    "v_accvgpr_read_b32 v5, a181\n"
    "v_accvgpr_read_b32 v9, a185\n"
    "v_mfma_f32_16x16x32_bf16 v[88:91], a[8:11], v[152:155], v[88:91]\n"
    "v_accvgpr_read_b32 v13, a189\n"
    "v_accvgpr_read_b32 v17, a193\n"
    "v_mfma_f32_16x16x32_bf16 v[92:95], a[8:11], v[156:159], v[92:95]\n"
    "v_accvgpr_read_b32 v21, a197\n"
    "v_accvgpr_read_b32 v25, a201\n"
    "v_mfma_f32_16x16x32_bf16 a[120:123], a[60:63], a[8:11], a[120:123]\n"
    "v_accvgpr_read_b32 v29, a205\n"
    "v_cvt_pk_bf16_f32 v196, v1, v5\n"
    "v_mfma_f32_16x16x32_bf16 v[96:99], a[52:55], v[160:163], v[96:99]\n"
    "v_cvt_pk_bf16_f32 v197, v9, v13\n"
    "v_cvt_pk_bf16_f32 v198, v17, v21\n"
    "v_mfma_f32_16x16x32_bf16 v[100:103], a[52:55], v[164:167], v[100:103]\n"
    "v_cvt_pk_bf16_f32 v199, v25, v29\n"
    "v_mfma_f32_16x16x32_bf16 v[104:107], a[52:55], v[168:171], v[104:107]\n"
    "buffer_store_dwordx4 v[196:199], v244, s[56:59], s72 offen offset:1024 sc1\n"
    "v_accvgpr_read_b32 v2, a178\n"
    "v_mfma_f32_16x16x32_bf16 v[108:111], a[52:55], v[172:175], v[108:111]\n"
    "v_accvgpr_read_b32 v6, a182\n"
    "v_accvgpr_read_b32 v10, a186\n"
    "v_mfma_f32_16x16x32_bf16 v[112:115], a[52:55], v[176:179], v[112:115]\n"
    "v_accvgpr_read_b32 v14, a190\n"
    "v_accvgpr_read_b32 v18, a194\n"
    "v_mfma_f32_16x16x32_bf16 v[116:119], a[52:55], v[180:183], v[116:119]\n"
    "v_accvgpr_read_b32 v22, a198\n"
    "v_accvgpr_read_b32 v26, a202\n"
    "v_mfma_f32_16x16x32_bf16 v[120:123], a[52:55], v[184:187], v[120:123]\n"
    "v_accvgpr_read_b32 v30, a206\n"
    "v_cvt_pk_bf16_f32 v200, v2, v6\n"
    "v_mfma_f32_16x16x32_bf16 v[124:127], a[52:55], v[188:191], v[124:127]\n"
    "v_cvt_pk_bf16_f32 v201, v10, v14\n"
    "v_cvt_pk_bf16_f32 v202, v18, v22\n"
    "v_mfma_f32_16x16x32_bf16 a[124:127], a[68:71], a[52:55], a[124:127]\n"
    "v_cvt_pk_bf16_f32 v203, v26, v30\n"
    "v_mfma_f32_16x16x32_bf16 v[96:99], a[12:15], v[128:131], v[96:99]\n"
    "buffer_store_dwordx4 v[200:203], v244, s[56:59], s72 offen offset:2048 sc1\n"
    "v_accvgpr_read_b32 v3, a179\n"
    "v_mfma_f32_16x16x32_bf16 v[100:103], a[12:15], v[132:135], v[100:103]\n"
    "v_accvgpr_read_b32 v7, a183\n"
    "v_accvgpr_read_b32 v11, a187\n"
    "v_mfma_f32_16x16x32_bf16 v[104:107], a[12:15], v[136:139], v[104:107]\n"
    "v_accvgpr_read_b32 v15, a191\n"
    "v_accvgpr_read_b32 v19, a195\n"
    "v_mfma_f32_16x16x32_bf16 v[108:111], a[12:15], v[140:143], v[108:111]\n"
    "v_accvgpr_read_b32 v23, a199\n"
    "v_accvgpr_read_b32 v27, a203\n"
    "v_mfma_f32_16x16x32_bf16 v[112:115], a[12:15], v[144:147], v[112:115]\n"
    "v_accvgpr_read_b32 v31, a207\n"
    "v_cvt_pk_bf16_f32 v204, v3, v7\n"
    "v_mfma_f32_16x16x32_bf16 v[116:119], a[12:15], v[148:151], v[116:119]\n"
    "v_cvt_pk_bf16_f32 v205, v11, v15\n"
    "v_cvt_pk_bf16_f32 v206, v19, v23\n"
    "v_mfma_f32_16x16x32_bf16 v[120:123], a[12:15], v[152:155], v[120:123]\n"
    "v_cvt_pk_bf16_f32 v207, v27, v31\n"
    "v_mfma_f32_16x16x32_bf16 v[124:127], a[12:15], v[156:159], v[124:127]\n"
    "buffer_store_dwordx4 v[204:207], v244, s[56:59], s72 offen offset:3072 sc1\n"
    "v_accvgpr_read_b32 v232, a128\n"
    "v_mfma_f32_16x16x32_bf16 a[124:127], a[60:63], a[12:15], a[124:127]\n"
    "v_accvgpr_read_b32 v233, a129\n"
    "v_accvgpr_read_b32 v234, a130\n"
    "v_accvgpr_read_b32 v235, a131\n"
    "v_cvt_pk_bf16_f32 v232, v232, v233\n"
    "v_cvt_pk_bf16_f32 v233, v234, v235\n"
    "buffer_store_dwordx2 v[232:233], v245, s[56:59], s71 offen offset:2048 sc1\n"
    "s_add_u32 s72, s70, 32768\n"
    "v_cvt_pk_bf16_f32 v208, v64, v68\n"
    "v_cvt_pk_bf16_f32 v209, v72, v76\n"
    "v_cvt_pk_bf16_f32 v210, v80, v84\n"
    "v_cvt_pk_bf16_f32 v211, v88, v92\n"
    "buffer_store_dwordx4 v[208:211], v244, s[56:59], s72 offen offset:0 sc1\n"
    "v_cvt_pk_bf16_f32 v212, v65, v69\n"
    "v_cvt_pk_bf16_f32 v213, v73, v77\n"
    "v_cvt_pk_bf16_f32 v214, v81, v85\n"
    "v_cvt_pk_bf16_f32 v215, v89, v93\n"
    "buffer_store_dwordx4 v[212:215], v244, s[56:59], s72 offen offset:1024 sc1\n"
    "v_cvt_pk_bf16_f32 v216, v66, v70\n"
    "v_cvt_pk_bf16_f32 v217, v74, v78\n"
    "v_cvt_pk_bf16_f32 v218, v82, v86\n"
    "v_cvt_pk_bf16_f32 v219, v90, v94\n"
    "buffer_store_dwordx4 v[216:219], v244, s[56:59], s72 offen offset:2048 sc1\n"
    "v_cvt_pk_bf16_f32 v220, v67, v71\n"
    "v_cvt_pk_bf16_f32 v221, v75, v79\n"
    "v_cvt_pk_bf16_f32 v222, v83, v87\n"
    "v_cvt_pk_bf16_f32 v223, v91, v95\n"
    "buffer_store_dwordx4 v[220:223], v244, s[56:59], s72 offen offset:3072 sc1\n"
    "v_accvgpr_read_b32 v232, a120\n"
    "v_accvgpr_read_b32 v233, a121\n"
    "v_accvgpr_read_b32 v234, a122\n"
    "v_accvgpr_read_b32 v235, a123\n"
    "v_cvt_pk_bf16_f32 v232, v232, v233\n"
    "v_cvt_pk_bf16_f32 v233, v234, v235\n"
    "buffer_store_dwordx2 v[232:233], v245, s[56:59], s71 offen offset:1024 sc1\n"
    "buffer_load_dwordx2 v[236:237], v241, s[60:63], 0 offen sc1\n"
    "s_add_u32 s72, s70, 49152\n"
    "v_cvt_pk_bf16_f32 v192, v96, v100\n"
    "v_cvt_pk_bf16_f32 v193, v104, v108\n"
    "v_cvt_pk_bf16_f32 v194, v112, v116\n"
    "v_cvt_pk_bf16_f32 v195, v120, v124\n"
    "buffer_store_dwordx4 v[192:195], v244, s[56:59], s72 offen offset:0 sc1\n"
    "v_cvt_pk_bf16_f32 v196, v97, v101\n"
    "v_cvt_pk_bf16_f32 v197, v105, v109\n"
    "v_cvt_pk_bf16_f32 v198, v113, v117\n"
    "v_cvt_pk_bf16_f32 v199, v121, v125\n"
    "buffer_store_dwordx4 v[196:199], v244, s[56:59], s72 offen offset:1024 sc1\n"
    "v_cvt_pk_bf16_f32 v200, v98, v102\n"
    "v_cvt_pk_bf16_f32 v201, v106, v110\n"
    "v_cvt_pk_bf16_f32 v202, v114, v118\n"
    "v_cvt_pk_bf16_f32 v203, v122, v126\n"
    "buffer_store_dwordx4 v[200:203], v244, s[56:59], s72 offen offset:2048 sc1\n"
    "v_cvt_pk_bf16_f32 v204, v99, v103\n"
    "v_cvt_pk_bf16_f32 v205, v107, v111\n"
    "v_cvt_pk_bf16_f32 v206, v115, v119\n"
    "v_cvt_pk_bf16_f32 v207, v123, v127\n"
    "buffer_store_dwordx4 v[204:207], v244, s[56:59], s72 offen offset:3072 sc1\n"
    "v_accvgpr_read_b32 v232, a124\n"
    "v_accvgpr_read_b32 v233, a125\n"
    "v_accvgpr_read_b32 v234, a126\n"
    "v_accvgpr_read_b32 v235, a127\n"
    "v_cvt_pk_bf16_f32 v232, v232, v233\n"
    "v_cvt_pk_bf16_f32 v233, v234, v235\n"
    "buffer_store_dwordx2 v[232:233], v245, s[56:59], s71 offen offset:1536 sc1\n"
    "s_mul_i32 s90, s5, 10\n"
    "s_add_u32 s90, s90, %2\n"
    "s_lshl_b32 s90, s90, 10\n"
    "s_mul_i32 s91, %2, 2304\n"
    "v_lshlrev_b32 v231, 4, v240\n"
    "v_add_u32 v224, s90, v231\n"
    "v_add_u32 v227, s91, v231\n"
    "s_add_u32 s90, s90, 4096\n"
    "s_add_u32 s91, s91, 9216\n"
    "v_add_u32 v225, s90, v231\n"
    "v_add_u32 v228, s91, v231\n"
    "s_add_u32 s90, s90, 4096\n"
    "s_add_u32 s91, s91, 9216\n"
    "v_add_u32 v226, s90, v231\n"
    "v_add_u32 v229, s91, v231\n"
    "s_cmp_lt_u32 %2, 2\n"
    "s_cbranch_scc1 L_mainonly%=\n"
    "s_sub_u32 s90, %2, 2\n"
    "s_lshl_b32 s90, s90, 3\n"
    "v_lshrrev_b32 v231, 3, v240\n"
    "v_add_u32 v231, s90, v231\n"
    "v_and_b32 v232, 7, v240\n"
    "v_mul_u32_u24 v229, 2304, v231\n"
    "v_lshl_add_u32 v229, v232, 4, v229\n"
    "v_add_u32 v229, 1024, v229\n"
    "s_mul_i32 s90, s5, 320\n"
    "s_add_u32 s90, s90, 81920\n"
    "v_lshlrev_b32 v231, 5, v231\n"
    "v_add_u32 v231, s90, v231\n"
    "v_lshrrev_b32 v226, 1, v232\n"
    "v_mul_u32_u24 v226, 2560, v226\n"
    "v_add_u32 v231, v231, v226\n"
    "v_and_b32 v232, 1, v232\n"
    "v_lshl_add_u32 v226, v232, 4, v231\n"
    "L_mainonly%=:\n"
    "v_mov_b32 v234, s80\n"
    "v_mov_b32 v235, s81\n"
    "s_lshl_b32 s91, s5, 3\n"
    "v_lshlrev_b32 v233, 7, v240\n"
    "s_mov_b32 s70, 0\n"
    "s_mov_b32 s92, 0xffff0000\n"
    "s_lshl_b32 s93, 1, s5\n"
    "s_mov_b32 s71, 0\n"
    "s_mov_b32 s82, 0x200000\n"
    "s_waitcnt vmcnt(0)\n"
    "s_barrier\n"
    "s_cmp_lg_u32 %2, 0\n"
    "s_cbranch_scc1 L_noflag%=\n"
    "s_mov_b64 exec, 255\n"
    "buffer_store_dwordx2 v[234:235], v233, s[60:63], s91 offen sc1\n"
    "s_mov_b64 exec, -1\n"
    "L_noflag%=:\n"
    "s_add_u32 s70, s5, 0\n"
    "s_and_b32 s70, s70, 7\n"
    "s_mul_i32 s70, s70, 92160\n"
    "buffer_load_dwordx4 v[0:3], v224, s[56:59], s70 offen sc1\n"
    "buffer_load_dwordx4 v[4:7], v225, s[56:59], s70 offen sc1\n"
    "buffer_load_dwordx4 v[8:11], v226, s[56:59], s70 offen sc1\n"
    "L_poll%=:\n"
    "v_cmp_eq_u64 vcc, s[80:81], v[236:237]\n"
    "s_nop 1\n"
    "s_and_b32 s72, vcc_lo, 0xff\n"
    "s_or_b32 s72, s72, s93\n"
    "s_cmp_eq_u32 s72, 0xff\n"
    "s_cbranch_scc1 L_all%=\n"
    "buffer_load_dwordx2 v[236:237], v241, s[60:63], 0 offen sc1\n"
    "s_waitcnt vmcnt(0)\n"
    "s_sub_u32 s82, s82, 1\n"
    "s_cmp_lg_u32 s82, 0\n"
    "s_cbranch_scc1 L_poll%=\n"
    "L_all%=:\n"
    "s_add_u32 s70, s5, 1\n"
    "s_and_b32 s70, s70, 7\n"
    "s_mul_i32 s70, s70, 92160\n"
    "buffer_load_dwordx4 v[12:15], v224, s[56:59], s70 offen sc1\n"
    "buffer_load_dwordx4 v[16:19], v225, s[56:59], s70 offen sc1\n"
    "buffer_load_dwordx4 v[20:23], v226, s[56:59], s70 offen sc1\n"
    "s_add_u32 s70, s5, 2\n"
    "s_and_b32 s70, s70, 7\n"
    "s_mul_i32 s70, s70, 92160\n"
    "buffer_load_dwordx4 v[24:27], v224, s[56:59], s70 offen sc1\n"
    "buffer_load_dwordx4 v[28:31], v225, s[56:59], s70 offen sc1\n"
    "buffer_load_dwordx4 v[32:35], v226, s[56:59], s70 offen sc1\n"
    "s_add_u32 s70, s5, 3\n"
    "s_and_b32 s70, s70, 7\n"
    "s_mul_i32 s70, s70, 92160\n"
    "buffer_load_dwordx4 v[36:39], v224, s[56:59], s70 offen sc1\n"
    "buffer_load_dwordx4 v[40:43], v225, s[56:59], s70 offen sc1\n"
    "buffer_load_dwordx4 v[44:47], v226, s[56:59], s70 offen sc1\n"
    "s_add_u32 s70, s5, 4\n"
    "s_and_b32 s70, s70, 7\n"
    "s_mul_i32 s70, s70, 92160\n"
    "buffer_load_dwordx4 v[48:51], v224, s[56:59], s70 offen sc1\n"
    "buffer_load_dwordx4 v[52:55], v225, s[56:59], s70 offen sc1\n"
    "buffer_load_dwordx4 v[56:59], v226, s[56:59], s70 offen sc1\n"
    "s_add_u32 s70, s5, 5\n"
    "s_and_b32 s70, s70, 7\n"
    "s_mul_i32 s70, s70, 92160\n"
    "buffer_load_dwordx4 v[60:63], v224, s[56:59], s70 offen sc1\n"
    "buffer_load_dwordx4 v[64:67], v225, s[56:59], s70 offen sc1\n"
    "buffer_load_dwordx4 v[68:71], v226, s[56:59], s70 offen sc1\n"
    "s_add_u32 s70, s5, 6\n"
    "s_and_b32 s70, s70, 7\n"
    "s_mul_i32 s70, s70, 92160\n"
    "buffer_load_dwordx4 v[72:75], v224, s[56:59], s70 offen sc1\n"
    "buffer_load_dwordx4 v[76:79], v225, s[56:59], s70 offen sc1\n"
    "buffer_load_dwordx4 v[80:83], v226, s[56:59], s70 offen sc1\n"
    "s_add_u32 s70, s5, 7\n"
    "s_and_b32 s70, s70, 7\n"
    "s_mul_i32 s70, s70, 92160\n"
    "buffer_load_dwordx4 v[84:87], v224, s[56:59], s70 offen sc1\n"
    "buffer_load_dwordx4 v[88:91], v225, s[56:59], s70 offen sc1\n"
    "buffer_load_dwordx4 v[92:95], v226, s[56:59], s70 offen sc1\n"
    "s_waitcnt vmcnt(23)\n"
    "v_lshlrev_b32 v96, 16, v0\n"
    "v_and_b32 v0, s92, v0\n"
    "v_lshlrev_b32 v97, 16, v1\n"
    "v_and_b32 v1, s92, v1\n"
    "v_lshlrev_b32 v98, 16, v2\n"
    "v_and_b32 v2, s92, v2\n"
    "v_lshlrev_b32 v99, 16, v3\n"
    "v_and_b32 v3, s92, v3\n"
    "s_waitcnt vmcnt(22)\n"
    "v_lshlrev_b32 v100, 16, v4\n"
    "v_and_b32 v4, s92, v4\n"
    "v_lshlrev_b32 v101, 16, v5\n"
    "v_and_b32 v5, s92, v5\n"
    "v_lshlrev_b32 v102, 16, v6\n"
    "v_and_b32 v6, s92, v6\n"
    "v_lshlrev_b32 v103, 16, v7\n"
    "v_and_b32 v7, s92, v7\n"
    "s_waitcnt vmcnt(21)\n"
    "v_lshlrev_b32 v104, 16, v8\n"
    "v_and_b32 v8, s92, v8\n"
    "v_lshlrev_b32 v105, 16, v9\n"
    "v_and_b32 v9, s92, v9\n"
    "v_lshlrev_b32 v106, 16, v10\n"
    "v_and_b32 v10, s92, v10\n"
    "v_lshlrev_b32 v107, 16, v11\n"
    "v_and_b32 v11, s92, v11\n"
    "s_waitcnt vmcnt(20)\n"
    "v_lshlrev_b32 v108, 16, v12\n"
    "v_and_b32 v12, s92, v12\n"
    "v_lshlrev_b32 v109, 16, v13\n"
    "v_and_b32 v13, s92, v13\n"
    "v_lshlrev_b32 v110, 16, v14\n"
    "v_and_b32 v14, s92, v14\n"
    "v_lshlrev_b32 v111, 16, v15\n"
    "v_and_b32 v15, s92, v15\n"
    "v_pk_add_f32 v[96:97], v[96:97], v[108:109]\n"
    "v_pk_add_f32 v[98:99], v[98:99], v[110:111]\n"
    "v_pk_add_f32 v[0:1], v[0:1], v[12:13]\n"
    "v_pk_add_f32 v[2:3], v[2:3], v[14:15]\n"
    "s_waitcnt vmcnt(19)\n"
    "v_lshlrev_b32 v112, 16, v16\n"
    "v_and_b32 v16, s92, v16\n"
    "v_lshlrev_b32 v113, 16, v17\n"
    "v_and_b32 v17, s92, v17\n"
    "v_lshlrev_b32 v114, 16, v18\n"
    "v_and_b32 v18, s92, v18\n"
    "v_lshlrev_b32 v115, 16, v19\n"
    "v_and_b32 v19, s92, v19\n"
    "v_pk_add_f32 v[100:101], v[100:101], v[112:113]\n"
    "v_pk_add_f32 v[102:103], v[102:103], v[114:115]\n"
    "v_pk_add_f32 v[4:5], v[4:5], v[16:17]\n"
    "v_pk_add_f32 v[6:7], v[6:7], v[18:19]\n"
    "s_waitcnt vmcnt(18)\n"
    "v_lshlrev_b32 v116, 16, v20\n"
    "v_and_b32 v20, s92, v20\n"
    "v_lshlrev_b32 v117, 16, v21\n"
    "v_and_b32 v21, s92, v21\n"
    "v_lshlrev_b32 v118, 16, v22\n"
    "v_and_b32 v22, s92, v22\n"
    "v_lshlrev_b32 v119, 16, v23\n"
    "v_and_b32 v23, s92, v23\n"
    "v_pk_add_f32 v[104:105], v[104:105], v[116:117]\n"
    "v_pk_add_f32 v[106:107], v[106:107], v[118:119]\n"
    "v_pk_add_f32 v[8:9], v[8:9], v[20:21]\n"
    "v_pk_add_f32 v[10:11], v[10:11], v[22:23]\n"
    "s_waitcnt vmcnt(17)\n"
    "v_lshlrev_b32 v120, 16, v24\n"
    "v_and_b32 v24, s92, v24\n"
    "v_lshlrev_b32 v121, 16, v25\n"
    "v_and_b32 v25, s92, v25\n"
    "v_lshlrev_b32 v122, 16, v26\n"
    "v_and_b32 v26, s92, v26\n"
    "v_lshlrev_b32 v123, 16, v27\n"
    "v_and_b32 v27, s92, v27\n"
    "v_pk_add_f32 v[96:97], v[96:97], v[120:121]\n"
    "v_pk_add_f32 v[98:99], v[98:99], v[122:123]\n"
    "v_pk_add_f32 v[0:1], v[0:1], v[24:25]\n"
    "v_pk_add_f32 v[2:3], v[2:3], v[26:27]\n"
    "s_waitcnt vmcnt(16)\n"
    "v_lshlrev_b32 v124, 16, v28\n"
    "v_and_b32 v28, s92, v28\n"
    "v_lshlrev_b32 v125, 16, v29\n"
    "v_and_b32 v29, s92, v29\n"
    "v_lshlrev_b32 v126, 16, v30\n"
    "v_and_b32 v30, s92, v30\n"
    "v_lshlrev_b32 v127, 16, v31\n"
    "v_and_b32 v31, s92, v31\n"
    "v_pk_add_f32 v[100:101], v[100:101], v[124:125]\n"
    "v_pk_add_f32 v[102:103], v[102:103], v[126:127]\n"
    "v_pk_add_f32 v[4:5], v[4:5], v[28:29]\n"
    "v_pk_add_f32 v[6:7], v[6:7], v[30:31]\n"
    "s_waitcnt vmcnt(15)\n"
    "v_lshlrev_b32 v128, 16, v32\n"
    "v_and_b32 v32, s92, v32\n"
    "v_lshlrev_b32 v129, 16, v33\n"
    "v_and_b32 v33, s92, v33\n"
    "v_lshlrev_b32 v130, 16, v34\n"
    "v_and_b32 v34, s92, v34\n"
    "v_lshlrev_b32 v131, 16, v35\n"
    "v_and_b32 v35, s92, v35\n"
    "v_pk_add_f32 v[104:105], v[104:105], v[128:129]\n"
    "v_pk_add_f32 v[106:107], v[106:107], v[130:131]\n"
    "v_pk_add_f32 v[8:9], v[8:9], v[32:33]\n"
    "v_pk_add_f32 v[10:11], v[10:11], v[34:35]\n"
    "s_waitcnt vmcnt(14)\n"
    "v_lshlrev_b32 v132, 16, v36\n"
    "v_and_b32 v36, s92, v36\n"
    "v_lshlrev_b32 v133, 16, v37\n"
    "v_and_b32 v37, s92, v37\n"
    "v_lshlrev_b32 v134, 16, v38\n"
    "v_and_b32 v38, s92, v38\n"
    "v_lshlrev_b32 v135, 16, v39\n"
    "v_and_b32 v39, s92, v39\n"
    "v_pk_add_f32 v[96:97], v[96:97], v[132:133]\n"
    "v_pk_add_f32 v[98:99], v[98:99], v[134:135]\n"
    "v_pk_add_f32 v[0:1], v[0:1], v[36:37]\n"
    "v_pk_add_f32 v[2:3], v[2:3], v[38:39]\n"
    "s_waitcnt vmcnt(13)\n"
    "v_lshlrev_b32 v136, 16, v40\n"
    "v_and_b32 v40, s92, v40\n"
    "v_lshlrev_b32 v137, 16, v41\n"
    "v_and_b32 v41, s92, v41\n"
    "v_lshlrev_b32 v138, 16, v42\n"
    "v_and_b32 v42, s92, v42\n"
    "v_lshlrev_b32 v139, 16, v43\n"
    "v_and_b32 v43, s92, v43\n"
    "v_pk_add_f32 v[100:101], v[100:101], v[136:137]\n"
    "v_pk_add_f32 v[102:103], v[102:103], v[138:139]\n"
    "v_pk_add_f32 v[4:5], v[4:5], v[40:41]\n"
    "v_pk_add_f32 v[6:7], v[6:7], v[42:43]\n"
    "s_waitcnt vmcnt(12)\n"
    "v_lshlrev_b32 v140, 16, v44\n"
    "v_and_b32 v44, s92, v44\n"
    "v_lshlrev_b32 v141, 16, v45\n"
    "v_and_b32 v45, s92, v45\n"
    "v_lshlrev_b32 v142, 16, v46\n"
    "v_and_b32 v46, s92, v46\n"
    "v_lshlrev_b32 v143, 16, v47\n"
    "v_and_b32 v47, s92, v47\n"
    "v_pk_add_f32 v[104:105], v[104:105], v[140:141]\n"
    "v_pk_add_f32 v[106:107], v[106:107], v[142:143]\n"
    "v_pk_add_f32 v[8:9], v[8:9], v[44:45]\n"
    "v_pk_add_f32 v[10:11], v[10:11], v[46:47]\n"
    "s_waitcnt vmcnt(11)\n"
    "v_lshlrev_b32 v144, 16, v48\n"
    "v_and_b32 v48, s92, v48\n"
    "v_lshlrev_b32 v145, 16, v49\n"
    "v_and_b32 v49, s92, v49\n"
    "v_lshlrev_b32 v146, 16, v50\n"
    "v_and_b32 v50, s92, v50\n"
    "v_lshlrev_b32 v147, 16, v51\n"
    "v_and_b32 v51, s92, v51\n"
    "v_pk_add_f32 v[96:97], v[96:97], v[144:145]\n"
    "v_pk_add_f32 v[98:99], v[98:99], v[146:147]\n"
    "v_pk_add_f32 v[0:1], v[0:1], v[48:49]\n"
    "v_pk_add_f32 v[2:3], v[2:3], v[50:51]\n"
    "s_waitcnt vmcnt(10)\n"
    "v_lshlrev_b32 v148, 16, v52\n"
    "v_and_b32 v52, s92, v52\n"
    "v_lshlrev_b32 v149, 16, v53\n"
    "v_and_b32 v53, s92, v53\n"
    "v_lshlrev_b32 v150, 16, v54\n"
    "v_and_b32 v54, s92, v54\n"
    "v_lshlrev_b32 v151, 16, v55\n"
    "v_and_b32 v55, s92, v55\n"
    "v_pk_add_f32 v[100:101], v[100:101], v[148:149]\n"
    "v_pk_add_f32 v[102:103], v[102:103], v[150:151]\n"
    "v_pk_add_f32 v[4:5], v[4:5], v[52:53]\n"
    "v_pk_add_f32 v[6:7], v[6:7], v[54:55]\n"
    "s_waitcnt vmcnt(9)\n"
    "v_lshlrev_b32 v152, 16, v56\n"
    "v_and_b32 v56, s92, v56\n"
    "v_lshlrev_b32 v153, 16, v57\n"
    "v_and_b32 v57, s92, v57\n"
    "v_lshlrev_b32 v154, 16, v58\n"
    "v_and_b32 v58, s92, v58\n"
    "v_lshlrev_b32 v155, 16, v59\n"
    "v_and_b32 v59, s92, v59\n"
    "v_pk_add_f32 v[104:105], v[104:105], v[152:153]\n"
    "v_pk_add_f32 v[106:107], v[106:107], v[154:155]\n"
    "v_pk_add_f32 v[8:9], v[8:9], v[56:57]\n"
    "v_pk_add_f32 v[10:11], v[10:11], v[58:59]\n"
    "s_waitcnt vmcnt(8)\n"
    "v_lshlrev_b32 v156, 16, v60\n"
    "v_and_b32 v60, s92, v60\n"
    "v_lshlrev_b32 v157, 16, v61\n"
    "v_and_b32 v61, s92, v61\n"
    "v_lshlrev_b32 v158, 16, v62\n"
    "v_and_b32 v62, s92, v62\n"
    "v_lshlrev_b32 v159, 16, v63\n"
    "v_and_b32 v63, s92, v63\n"
    "v_pk_add_f32 v[96:97], v[96:97], v[156:157]\n"
    "v_pk_add_f32 v[98:99], v[98:99], v[158:159]\n"
    "v_pk_add_f32 v[0:1], v[0:1], v[60:61]\n"
    "v_pk_add_f32 v[2:3], v[2:3], v[62:63]\n"
    "s_waitcnt vmcnt(7)\n"
    "v_lshlrev_b32 v160, 16, v64\n"
    "v_and_b32 v64, s92, v64\n"
    "v_lshlrev_b32 v161, 16, v65\n"
    "v_and_b32 v65, s92, v65\n"
    "v_lshlrev_b32 v162, 16, v66\n"
    "v_and_b32 v66, s92, v66\n"
    "v_lshlrev_b32 v163, 16, v67\n"
    "v_and_b32 v67, s92, v67\n"
    "v_pk_add_f32 v[100:101], v[100:101], v[160:161]\n"
    "v_pk_add_f32 v[102:103], v[102:103], v[162:163]\n"
    "v_pk_add_f32 v[4:5], v[4:5], v[64:65]\n"
    "v_pk_add_f32 v[6:7], v[6:7], v[66:67]\n"
    "s_waitcnt vmcnt(6)\n"
    "v_lshlrev_b32 v164, 16, v68\n"
    "v_and_b32 v68, s92, v68\n"
    "v_lshlrev_b32 v165, 16, v69\n"
    "v_and_b32 v69, s92, v69\n"
    "v_lshlrev_b32 v166, 16, v70\n"
    "v_and_b32 v70, s92, v70\n"
    "v_lshlrev_b32 v167, 16, v71\n"
    "v_and_b32 v71, s92, v71\n"
    "v_pk_add_f32 v[104:105], v[104:105], v[164:165]\n"
    "v_pk_add_f32 v[106:107], v[106:107], v[166:167]\n"
    "v_pk_add_f32 v[8:9], v[8:9], v[68:69]\n"
    "v_pk_add_f32 v[10:11], v[10:11], v[70:71]\n"
    "s_waitcnt vmcnt(5)\n"
    "v_lshlrev_b32 v168, 16, v72\n"
    "v_and_b32 v72, s92, v72\n"
    "v_lshlrev_b32 v169, 16, v73\n"
    "v_and_b32 v73, s92, v73\n"
    "v_lshlrev_b32 v170, 16, v74\n"
    "v_and_b32 v74, s92, v74\n"
    "v_lshlrev_b32 v171, 16, v75\n"
    "v_and_b32 v75, s92, v75\n"
    "v_pk_add_f32 v[96:97], v[96:97], v[168:169]\n"
    "v_pk_add_f32 v[98:99], v[98:99], v[170:171]\n"
    "v_pk_add_f32 v[0:1], v[0:1], v[72:73]\n"
    "v_pk_add_f32 v[2:3], v[2:3], v[74:75]\n"
    "s_waitcnt vmcnt(4)\n"
    "v_lshlrev_b32 v172, 16, v76\n"
    "v_and_b32 v76, s92, v76\n"
    "v_lshlrev_b32 v173, 16, v77\n"
    "v_and_b32 v77, s92, v77\n"
    "v_lshlrev_b32 v174, 16, v78\n"
    "v_and_b32 v78, s92, v78\n"
    "v_lshlrev_b32 v175, 16, v79\n"
    "v_and_b32 v79, s92, v79\n"
    "v_pk_add_f32 v[100:101], v[100:101], v[172:173]\n"
    "v_pk_add_f32 v[102:103], v[102:103], v[174:175]\n"
    "v_pk_add_f32 v[4:5], v[4:5], v[76:77]\n"
    "v_pk_add_f32 v[6:7], v[6:7], v[78:79]\n"
    "s_waitcnt vmcnt(3)\n"
    "v_lshlrev_b32 v176, 16, v80\n"
    "v_and_b32 v80, s92, v80\n"
    "v_lshlrev_b32 v177, 16, v81\n"
    "v_and_b32 v81, s92, v81\n"
    "v_lshlrev_b32 v178, 16, v82\n"
    "v_and_b32 v82, s92, v82\n"
    "v_lshlrev_b32 v179, 16, v83\n"
    "v_and_b32 v83, s92, v83\n"
    "v_pk_add_f32 v[104:105], v[104:105], v[176:177]\n"
    "v_pk_add_f32 v[106:107], v[106:107], v[178:179]\n"
    "v_pk_add_f32 v[8:9], v[8:9], v[80:81]\n"
    "v_pk_add_f32 v[10:11], v[10:11], v[82:83]\n"
    "s_waitcnt vmcnt(2)\n"
    "v_lshlrev_b32 v180, 16, v84\n"
    "v_and_b32 v84, s92, v84\n"
    "v_lshlrev_b32 v181, 16, v85\n"
    "v_and_b32 v85, s92, v85\n"
    "v_lshlrev_b32 v182, 16, v86\n"
    "v_and_b32 v86, s92, v86\n"
    "v_lshlrev_b32 v183, 16, v87\n"
    "v_and_b32 v87, s92, v87\n"
    "v_pk_add_f32 v[96:97], v[96:97], v[180:181]\n"
    "v_pk_add_f32 v[98:99], v[98:99], v[182:183]\n"
    "v_pk_add_f32 v[0:1], v[0:1], v[84:85]\n"
    "v_pk_add_f32 v[2:3], v[2:3], v[86:87]\n"
    "v_cvt_pk_bf16_f32 v0, v96, v0\n"
    "v_cvt_pk_bf16_f32 v1, v97, v1\n"
    "v_cvt_pk_bf16_f32 v2, v98, v2\n"
    "v_cvt_pk_bf16_f32 v3, v99, v3\n"
    "buffer_store_dwordx4 v[0:3], v227, s[64:67], 0 offen sc1\n"
    "s_waitcnt vmcnt(2)\n"
    "v_lshlrev_b32 v184, 16, v88\n"
    "v_and_b32 v88, s92, v88\n"
    "v_lshlrev_b32 v185, 16, v89\n"
    "v_and_b32 v89, s92, v89\n"
    "v_lshlrev_b32 v186, 16, v90\n"
    "v_and_b32 v90, s92, v90\n"
    "v_lshlrev_b32 v187, 16, v91\n"
    "v_and_b32 v91, s92, v91\n"
    "v_pk_add_f32 v[100:101], v[100:101], v[184:185]\n"
    "v_pk_add_f32 v[102:103], v[102:103], v[186:187]\n"
    "v_pk_add_f32 v[4:5], v[4:5], v[88:89]\n"
    "v_pk_add_f32 v[6:7], v[6:7], v[90:91]\n"
    "v_cvt_pk_bf16_f32 v4, v100, v4\n"
    "v_cvt_pk_bf16_f32 v5, v101, v5\n"
    "v_cvt_pk_bf16_f32 v6, v102, v6\n"
    "v_cvt_pk_bf16_f32 v7, v103, v7\n"
    "buffer_store_dwordx4 v[4:7], v228, s[64:67], 0 offen sc1\n"
    "s_waitcnt vmcnt(2)\n"
    "v_lshlrev_b32 v188, 16, v92\n"
    "v_and_b32 v92, s92, v92\n"
    "v_lshlrev_b32 v189, 16, v93\n"
    "v_and_b32 v93, s92, v93\n"
    "v_lshlrev_b32 v190, 16, v94\n"
    "v_and_b32 v94, s92, v94\n"
    "v_lshlrev_b32 v191, 16, v95\n"
    "v_and_b32 v95, s92, v95\n"
    "v_pk_add_f32 v[104:105], v[104:105], v[188:189]\n"
    "v_pk_add_f32 v[106:107], v[106:107], v[190:191]\n"
    "v_pk_add_f32 v[8:9], v[8:9], v[92:93]\n"
    "v_pk_add_f32 v[10:11], v[10:11], v[94:95]\n"
    "v_cvt_pk_bf16_f32 v8, v104, v8\n"
    "v_cvt_pk_bf16_f32 v9, v105, v9\n"
    "v_cvt_pk_bf16_f32 v10, v106, v10\n"
    "v_cvt_pk_bf16_f32 v11, v107, v11\n"
    "buffer_store_dwordx4 v[8:11], v229, s[64:67], 0 offen sc1\n"
    "L_fin%=:\n"
    "s_barrier\n"
    "s_cmp_lg_u32 %2, 0\n"
    "s_cbranch_scc1 L_noep%=\n"
    "s_sub_u32 s90, s80, 0x9e3779b9\n"
    "s_subb_u32 s91, s81, 0x7f4a7c15\n"
    "v_mov_b32 v236, s90\n"
    "v_mov_b32 v237, s91\n"
    "s_mov_b64 exec, 255\n"
    "buffer_store_dwordx2 v[236:237], v241, s[60:63], 0 offen sc1\n"
    "s_mov_b64 exec, -1\n"
    "s_or_b32 s90, %2, s5\n"
    "s_cmp_lg_u32 s90, 0\n"
    "s_cbranch_scc1 L_noep%=\n"
    "s_mov_b64 exec, 1\n"
    "buffer_store_dwordx2 v[234:235], v233, s[60:63], 0 offen offset:1024 sc1\n"
    "s_mov_b64 exec, -1\n"
    "L_noep%=:\n"
    "s_branch L_end%=\n"
    "L_nq0_%=:\n"
    "s_memrealtime s[96:97]\n"
    "s_waitcnt lgkmcnt(0)\n"
    "s_sub_u32 s97, s35, s96\n"
    "s_sub_u32 s97, s97, 1\n"
    "s_cmp_lt_u32 s97, 46\n"
    "s_cbranch_scc1 L_nq0_%=\n"
    "s_add_u32 s35, s96, 46\n"
    "s_branch L_nq0r_%=\n"
    "L_nq1_%=:\n"
    "s_memrealtime s[96:97]\n"
    "s_waitcnt lgkmcnt(0)\n"
    "s_sub_u32 s97, s35, s96\n"
    "s_sub_u32 s97, s97, 1\n"
    "s_cmp_lt_u32 s97, 46\n"
    "s_cbranch_scc1 L_nq1_%=\n"
    "s_add_u32 s35, s96, 46\n"
    "s_branch L_nq1r_%=\n"
    "L_nq2_%=:\n"
    "s_memrealtime s[96:97]\n"
    "s_waitcnt lgkmcnt(0)\n"
    "s_sub_u32 s97, s35, s96\n"
    "s_sub_u32 s97, s97, 1\n"
    "s_cmp_lt_u32 s97, 46\n"
    "s_cbranch_scc1 L_nq2_%=\n"
    "s_add_u32 s35, s96, 46\n"
    "s_branch L_nq2r_%=\n"
    "L_rareA_h5%=:\n"
    "s_call_b64 s[96:97], L_rb0_1_0%=\n"
    "s_branch L_rareA_h5_r%=\n"
    "L_rb0_1_0%=:\n"
    "s_nop 7\n"
    "v_max3_f32 v237, v168, v169, v170\n"
    "v_max3_f32 v238, v171, v172, v173\n"
    "v_max3_f32 v237, v237, v174, v175\n"
    "v_max_f32 v237, v237, v238\n"
    "v_mov_b32 v238, v237\n"
    "s_nop 1\n"
    "v_permlane16_swap_b32 v237, v238\n"
    "v_max_f32 v237, v237, v238\n"
    "v_mov_b32 v238, v237\n"
    "s_nop 1\n"
    "v_permlane32_swap_b32 v237, v238\n"
    "v_max_f32 v237, v237, v238\n"
    "v_max_f32 v237, 0, v237\n"
    "v_sub_f32 v168, v168, v237\n"
    "v_sub_f32 v169, v169, v237\n"
    "v_sub_f32 v170, v170, v237\n"
    "v_sub_f32 v171, v171, v237\n"
    "v_sub_f32 v172, v172, v237\n"
    "v_sub_f32 v173, v173, v237\n"
    "v_sub_f32 v174, v174, v237\n"
    "v_sub_f32 v175, v175, v237\n"
    "v_exp_f32 v176, v168\n"
    "v_exp_f32 v177, v169\n"
    "v_exp_f32 v178, v170\n"
    "v_exp_f32 v179, v171\n"
    "v_exp_f32 v180, v172\n"
    "v_exp_f32 v181, v173\n"
    "v_exp_f32 v182, v174\n"
    "v_exp_f32 v183, v175\n"
    "v_sub_f32 v192, v192, v237\n"
    "v_sub_f32 v193, v193, v237\n"
    "v_sub_f32 v194, v194, v237\n"
    "v_sub_f32 v195, v195, v237\n"
    "v_exp_f32 v238, -v237\n"
    "v_add_f32 v184, v176, v177\n"
    "v_add_f32 v185, v178, v179\n"
    "v_add_f32 v186, v180, v181\n"
    "v_add_f32 v187, v182, v183\n"
    "v_add_f32 v184, v184, v185\n"
    "v_add_f32 v186, v186, v187\n"
    "v_add_f32 v184, v184, v186\n"
    "v_cvt_pk_bf16_f32 v228, v176, v177\n"
    "v_cvt_pk_bf16_f32 v229, v178, v179\n"
    "v_cvt_pk_bf16_f32 v230, v180, v181\n"
    "v_cvt_pk_bf16_f32 v231, v182, v183\n"
    "v_mul_f32 v232, v232, v238\n"
    "v_mul_f32 v0, v0, v238\n"
    "v_mul_f32 v1, v1, v238\n"
    "v_mul_f32 v2, v2, v238\n"
    "v_mul_f32 v3, v3, v238\n"
    "v_mul_f32 v4, v4, v238\n"
    "v_mul_f32 v5, v5, v238\n"
    "v_mul_f32 v6, v6, v238\n"
    "v_mul_f32 v7, v7, v238\n"
    "v_mul_f32 v8, v8, v238\n"
    "v_mul_f32 v9, v9, v238\n"
    "v_mul_f32 v10, v10, v238\n"
    "v_mul_f32 v11, v11, v238\n"
    "v_mul_f32 v12, v12, v238\n"
    "v_mul_f32 v13, v13, v238\n"
    "v_mul_f32 v14, v14, v238\n"
    "v_mul_f32 v15, v15, v238\n"
    "v_mul_f32 v16, v16, v238\n"
    "v_mul_f32 v17, v17, v238\n"
    "v_mul_f32 v18, v18, v238\n"
    "v_mul_f32 v19, v19, v238\n"
    "v_mul_f32 v20, v20, v238\n"
    "v_mul_f32 v21, v21, v238\n"
    "v_mul_f32 v22, v22, v238\n"
    "v_mul_f32 v23, v23, v238\n"
    "v_mul_f32 v24, v24, v238\n"
    "v_mul_f32 v25, v25, v238\n"
    "v_mul_f32 v26, v26, v238\n"
    "v_mul_f32 v27, v27, v238\n"
    "v_mul_f32 v28, v28, v238\n"
    "v_mul_f32 v29, v29, v238\n"
    "v_mul_f32 v30, v30, v238\n"
    "v_mul_f32 v31, v31, v238\n"
    "s_nop 1\n"
    "s_setpc_b64 s[96:97]\n"
    "L_rareA_l0%=:\n"
    "s_call_b64 s[96:97], L_rb1_0_0%=\n"
    "s_branch L_rareA_l0_r%=\n"
    "L_rb1_0_0%=:\n"
    "s_nop 7\n"
    "v_max3_f32 v237, v160, v161, v162\n"
    "v_max3_f32 v238, v163, v164, v165\n"
    "v_max3_f32 v237, v237, v166, v167\n"
    "v_max_f32 v237, v237, v238\n"
    "v_mov_b32 v238, v237\n"
    "s_nop 1\n"
    "v_permlane16_swap_b32 v237, v238\n"
    "v_max_f32 v237, v237, v238\n"
    "v_mov_b32 v238, v237\n"
    "s_nop 1\n"
    "v_permlane32_swap_b32 v237, v238\n"
    "v_max_f32 v237, v237, v238\n"
    "v_max_f32 v237, 0, v237\n"
    "v_sub_f32 v160, v160, v237\n"
    "v_sub_f32 v161, v161, v237\n"
    "v_sub_f32 v162, v162, v237\n"
    "v_sub_f32 v163, v163, v237\n"
    "v_sub_f32 v164, v164, v237\n"
    "v_sub_f32 v165, v165, v237\n"
    "v_sub_f32 v166, v166, v237\n"
    "v_sub_f32 v167, v167, v237\n"
    "v_exp_f32 v176, v160\n"
    "v_exp_f32 v177, v161\n"
    "v_exp_f32 v178, v162\n"
    "v_exp_f32 v179, v163\n"
    "v_exp_f32 v180, v164\n"
    "v_exp_f32 v181, v165\n"
    "v_exp_f32 v182, v166\n"
    "v_exp_f32 v183, v167\n"
    "v_sub_f32 v196, v196, v237\n"
    "v_sub_f32 v197, v197, v237\n"
    "v_sub_f32 v198, v198, v237\n"
    "v_sub_f32 v199, v199, v237\n"
    "v_exp_f32 v238, -v237\n"
    "v_add_f32 v184, v176, v177\n"
    "v_add_f32 v185, v178, v179\n"
    "v_add_f32 v186, v180, v181\n"
    "v_add_f32 v187, v182, v183\n"
    "v_add_f32 v184, v184, v185\n"
    "v_add_f32 v186, v186, v187\n"
    "v_add_f32 v184, v184, v186\n"
    "v_cvt_pk_bf16_f32 v224, v176, v177\n"
    "v_cvt_pk_bf16_f32 v225, v178, v179\n"
    "v_cvt_pk_bf16_f32 v226, v180, v181\n"
    "v_cvt_pk_bf16_f32 v227, v182, v183\n"
    "v_mul_f32 v233, v233, v238\n"
    "v_mul_f32 v32, v32, v238\n"
    "v_mul_f32 v33, v33, v238\n"
    "v_mul_f32 v34, v34, v238\n"
    "v_mul_f32 v35, v35, v238\n"
    "v_mul_f32 v36, v36, v238\n"
    "v_mul_f32 v37, v37, v238\n"
    "v_mul_f32 v38, v38, v238\n"
    "v_mul_f32 v39, v39, v238\n"
    "v_mul_f32 v40, v40, v238\n"
    "v_mul_f32 v41, v41, v238\n"
    "v_mul_f32 v42, v42, v238\n"
    "v_mul_f32 v43, v43, v238\n"
    "v_mul_f32 v44, v44, v238\n"
    "v_mul_f32 v45, v45, v238\n"
    "v_mul_f32 v46, v46, v238\n"
    "v_mul_f32 v47, v47, v238\n"
    "v_mul_f32 v48, v48, v238\n"
    "v_mul_f32 v49, v49, v238\n"
    "v_mul_f32 v50, v50, v238\n"
    "v_mul_f32 v51, v51, v238\n"
    "v_mul_f32 v52, v52, v238\n"
    "v_mul_f32 v53, v53, v238\n"
    "v_mul_f32 v54, v54, v238\n"
    "v_mul_f32 v55, v55, v238\n"
    "v_mul_f32 v56, v56, v238\n"
    "v_mul_f32 v57, v57, v238\n"
    "v_mul_f32 v58, v58, v238\n"
    "v_mul_f32 v59, v59, v238\n"
    "v_mul_f32 v60, v60, v238\n"
    "v_mul_f32 v61, v61, v238\n"
    "v_mul_f32 v62, v62, v238\n"
    "v_mul_f32 v63, v63, v238\n"
    "s_nop 1\n"
    "s_setpc_b64 s[96:97]\n"
    "L_rareA_l1%=:\n"
    "s_call_b64 s[96:97], L_rb2_1_0%=\n"
    "s_branch L_rareA_l1_r%=\n"
    "L_rb2_1_0%=:\n"
    "s_nop 7\n"
    "v_max3_f32 v237, v168, v169, v170\n"
    "v_max3_f32 v238, v171, v172, v173\n"
    "v_max3_f32 v237, v237, v174, v175\n"
    "v_max_f32 v237, v237, v238\n"
    "v_mov_b32 v238, v237\n"
    "s_nop 1\n"
    "v_permlane16_swap_b32 v237, v238\n"
    "v_max_f32 v237, v237, v238\n"
    "v_mov_b32 v238, v237\n"
    "s_nop 1\n"
    "v_permlane32_swap_b32 v237, v238\n"
    "v_max_f32 v237, v237, v238\n"
    "v_max_f32 v237, 0, v237\n"
    "v_sub_f32 v168, v168, v237\n"
    "v_sub_f32 v169, v169, v237\n"
    "v_sub_f32 v170, v170, v237\n"
    "v_sub_f32 v171, v171, v237\n"
    "v_sub_f32 v172, v172, v237\n"
    "v_sub_f32 v173, v173, v237\n"
    "v_sub_f32 v174, v174, v237\n"
    "v_sub_f32 v175, v175, v237\n"
    "v_exp_f32 v176, v168\n"
    "v_exp_f32 v177, v169\n"
    "v_exp_f32 v178, v170\n"
    "v_exp_f32 v179, v171\n"
    "v_exp_f32 v180, v172\n"
    "v_exp_f32 v181, v173\n"
    "v_exp_f32 v182, v174\n"
    "v_exp_f32 v183, v175\n"
    "v_sub_f32 v200, v200, v237\n"
    "v_sub_f32 v201, v201, v237\n"
    "v_sub_f32 v202, v202, v237\n"
    "v_sub_f32 v203, v203, v237\n"
    "v_exp_f32 v238, -v237\n"
    "v_add_f32 v184, v176, v177\n"
    "v_add_f32 v185, v178, v179\n"
    "v_add_f32 v186, v180, v181\n"
    "v_add_f32 v187, v182, v183\n"
    "v_add_f32 v184, v184, v185\n"
    "v_add_f32 v186, v186, v187\n"
    "v_add_f32 v184, v184, v186\n"
    "v_cvt_pk_bf16_f32 v228, v176, v177\n"
    "v_cvt_pk_bf16_f32 v229, v178, v179\n"
    "v_cvt_pk_bf16_f32 v230, v180, v181\n"
    "v_cvt_pk_bf16_f32 v231, v182, v183\n"
    "v_mul_f32 v234, v234, v238\n"
    "v_mul_f32 v64, v64, v238\n"
    "v_mul_f32 v65, v65, v238\n"
    "v_mul_f32 v66, v66, v238\n"
    "v_mul_f32 v67, v67, v238\n"
    "v_mul_f32 v68, v68, v238\n"
    "v_mul_f32 v69, v69, v238\n"
    "v_mul_f32 v70, v70, v238\n"
    "v_mul_f32 v71, v71, v238\n"
    "v_mul_f32 v72, v72, v238\n"
    "v_mul_f32 v73, v73, v238\n"
    "v_mul_f32 v74, v74, v238\n"
    "v_mul_f32 v75, v75, v238\n"
    "v_mul_f32 v76, v76, v238\n"
    "v_mul_f32 v77, v77, v238\n"
    "v_mul_f32 v78, v78, v238\n"
    "v_mul_f32 v79, v79, v238\n"
    "v_mul_f32 v80, v80, v238\n"
    "v_mul_f32 v81, v81, v238\n"
    "v_mul_f32 v82, v82, v238\n"
    "v_mul_f32 v83, v83, v238\n"
    "v_mul_f32 v84, v84, v238\n"
    "v_mul_f32 v85, v85, v238\n"
    "v_mul_f32 v86, v86, v238\n"
    "v_mul_f32 v87, v87, v238\n"
    "v_mul_f32 v88, v88, v238\n"
    "v_mul_f32 v89, v89, v238\n"
    "v_mul_f32 v90, v90, v238\n"
    "v_mul_f32 v91, v91, v238\n"
    "v_mul_f32 v92, v92, v238\n"
    "v_mul_f32 v93, v93, v238\n"
    "v_mul_f32 v94, v94, v238\n"
    "v_mul_f32 v95, v95, v238\n"
    "s_nop 1\n"
    "s_setpc_b64 s[96:97]\n"
    "L_rareA_l2%=:\n"
    "s_call_b64 s[96:97], L_rb3_0_0%=\n"
    "s_branch L_rareA_l2_r%=\n"
    "L_rb3_0_0%=:\n"
    "s_nop 7\n"
    "v_max3_f32 v237, v160, v161, v162\n"
    "v_max3_f32 v238, v163, v164, v165\n"
    "v_max3_f32 v237, v237, v166, v167\n"
    "v_max_f32 v237, v237, v238\n"
    "v_mov_b32 v238, v237\n"
    "s_nop 1\n"
    "v_permlane16_swap_b32 v237, v238\n"
    "v_max_f32 v237, v237, v238\n"
    "v_mov_b32 v238, v237\n"
    "s_nop 1\n"
    "v_permlane32_swap_b32 v237, v238\n"
    "v_max_f32 v237, v237, v238\n"
    "v_max_f32 v237, 0, v237\n"
    "v_sub_f32 v160, v160, v237\n"
    "v_sub_f32 v161, v161, v237\n"
    "v_sub_f32 v162, v162, v237\n"
    "v_sub_f32 v163, v163, v237\n"
    "v_sub_f32 v164, v164, v237\n"
    "v_sub_f32 v165, v165, v237\n"
    "v_sub_f32 v166, v166, v237\n"
    "v_sub_f32 v167, v167, v237\n"
    "v_exp_f32 v176, v160\n"
    "v_exp_f32 v177, v161\n"
    "v_exp_f32 v178, v162\n"
    "v_exp_f32 v179, v163\n"
    "v_exp_f32 v180, v164\n"
    "v_exp_f32 v181, v165\n"
    "v_exp_f32 v182, v166\n"
    "v_exp_f32 v183, v167\n"
    "v_sub_f32 v204, v204, v237\n"
    "v_sub_f32 v205, v205, v237\n"
    "v_sub_f32 v206, v206, v237\n"
    "v_sub_f32 v207, v207, v237\n"
    "v_exp_f32 v238, -v237\n"
    "v_add_f32 v184, v176, v177\n"
    "v_add_f32 v185, v178, v179\n"
    "v_add_f32 v186, v180, v181\n"
    "v_add_f32 v187, v182, v183\n"
    "v_add_f32 v184, v184, v185\n"
    "v_add_f32 v186, v186, v187\n"
    "v_add_f32 v184, v184, v186\n"
    "v_cvt_pk_bf16_f32 v224, v176, v177\n"
    "v_cvt_pk_bf16_f32 v225, v178, v179\n"
    "v_cvt_pk_bf16_f32 v226, v180, v181\n"
    "v_cvt_pk_bf16_f32 v227, v182, v183\n"
    "v_mul_f32 v235, v235, v238\n"
    "v_mul_f32 v96, v96, v238\n"
    "v_mul_f32 v97, v97, v238\n"
    "v_mul_f32 v98, v98, v238\n"
    "v_mul_f32 v99, v99, v238\n"
    "v_mul_f32 v100, v100, v238\n"
    "v_mul_f32 v101, v101, v238\n"
    "v_mul_f32 v102, v102, v238\n"
    "v_mul_f32 v103, v103, v238\n"
    "v_mul_f32 v104, v104, v238\n"
    "v_mul_f32 v105, v105, v238\n"
    "v_mul_f32 v106, v106, v238\n"
    "v_mul_f32 v107, v107, v238\n"
    "v_mul_f32 v108, v108, v238\n"
    "v_mul_f32 v109, v109, v238\n"
    "v_mul_f32 v110, v110, v238\n"
    "v_mul_f32 v111, v111, v238\n"
    "v_mul_f32 v112, v112, v238\n"
    "v_mul_f32 v113, v113, v238\n"
    "v_mul_f32 v114, v114, v238\n"
    "v_mul_f32 v115, v115, v238\n"
    "v_mul_f32 v116, v116, v238\n"
    "v_mul_f32 v117, v117, v238\n"
    "v_mul_f32 v118, v118, v238\n"
    "v_mul_f32 v119, v119, v238\n"
    "v_mul_f32 v120, v120, v238\n"
    "v_mul_f32 v121, v121, v238\n"
    "v_mul_f32 v122, v122, v238\n"
    "v_mul_f32 v123, v123, v238\n"
    "v_mul_f32 v124, v124, v238\n"
    "v_mul_f32 v125, v125, v238\n"
    "v_mul_f32 v126, v126, v238\n"
    "v_mul_f32 v127, v127, v238\n"
    "s_nop 1\n"
    "s_setpc_b64 s[96:97]\n"
    "L_rareA_l3%=:\n"
    "s_call_b64 s[96:97], L_rb4_1_0%=\n"
    "s_branch L_rareA_l3_r%=\n"
    "L_rb4_1_0%=:\n"
    "s_nop 7\n"
    "v_max3_f32 v237, v168, v169, v170\n"
    "v_max3_f32 v238, v171, v172, v173\n"
    "v_max3_f32 v237, v237, v174, v175\n"
    "v_max_f32 v237, v237, v238\n"
    "v_mov_b32 v238, v237\n"
    "s_nop 1\n"
    "v_permlane16_swap_b32 v237, v238\n"
    "v_max_f32 v237, v237, v238\n"
    "v_mov_b32 v238, v237\n"
    "s_nop 1\n"
    "v_permlane32_swap_b32 v237, v238\n"
    "v_max_f32 v237, v237, v238\n"
    "v_max_f32 v237, 0, v237\n"
    "v_sub_f32 v168, v168, v237\n"
    "v_sub_f32 v169, v169, v237\n"
    "v_sub_f32 v170, v170, v237\n"
    "v_sub_f32 v171, v171, v237\n"
    "v_sub_f32 v172, v172, v237\n"
    "v_sub_f32 v173, v173, v237\n"
    "v_sub_f32 v174, v174, v237\n"
    "v_sub_f32 v175, v175, v237\n"
    "v_exp_f32 v176, v168\n"
    "v_exp_f32 v177, v169\n"
    "v_exp_f32 v178, v170\n"
    "v_exp_f32 v179, v171\n"
    "v_exp_f32 v180, v172\n"
    "v_exp_f32 v181, v173\n"
    "v_exp_f32 v182, v174\n"
    "v_exp_f32 v183, v175\n"
    "v_sub_f32 v208, v208, v237\n"
    "v_sub_f32 v209, v209, v237\n"
    "v_sub_f32 v210, v210, v237\n"
    "v_sub_f32 v211, v211, v237\n"
    "v_exp_f32 v238, -v237\n"
    "v_add_f32 v184, v176, v177\n"
    "v_add_f32 v185, v178, v179\n"
    "v_add_f32 v186, v180, v181\n"
    "v_add_f32 v187, v182, v183\n"
    "v_add_f32 v184, v184, v185\n"
    "v_add_f32 v186, v186, v187\n"
    "v_add_f32 v184, v184, v186\n"
    "v_cvt_pk_bf16_f32 v228, v176, v177\n"
    "v_cvt_pk_bf16_f32 v229, v178, v179\n"
    "v_cvt_pk_bf16_f32 v230, v180, v181\n"
    "v_cvt_pk_bf16_f32 v231, v182, v183\n"
    "v_mul_f32 v236, v236, v238\n"
    "v_mul_f32 v128, v128, v238\n"
    "v_mul_f32 v129, v129, v238\n"
    "v_mul_f32 v130, v130, v238\n"
    "v_mul_f32 v131, v131, v238\n"
    "v_mul_f32 v132, v132, v238\n"
    "v_mul_f32 v133, v133, v238\n"
    "v_mul_f32 v134, v134, v238\n"
    "v_mul_f32 v135, v135, v238\n"
    "v_mul_f32 v136, v136, v238\n"
    "v_mul_f32 v137, v137, v238\n"
    "v_mul_f32 v138, v138, v238\n"
    "v_mul_f32 v139, v139, v238\n"
    "v_mul_f32 v140, v140, v238\n"
    "v_mul_f32 v141, v141, v238\n"
    "v_mul_f32 v142, v142, v238\n"
    "v_mul_f32 v143, v143, v238\n"
    "v_mul_f32 v144, v144, v238\n"
    "v_mul_f32 v145, v145, v238\n"
    "v_mul_f32 v146, v146, v238\n"
    "v_mul_f32 v147, v147, v238\n"
    "v_mul_f32 v148, v148, v238\n"
    "v_mul_f32 v149, v149, v238\n"
    "v_mul_f32 v150, v150, v238\n"
    "v_mul_f32 v151, v151, v238\n"
    "v_mul_f32 v152, v152, v238\n"
    "v_mul_f32 v153, v153, v238\n"
    "v_mul_f32 v154, v154, v238\n"
    "v_mul_f32 v155, v155, v238\n"
    "v_mul_f32 v156, v156, v238\n"
    "v_mul_f32 v157, v157, v238\n"
    "v_mul_f32 v158, v158, v238\n"
    "v_mul_f32 v159, v159, v238\n"
    "s_nop 1\n"
    "s_setpc_b64 s[96:97]\n"
    "L_rareA_l4%=:\n"
    "s_call_b64 s[96:97], L_rb0_0_0%=\n"
    "s_branch L_rareA_l4_r%=\n"
    "L_rb0_0_0%=:\n"
    "s_nop 7\n"
    "v_max3_f32 v237, v160, v161, v162\n"
    "v_max3_f32 v238, v163, v164, v165\n"
    "v_max3_f32 v237, v237, v166, v167\n"
    "v_max_f32 v237, v237, v238\n"
    "v_mov_b32 v238, v237\n"
    "s_nop 1\n"
    "v_permlane16_swap_b32 v237, v238\n"
    "v_max_f32 v237, v237, v238\n"
    "v_mov_b32 v238, v237\n"
    "s_nop 1\n"
    "v_permlane32_swap_b32 v237, v238\n"
    "v_max_f32 v237, v237, v238\n"
    "v_max_f32 v237, 0, v237\n"
    "v_sub_f32 v160, v160, v237\n"
    "v_sub_f32 v161, v161, v237\n"
    "v_sub_f32 v162, v162, v237\n"
    "v_sub_f32 v163, v163, v237\n"
    "v_sub_f32 v164, v164, v237\n"
    "v_sub_f32 v165, v165, v237\n"
    "v_sub_f32 v166, v166, v237\n"
    "v_sub_f32 v167, v167, v237\n"
    "v_exp_f32 v176, v160\n"
    "v_exp_f32 v177, v161\n"
    "v_exp_f32 v178, v162\n"
    "v_exp_f32 v179, v163\n"
    "v_exp_f32 v180, v164\n"
    "v_exp_f32 v181, v165\n"
    "v_exp_f32 v182, v166\n"
    "v_exp_f32 v183, v167\n"
    "v_sub_f32 v192, v192, v237\n"
    "v_sub_f32 v193, v193, v237\n"
    "v_sub_f32 v194, v194, v237\n"
    "v_sub_f32 v195, v195, v237\n"
    "v_exp_f32 v238, -v237\n"
    "v_add_f32 v184, v176, v177\n"
    "v_add_f32 v185, v178, v179\n"
    "v_add_f32 v186, v180, v181\n"
    "v_add_f32 v187, v182, v183\n"
    "v_add_f32 v184, v184, v185\n"
    "v_add_f32 v186, v186, v187\n"
    "v_add_f32 v184, v184, v186\n"
    "v_cvt_pk_bf16_f32 v224, v176, v177\n"
    "v_cvt_pk_bf16_f32 v225, v178, v179\n"
    "v_cvt_pk_bf16_f32 v226, v180, v181\n"
    "v_cvt_pk_bf16_f32 v227, v182, v183\n"
    "v_mul_f32 v232, v232, v238\n"
    "v_mul_f32 v0, v0, v238\n"
    "v_mul_f32 v1, v1, v238\n"
    "v_mul_f32 v2, v2, v238\n"
    "v_mul_f32 v3, v3, v238\n"
    "v_mul_f32 v4, v4, v238\n"
    "v_mul_f32 v5, v5, v238\n"
    "v_mul_f32 v6, v6, v238\n"
    "v_mul_f32 v7, v7, v238\n"
    "v_mul_f32 v8, v8, v238\n"
    "v_mul_f32 v9, v9, v238\n"
    "v_mul_f32 v10, v10, v238\n"
    "v_mul_f32 v11, v11, v238\n"
    "v_mul_f32 v12, v12, v238\n"
    "v_mul_f32 v13, v13, v238\n"
    "v_mul_f32 v14, v14, v238\n"
    "v_mul_f32 v15, v15, v238\n"
    "v_mul_f32 v16, v16, v238\n"
    "v_mul_f32 v17, v17, v238\n"
    "v_mul_f32 v18, v18, v238\n"
    "v_mul_f32 v19, v19, v238\n"
    "v_mul_f32 v20, v20, v238\n"
    "v_mul_f32 v21, v21, v238\n"
    "v_mul_f32 v22, v22, v238\n"
    "v_mul_f32 v23, v23, v238\n"
    "v_mul_f32 v24, v24, v238\n"
    "v_mul_f32 v25, v25, v238\n"
    "v_mul_f32 v26, v26, v238\n"
    "v_mul_f32 v27, v27, v238\n"
    "v_mul_f32 v28, v28, v238\n"
    "v_mul_f32 v29, v29, v238\n"
    "v_mul_f32 v30, v30, v238\n"
    "v_mul_f32 v31, v31, v238\n"
    "s_nop 1\n"
    "s_setpc_b64 s[96:97]\n"
    "L_rareA_l5%=:\n"
    "s_call_b64 s[96:97], L_rb1_1_0%=\n"
    "s_branch L_rareA_l5_r%=\n"
    "L_rb1_1_0%=:\n"
    "s_nop 7\n"
    "v_max3_f32 v237, v168, v169, v170\n"
    "v_max3_f32 v238, v171, v172, v173\n"
    "v_max3_f32 v237, v237, v174, v175\n"
    "v_max_f32 v237, v237, v238\n"
    "v_mov_b32 v238, v237\n"
    "s_nop 1\n"
    "v_permlane16_swap_b32 v237, v238\n"
    "v_max_f32 v237, v237, v238\n"
    "v_mov_b32 v238, v237\n"
    "s_nop 1\n"
    "v_permlane32_swap_b32 v237, v238\n"
    "v_max_f32 v237, v237, v238\n"
    "v_max_f32 v237, 0, v237\n"
    "v_sub_f32 v168, v168, v237\n"
    "v_sub_f32 v169, v169, v237\n"
    "v_sub_f32 v170, v170, v237\n"
    "v_sub_f32 v171, v171, v237\n"
    "v_sub_f32 v172, v172, v237\n"
    "v_sub_f32 v173, v173, v237\n"
    "v_sub_f32 v174, v174, v237\n"
    "v_sub_f32 v175, v175, v237\n"
    "v_exp_f32 v176, v168\n"
    "v_exp_f32 v177, v169\n"
    "v_exp_f32 v178, v170\n"
    "v_exp_f32 v179, v171\n"
    "v_exp_f32 v180, v172\n"
    "v_exp_f32 v181, v173\n"
    "v_exp_f32 v182, v174\n"
    "v_exp_f32 v183, v175\n"
    "v_sub_f32 v196, v196, v237\n"
    "v_sub_f32 v197, v197, v237\n"
    "v_sub_f32 v198, v198, v237\n"
    "v_sub_f32 v199, v199, v237\n"
    "v_exp_f32 v238, -v237\n"
    "v_add_f32 v184, v176, v177\n"
    "v_add_f32 v185, v178, v179\n"
    "v_add_f32 v186, v180, v181\n"
    "v_add_f32 v187, v182, v183\n"
    "v_add_f32 v184, v184, v185\n"
    "v_add_f32 v186, v186, v187\n"
    "v_add_f32 v184, v184, v186\n"
    "v_cvt_pk_bf16_f32 v228, v176, v177\n"
    "v_cvt_pk_bf16_f32 v229, v178, v179\n"
    "v_cvt_pk_bf16_f32 v230, v180, v181\n"
    "v_cvt_pk_bf16_f32 v231, v182, v183\n"
    "v_mul_f32 v233, v233, v238\n"
    "v_mul_f32 v32, v32, v238\n"
    "v_mul_f32 v33, v33, v238\n"
    "v_mul_f32 v34, v34, v238\n"
    "v_mul_f32 v35, v35, v238\n"
    "v_mul_f32 v36, v36, v238\n"
    "v_mul_f32 v37, v37, v238\n"
    "v_mul_f32 v38, v38, v238\n"
    "v_mul_f32 v39, v39, v238\n"
    "v_mul_f32 v40, v40, v238\n"
    "v_mul_f32 v41, v41, v238\n"
    "v_mul_f32 v42, v42, v238\n"
    "v_mul_f32 v43, v43, v238\n"
    "v_mul_f32 v44, v44, v238\n"
    "v_mul_f32 v45, v45, v238\n"
    "v_mul_f32 v46, v46, v238\n"
    "v_mul_f32 v47, v47, v238\n"
    "v_mul_f32 v48, v48, v238\n"
    "v_mul_f32 v49, v49, v238\n"
    "v_mul_f32 v50, v50, v238\n"
    "v_mul_f32 v51, v51, v238\n"
    "v_mul_f32 v52, v52, v238\n"
    "v_mul_f32 v53, v53, v238\n"
    "v_mul_f32 v54, v54, v238\n"
    "v_mul_f32 v55, v55, v238\n"
    "v_mul_f32 v56, v56, v238\n"
    "v_mul_f32 v57, v57, v238\n"
    "v_mul_f32 v58, v58, v238\n"
    "v_mul_f32 v59, v59, v238\n"
    "v_mul_f32 v60, v60, v238\n"
    "v_mul_f32 v61, v61, v238\n"
    "v_mul_f32 v62, v62, v238\n"
    "v_mul_f32 v63, v63, v238\n"
    "s_nop 1\n"
    "s_setpc_b64 s[96:97]\n"
    "L_rareA_l6%=:\n"
    "s_call_b64 s[96:97], L_rb2_0_0%=\n"
    "s_branch L_rareA_l6_r%=\n"
    "L_rb2_0_0%=:\n"
    "s_nop 7\n"
    "v_max3_f32 v237, v160, v161, v162\n"
    "v_max3_f32 v238, v163, v164, v165\n"
    "v_max3_f32 v237, v237, v166, v167\n"
    "v_max_f32 v237, v237, v238\n"
    "v_mov_b32 v238, v237\n"
    "s_nop 1\n"
    "v_permlane16_swap_b32 v237, v238\n"
    "v_max_f32 v237, v237, v238\n"
    "v_mov_b32 v238, v237\n"
    "s_nop 1\n"
    "v_permlane32_swap_b32 v237, v238\n"
    "v_max_f32 v237, v237, v238\n"
    "v_max_f32 v237, 0, v237\n"
    "v_sub_f32 v160, v160, v237\n"
    "v_sub_f32 v161, v161, v237\n"
    "v_sub_f32 v162, v162, v237\n"
    "v_sub_f32 v163, v163, v237\n"
    "v_sub_f32 v164, v164, v237\n"
    "v_sub_f32 v165, v165, v237\n"
    "v_sub_f32 v166, v166, v237\n"
    "v_sub_f32 v167, v167, v237\n"
    "v_exp_f32 v176, v160\n"
    "v_exp_f32 v177, v161\n"
    "v_exp_f32 v178, v162\n"
    "v_exp_f32 v179, v163\n"
    "v_exp_f32 v180, v164\n"
    "v_exp_f32 v181, v165\n"
    "v_exp_f32 v182, v166\n"
    "v_exp_f32 v183, v167\n"
    "v_sub_f32 v200, v200, v237\n"
    "v_sub_f32 v201, v201, v237\n"
    "v_sub_f32 v202, v202, v237\n"
    "v_sub_f32 v203, v203, v237\n"
    "v_exp_f32 v238, -v237\n"
    "v_add_f32 v184, v176, v177\n"
    "v_add_f32 v185, v178, v179\n"
    "v_add_f32 v186, v180, v181\n"
    "v_add_f32 v187, v182, v183\n"
    "v_add_f32 v184, v184, v185\n"
    "v_add_f32 v186, v186, v187\n"
    "v_add_f32 v184, v184, v186\n"
    "v_cvt_pk_bf16_f32 v224, v176, v177\n"
    "v_cvt_pk_bf16_f32 v225, v178, v179\n"
    "v_cvt_pk_bf16_f32 v226, v180, v181\n"
    "v_cvt_pk_bf16_f32 v227, v182, v183\n"
    "v_mul_f32 v234, v234, v238\n"
    "v_mul_f32 v64, v64, v238\n"
    "v_mul_f32 v65, v65, v238\n"
    "v_mul_f32 v66, v66, v238\n"
    "v_mul_f32 v67, v67, v238\n"
    "v_mul_f32 v68, v68, v238\n"
    "v_mul_f32 v69, v69, v238\n"
    "v_mul_f32 v70, v70, v238\n"
    "v_mul_f32 v71, v71, v238\n"
    "v_mul_f32 v72, v72, v238\n"
    "v_mul_f32 v73, v73, v238\n"
    "v_mul_f32 v74, v74, v238\n"
    "v_mul_f32 v75, v75, v238\n"
    "v_mul_f32 v76, v76, v238\n"
    "v_mul_f32 v77, v77, v238\n"
    "v_mul_f32 v78, v78, v238\n"
    "v_mul_f32 v79, v79, v238\n"
    "v_mul_f32 v80, v80, v238\n"
    "v_mul_f32 v81, v81, v238\n"
    "v_mul_f32 v82, v82, v238\n"
    "v_mul_f32 v83, v83, v238\n"
    "v_mul_f32 v84, v84, v238\n"
    "v_mul_f32 v85, v85, v238\n"
    "v_mul_f32 v86, v86, v238\n"
    "v_mul_f32 v87, v87, v238\n"
    "v_mul_f32 v88, v88, v238\n"
    "v_mul_f32 v89, v89, v238\n"
    "v_mul_f32 v90, v90, v238\n"
    "v_mul_f32 v91, v91, v238\n"
    "v_mul_f32 v92, v92, v238\n"
    "v_mul_f32 v93, v93, v238\n"
    "v_mul_f32 v94, v94, v238\n"
    "v_mul_f32 v95, v95, v238\n"
    "s_nop 1\n"
    "s_setpc_b64 s[96:97]\n"
    "L_rareA_l7%=:\n"
    "s_call_b64 s[96:97], L_rb3_1_0%=\n"
    "s_branch L_rareA_l7_r%=\n"
    "L_rb3_1_0%=:\n"
    "s_nop 7\n"
    "v_max3_f32 v237, v168, v169, v170\n"
    "v_max3_f32 v238, v171, v172, v173\n"
    "v_max3_f32 v237, v237, v174, v175\n"
    "v_max_f32 v237, v237, v238\n"
    "v_mov_b32 v238, v237\n"
    "s_nop 1\n"
    "v_permlane16_swap_b32 v237, v238\n"
    "v_max_f32 v237, v237, v238\n"
    "v_mov_b32 v238, v237\n"
    "s_nop 1\n"
    "v_permlane32_swap_b32 v237, v238\n"
    "v_max_f32 v237, v237, v238\n"
    "v_max_f32 v237, 0, v237\n"
    "v_sub_f32 v168, v168, v237\n"
    "v_sub_f32 v169, v169, v237\n"
    "v_sub_f32 v170, v170, v237\n"
    "v_sub_f32 v171, v171, v237\n"
    "v_sub_f32 v172, v172, v237\n"
    "v_sub_f32 v173, v173, v237\n"
    "v_sub_f32 v174, v174, v237\n"
    "v_sub_f32 v175, v175, v237\n"
    "v_exp_f32 v176, v168\n"
    "v_exp_f32 v177, v169\n"
    "v_exp_f32 v178, v170\n"
    "v_exp_f32 v179, v171\n"
    "v_exp_f32 v180, v172\n"
    "v_exp_f32 v181, v173\n"
    "v_exp_f32 v182, v174\n"
    "v_exp_f32 v183, v175\n"
    "v_sub_f32 v204, v204, v237\n"
    "v_sub_f32 v205, v205, v237\n"
    "v_sub_f32 v206, v206, v237\n"
    "v_sub_f32 v207, v207, v237\n"
    "v_exp_f32 v238, -v237\n"
    "v_add_f32 v184, v176, v177\n"
    "v_add_f32 v185, v178, v179\n"
    "v_add_f32 v186, v180, v181\n"
    "v_add_f32 v187, v182, v183\n"
    "v_add_f32 v184, v184, v185\n"
    "v_add_f32 v186, v186, v187\n"
    "v_add_f32 v184, v184, v186\n"
    "v_cvt_pk_bf16_f32 v228, v176, v177\n"
    "v_cvt_pk_bf16_f32 v229, v178, v179\n"
    "v_cvt_pk_bf16_f32 v230, v180, v181\n"
    "v_cvt_pk_bf16_f32 v231, v182, v183\n"
    "v_mul_f32 v235, v235, v238\n"
    "v_mul_f32 v96, v96, v238\n"
    "v_mul_f32 v97, v97, v238\n"
    "v_mul_f32 v98, v98, v238\n"
    "v_mul_f32 v99, v99, v238\n"
    "v_mul_f32 v100, v100, v238\n"
    "v_mul_f32 v101, v101, v238\n"
    "v_mul_f32 v102, v102, v238\n"
    "v_mul_f32 v103, v103, v238\n"
    "v_mul_f32 v104, v104, v238\n"
    "v_mul_f32 v105, v105, v238\n"
    "v_mul_f32 v106, v106, v238\n"
    "v_mul_f32 v107, v107, v238\n"
    "v_mul_f32 v108, v108, v238\n"
    "v_mul_f32 v109, v109, v238\n"
    "v_mul_f32 v110, v110, v238\n"
    "v_mul_f32 v111, v111, v238\n"
    "v_mul_f32 v112, v112, v238\n"
    "v_mul_f32 v113, v113, v238\n"
    "v_mul_f32 v114, v114, v238\n"
    "v_mul_f32 v115, v115, v238\n"
    "v_mul_f32 v116, v116, v238\n"
    "v_mul_f32 v117, v117, v238\n"
    "v_mul_f32 v118, v118, v238\n"
    "v_mul_f32 v119, v119, v238\n"
    "v_mul_f32 v120, v120, v238\n"
    "v_mul_f32 v121, v121, v238\n"
    "v_mul_f32 v122, v122, v238\n"
    "v_mul_f32 v123, v123, v238\n"
    "v_mul_f32 v124, v124, v238\n"
    "v_mul_f32 v125, v125, v238\n"
    "v_mul_f32 v126, v126, v238\n"
    "v_mul_f32 v127, v127, v238\n"
    "s_nop 1\n"
    "s_setpc_b64 s[96:97]\n"
    "L_rareA_l8%=:\n"
    "s_call_b64 s[96:97], L_rb4_0_0%=\n"
    "s_branch L_rareA_l8_r%=\n"
    "L_rb4_0_0%=:\n"
    "s_nop 7\n"
    "v_max3_f32 v237, v160, v161, v162\n"
    "v_max3_f32 v238, v163, v164, v165\n"
    "v_max3_f32 v237, v237, v166, v167\n"
    "v_max_f32 v237, v237, v238\n"
    "v_mov_b32 v238, v237\n"
    "s_nop 1\n"
    "v_permlane16_swap_b32 v237, v238\n"
    "v_max_f32 v237, v237, v238\n"
    "v_mov_b32 v238, v237\n"
    "s_nop 1\n"
    "v_permlane32_swap_b32 v237, v238\n"
    "v_max_f32 v237, v237, v238\n"
    "v_max_f32 v237, 0, v237\n"
    "v_sub_f32 v160, v160, v237\n"
    "v_sub_f32 v161, v161, v237\n"
    "v_sub_f32 v162, v162, v237\n"
    "v_sub_f32 v163, v163, v237\n"
    "v_sub_f32 v164, v164, v237\n"
    "v_sub_f32 v165, v165, v237\n"
    "v_sub_f32 v166, v166, v237\n"
    "v_sub_f32 v167, v167, v237\n"
    "v_exp_f32 v176, v160\n"
    "v_exp_f32 v177, v161\n"
    "v_exp_f32 v178, v162\n"
    "v_exp_f32 v179, v163\n"
    "v_exp_f32 v180, v164\n"
    "v_exp_f32 v181, v165\n"
    "v_exp_f32 v182, v166\n"
    "v_exp_f32 v183, v167\n"
    "v_sub_f32 v208, v208, v237\n"
    "v_sub_f32 v209, v209, v237\n"
    "v_sub_f32 v210, v210, v237\n"
    "v_sub_f32 v211, v211, v237\n"
    "v_exp_f32 v238, -v237\n"
    "v_add_f32 v184, v176, v177\n"
    "v_add_f32 v185, v178, v179\n"
    "v_add_f32 v186, v180, v181\n"
    "v_add_f32 v187, v182, v183\n"
    "v_add_f32 v184, v184, v185\n"
    "v_add_f32 v186, v186, v187\n"
    "v_add_f32 v184, v184, v186\n"
    "v_cvt_pk_bf16_f32 v224, v176, v177\n"
    "v_cvt_pk_bf16_f32 v225, v178, v179\n"
    "v_cvt_pk_bf16_f32 v226, v180, v181\n"
    "v_cvt_pk_bf16_f32 v227, v182, v183\n"
    "v_mul_f32 v236, v236, v238\n"
    "v_mul_f32 v128, v128, v238\n"
    "v_mul_f32 v129, v129, v238\n"
    "v_mul_f32 v130, v130, v238\n"
    "v_mul_f32 v131, v131, v238\n"
    "v_mul_f32 v132, v132, v238\n"
    "v_mul_f32 v133, v133, v238\n"
    "v_mul_f32 v134, v134, v238\n"
    "v_mul_f32 v135, v135, v238\n"
    "v_mul_f32 v136, v136, v238\n"
    "v_mul_f32 v137, v137, v238\n"
    "v_mul_f32 v138, v138, v238\n"
    "v_mul_f32 v139, v139, v238\n"
    "v_mul_f32 v140, v140, v238\n"
    "v_mul_f32 v141, v141, v238\n"
    "v_mul_f32 v142, v142, v238\n"
    "v_mul_f32 v143, v143, v238\n"
    "v_mul_f32 v144, v144, v238\n"
    "v_mul_f32 v145, v145, v238\n"
    "v_mul_f32 v146, v146, v238\n"
    "v_mul_f32 v147, v147, v238\n"
    "v_mul_f32 v148, v148, v238\n"
    "v_mul_f32 v149, v149, v238\n"
    "v_mul_f32 v150, v150, v238\n"
    "v_mul_f32 v151, v151, v238\n"
    "v_mul_f32 v152, v152, v238\n"
    "v_mul_f32 v153, v153, v238\n"
    "v_mul_f32 v154, v154, v238\n"
    "v_mul_f32 v155, v155, v238\n"
    "v_mul_f32 v156, v156, v238\n"
    "v_mul_f32 v157, v157, v238\n"
    "v_mul_f32 v158, v158, v238\n"
    "v_mul_f32 v159, v159, v238\n"
    "s_nop 1\n"
    "s_setpc_b64 s[96:97]\n"
    "L_rareA_l9%=:\n"
    "s_call_b64 s[96:97], L_rb0_1_0%=\n"
    "s_branch L_rareA_l9_r%=\n"
    "L_rareA_e66%=:\n"
    "s_call_b64 s[96:97], L_rb1_0_0%=\n"
    "s_branch L_rareA_e66_r%=\n"
    "L_rareA_e67%=:\n"
    "s_call_b64 s[96:97], L_rb2_1_0%=\n"
    "s_branch L_rareA_e67_r%=\n"
    "L_rareA_e68%=:\n"
    "s_call_b64 s[96:97], L_rb3_0_0%=\n"
    "s_branch L_rareA_e68_r%=\n"
    "L_rareA_e69%=:\n"
    "s_call_b64 s[96:97], L_rb4_1_0%=\n"
    "s_branch L_rareA_e69_r%=\n"
    "L_rareA_e70%=:\n"
    "s_call_b64 s[96:97], L_rb0_0_0%=\n"
    "s_branch L_rareA_e70_r%=\n"
    "L_rareA_e71%=:\n"
    "s_call_b64 s[96:97], L_rb1_1_0%=\n"
    "s_branch L_rareA_e71_r%=\n"
    "L_rareA_e72%=:\n"
    "s_call_b64 s[96:97], L_rb2_0_0%=\n"
    "s_branch L_rareA_e72_r%=\n"
    "L_rareA_e73%=:\n"
    "s_call_b64 s[96:97], L_rb3_1_0%=\n"
    "s_branch L_rareA_e73_r%=\n"
    "L_rareA_e74%=:\n"
    "s_call_b64 s[96:97], L_rb4_0_0%=\n"
    "s_branch L_rareA_e74_r%=\n"
    "L_rareA_e75%=:\n"
    "s_call_b64 s[96:97], L_rb0_1_0%=\n"
    "s_branch L_rareA_e75_r%=\n"
    "L_rareT_76%=:\n"
    "s_call_b64 s[96:97], L_rb1_0_1%=\n"
    "s_branch L_rareT_76_r%=\n"
    "L_rb1_0_1%=:\n"
    "s_nop 7\n"
    "v_max3_f32 v237, v160, v161, v162\n"
    "v_max3_f32 v238, v163, v164, v165\n"
    "v_max3_f32 v237, v237, v166, v167\n"
    "v_max_f32 v237, v237, v238\n"
    "v_mov_b32 v238, v237\n"
    "s_nop 1\n"
    "v_permlane16_swap_b32 v237, v238\n"
    "v_max_f32 v237, v237, v238\n"
    "v_mov_b32 v238, v237\n"
    "s_nop 1\n"
    "v_permlane32_swap_b32 v237, v238\n"
    "v_max_f32 v237, v237, v238\n"
    "v_max_f32 v237, 0, v237\n"
    "v_sub_f32 v160, v160, v237\n"
    "v_sub_f32 v161, v161, v237\n"
    "v_sub_f32 v162, v162, v237\n"
    "v_sub_f32 v163, v163, v237\n"
    "v_sub_f32 v164, v164, v237\n"
    "v_sub_f32 v165, v165, v237\n"
    "v_sub_f32 v166, v166, v237\n"
    "v_sub_f32 v167, v167, v237\n"
    "v_exp_f32 v176, v160\n"
    "v_exp_f32 v177, v161\n"
    "v_exp_f32 v178, v162\n"
    "v_exp_f32 v179, v163\n"
    "v_exp_f32 v180, v164\n"
    "v_exp_f32 v181, v165\n"
    "v_exp_f32 v182, v166\n"
    "v_exp_f32 v183, v167\n"
    "v_exp_f32 v238, -v237\n"
    "v_add_f32 v184, v176, v177\n"
    "v_add_f32 v185, v178, v179\n"
    "v_add_f32 v186, v180, v181\n"
    "v_add_f32 v187, v182, v183\n"
    "v_add_f32 v184, v184, v185\n"
    "v_add_f32 v186, v186, v187\n"
    "v_add_f32 v184, v184, v186\n"
    "v_cvt_pk_bf16_f32 v224, v176, v177\n"
    "v_cvt_pk_bf16_f32 v225, v178, v179\n"
    "v_cvt_pk_bf16_f32 v226, v180, v181\n"
    "v_cvt_pk_bf16_f32 v227, v182, v183\n"
    "v_mul_f32 v233, v233, v238\n"
    "v_mul_f32 v32, v32, v238\n"
    "v_mul_f32 v33, v33, v238\n"
    "v_mul_f32 v34, v34, v238\n"
    "v_mul_f32 v35, v35, v238\n"
    "v_mul_f32 v36, v36, v238\n"
    "v_mul_f32 v37, v37, v238\n"
    "v_mul_f32 v38, v38, v238\n"
    "v_mul_f32 v39, v39, v238\n"
    "v_mul_f32 v40, v40, v238\n"
    "v_mul_f32 v41, v41, v238\n"
    "v_mul_f32 v42, v42, v238\n"
    "v_mul_f32 v43, v43, v238\n"
    "v_mul_f32 v44, v44, v238\n"
    "v_mul_f32 v45, v45, v238\n"
    "v_mul_f32 v46, v46, v238\n"
    "v_mul_f32 v47, v47, v238\n"
    "v_mul_f32 v48, v48, v238\n"
    "v_mul_f32 v49, v49, v238\n"
    "v_mul_f32 v50, v50, v238\n"
    "v_mul_f32 v51, v51, v238\n"
    "v_mul_f32 v52, v52, v238\n"
    "v_mul_f32 v53, v53, v238\n"
    "v_mul_f32 v54, v54, v238\n"
    "v_mul_f32 v55, v55, v238\n"
    "v_mul_f32 v56, v56, v238\n"
    "v_mul_f32 v57, v57, v238\n"
    "v_mul_f32 v58, v58, v238\n"
    "v_mul_f32 v59, v59, v238\n"
    "v_mul_f32 v60, v60, v238\n"
    "v_mul_f32 v61, v61, v238\n"
    "v_mul_f32 v62, v62, v238\n"
    "v_mul_f32 v63, v63, v238\n"
    "s_nop 1\n"
    "s_setpc_b64 s[96:97]\n"
    "L_rareT_77%=:\n"
    "s_call_b64 s[96:97], L_rb2_1_1%=\n"
    "s_branch L_rareT_77_r%=\n"
    "L_rb2_1_1%=:\n"
    "s_nop 7\n"
    "v_max3_f32 v237, v168, v169, v170\n"
    "v_max3_f32 v238, v171, v172, v173\n"
    "v_max3_f32 v237, v237, v174, v175\n"
    "v_max_f32 v237, v237, v238\n"
    "v_mov_b32 v238, v237\n"
    "s_nop 1\n"
    "v_permlane16_swap_b32 v237, v238\n"
    "v_max_f32 v237, v237, v238\n"
    "v_mov_b32 v238, v237\n"
    "s_nop 1\n"
    "v_permlane32_swap_b32 v237, v238\n"
    "v_max_f32 v237, v237, v238\n"
    "v_max_f32 v237, 0, v237\n"
    "v_sub_f32 v168, v168, v237\n"
    "v_sub_f32 v169, v169, v237\n"
    "v_sub_f32 v170, v170, v237\n"
    "v_sub_f32 v171, v171, v237\n"
    "v_sub_f32 v172, v172, v237\n"
    "v_sub_f32 v173, v173, v237\n"
    "v_sub_f32 v174, v174, v237\n"
    "v_sub_f32 v175, v175, v237\n"
    "v_exp_f32 v176, v168\n"
    "v_exp_f32 v177, v169\n"
    "v_exp_f32 v178, v170\n"
    "v_exp_f32 v179, v171\n"
    "v_exp_f32 v180, v172\n"
    "v_exp_f32 v181, v173\n"
    "v_exp_f32 v182, v174\n"
    "v_exp_f32 v183, v175\n"
    "v_exp_f32 v238, -v237\n"
    "v_add_f32 v184, v176, v177\n"
    "v_add_f32 v185, v178, v179\n"
    "v_add_f32 v186, v180, v181\n"
    "v_add_f32 v187, v182, v183\n"
    "v_add_f32 v184, v184, v185\n"
    "v_add_f32 v186, v186, v187\n"
    "v_add_f32 v184, v184, v186\n"
    "v_cvt_pk_bf16_f32 v188, v176, v177\n"
    "v_cvt_pk_bf16_f32 v189, v178, v179\n"
    "v_cvt_pk_bf16_f32 v190, v180, v181\n"
    "v_cvt_pk_bf16_f32 v191, v182, v183\n"
    "v_mul_f32 v234, v234, v238\n"
    "v_mul_f32 v64, v64, v238\n"
    "v_mul_f32 v65, v65, v238\n"
    "v_mul_f32 v66, v66, v238\n"
    "v_mul_f32 v67, v67, v238\n"
    "v_mul_f32 v68, v68, v238\n"
    "v_mul_f32 v69, v69, v238\n"
    "v_mul_f32 v70, v70, v238\n"
    "v_mul_f32 v71, v71, v238\n"
    "v_mul_f32 v72, v72, v238\n"
    "v_mul_f32 v73, v73, v238\n"
    "v_mul_f32 v74, v74, v238\n"
    "v_mul_f32 v75, v75, v238\n"
    "v_mul_f32 v76, v76, v238\n"
    "v_mul_f32 v77, v77, v238\n"
    "v_mul_f32 v78, v78, v238\n"
    "v_mul_f32 v79, v79, v238\n"
    "v_mul_f32 v80, v80, v238\n"
    "v_mul_f32 v81, v81, v238\n"
    "v_mul_f32 v82, v82, v238\n"
    "v_mul_f32 v83, v83, v238\n"
    "v_mul_f32 v84, v84, v238\n"
    "v_mul_f32 v85, v85, v238\n"
    "v_mul_f32 v86, v86, v238\n"
    "v_mul_f32 v87, v87, v238\n"
    "v_mul_f32 v88, v88, v238\n"
    "v_mul_f32 v89, v89, v238\n"
    "v_mul_f32 v90, v90, v238\n"
    "v_mul_f32 v91, v91, v238\n"
    "v_mul_f32 v92, v92, v238\n"
    "v_mul_f32 v93, v93, v238\n"
    "v_mul_f32 v94, v94, v238\n"
    "v_mul_f32 v95, v95, v238\n"
    "s_nop 1\n"
    "s_setpc_b64 s[96:97]\n"
    "L_rareT_78%=:\n"
    "s_call_b64 s[96:97], L_rb3_0_1%=\n"
    "s_branch L_rareT_78_r%=\n"
    "L_rb3_0_1%=:\n"
    "s_nop 7\n"
    "v_max3_f32 v237, v160, v161, v162\n"
    "v_max3_f32 v238, v163, v164, v165\n"
    "v_max3_f32 v237, v237, v166, v167\n"
    "v_max_f32 v237, v237, v238\n"
    "v_mov_b32 v238, v237\n"
    "s_nop 1\n"
    "v_permlane16_swap_b32 v237, v238\n"
    "v_max_f32 v237, v237, v238\n"
    "v_mov_b32 v238, v237\n"
    "s_nop 1\n"
    "v_permlane32_swap_b32 v237, v238\n"
    "v_max_f32 v237, v237, v238\n"
    "v_max_f32 v237, 0, v237\n"
    "v_sub_f32 v160, v160, v237\n"
    "v_sub_f32 v161, v161, v237\n"
    "v_sub_f32 v162, v162, v237\n"
    "v_sub_f32 v163, v163, v237\n"
    "v_sub_f32 v164, v164, v237\n"
    "v_sub_f32 v165, v165, v237\n"
    "v_sub_f32 v166, v166, v237\n"
    "v_sub_f32 v167, v167, v237\n"
    "v_exp_f32 v176, v160\n"
    "v_exp_f32 v177, v161\n"
    "v_exp_f32 v178, v162\n"
    "v_exp_f32 v179, v163\n"
    "v_exp_f32 v180, v164\n"
    "v_exp_f32 v181, v165\n"
    "v_exp_f32 v182, v166\n"
    "v_exp_f32 v183, v167\n"
    "v_exp_f32 v238, -v237\n"
    "v_add_f32 v184, v176, v177\n"
    "v_add_f32 v185, v178, v179\n"
    "v_add_f32 v186, v180, v181\n"
    "v_add_f32 v187, v182, v183\n"
    "v_add_f32 v184, v184, v185\n"
    "v_add_f32 v186, v186, v187\n"
    "v_add_f32 v184, v184, v186\n"
    "v_cvt_pk_bf16_f32 v242, v176, v177\n"
    "v_cvt_pk_bf16_f32 v243, v178, v179\n"
    "v_cvt_pk_bf16_f32 v244, v180, v181\n"
    "v_cvt_pk_bf16_f32 v245, v182, v183\n"
    "v_mul_f32 v235, v235, v238\n"
    "v_mul_f32 v96, v96, v238\n"
    "v_mul_f32 v97, v97, v238\n"
    "v_mul_f32 v98, v98, v238\n"
    "v_mul_f32 v99, v99, v238\n"
    "v_mul_f32 v100, v100, v238\n"
    "v_mul_f32 v101, v101, v238\n"
    "v_mul_f32 v102, v102, v238\n"
    "v_mul_f32 v103, v103, v238\n"
    "v_mul_f32 v104, v104, v238\n"
    "v_mul_f32 v105, v105, v238\n"
    "v_mul_f32 v106, v106, v238\n"
    "v_mul_f32 v107, v107, v238\n"
    "v_mul_f32 v108, v108, v238\n"
    "v_mul_f32 v109, v109, v238\n"
    "v_mul_f32 v110, v110, v238\n"
    "v_mul_f32 v111, v111, v238\n"
    "v_mul_f32 v112, v112, v238\n"
    "v_mul_f32 v113, v113, v238\n"
    "v_mul_f32 v114, v114, v238\n"
    "v_mul_f32 v115, v115, v238\n"
    "v_mul_f32 v116, v116, v238\n"
    "v_mul_f32 v117, v117, v238\n"
    "v_mul_f32 v118, v118, v238\n"
    "v_mul_f32 v119, v119, v238\n"
    "v_mul_f32 v120, v120, v238\n"
    "v_mul_f32 v121, v121, v238\n"
    "v_mul_f32 v122, v122, v238\n"
    "v_mul_f32 v123, v123, v238\n"
    "v_mul_f32 v124, v124, v238\n"
    "v_mul_f32 v125, v125, v238\n"
    "v_mul_f32 v126, v126, v238\n"
    "v_mul_f32 v127, v127, v238\n"
    "s_nop 1\n"
    "s_setpc_b64 s[96:97]\n"
    "L_rareT_79%=:\n"
    "s_call_b64 s[96:97], L_rb4_1_1%=\n"
    "s_branch L_rareT_79_r%=\n"
    "L_rb4_1_1%=:\n"
    "s_nop 7\n"
    "v_max3_f32 v237, v168, v169, v170\n"
    "v_max3_f32 v238, v171, v172, v173\n"
    "v_max3_f32 v237, v237, v174, v175\n"
    "v_max_f32 v237, v237, v238\n"
    "v_mov_b32 v238, v237\n"
    "s_nop 1\n"
    "v_permlane16_swap_b32 v237, v238\n"
    "v_max_f32 v237, v237, v238\n"
    "v_mov_b32 v238, v237\n"
    "s_nop 1\n"
    "v_permlane32_swap_b32 v237, v238\n"
    "v_max_f32 v237, v237, v238\n"
    "v_max_f32 v237, 0, v237\n"
    "v_sub_f32 v168, v168, v237\n"
    "v_sub_f32 v169, v169, v237\n"
    "v_sub_f32 v170, v170, v237\n"
    "v_sub_f32 v171, v171, v237\n"
    "v_sub_f32 v172, v172, v237\n"
    "v_sub_f32 v173, v173, v237\n"
    "v_sub_f32 v174, v174, v237\n"
    "v_sub_f32 v175, v175, v237\n"
    "v_exp_f32 v176, v168\n"
    "v_exp_f32 v177, v169\n"
    "v_exp_f32 v178, v170\n"
    "v_exp_f32 v179, v171\n"
    "v_exp_f32 v180, v172\n"
    "v_exp_f32 v181, v173\n"
    "v_exp_f32 v182, v174\n"
    "v_exp_f32 v183, v175\n"
    "v_exp_f32 v238, -v237\n"
    "v_add_f32 v184, v176, v177\n"
    "v_add_f32 v185, v178, v179\n"
    "v_add_f32 v186, v180, v181\n"
    "v_add_f32 v187, v182, v183\n"
    "v_add_f32 v184, v184, v185\n"
    "v_add_f32 v186, v186, v187\n"
    "v_add_f32 v184, v184, v186\n"
    "v_cvt_pk_bf16_f32 v164, v176, v177\n"
    "v_cvt_pk_bf16_f32 v165, v178, v179\n"
    "v_cvt_pk_bf16_f32 v166, v180, v181\n"
    "v_cvt_pk_bf16_f32 v167, v182, v183\n"
    "v_mul_f32 v236, v236, v238\n"
    "v_mul_f32 v128, v128, v238\n"
    "v_mul_f32 v129, v129, v238\n"
    "v_mul_f32 v130, v130, v238\n"
    "v_mul_f32 v131, v131, v238\n"
    "v_mul_f32 v132, v132, v238\n"
    "v_mul_f32 v133, v133, v238\n"
    "v_mul_f32 v134, v134, v238\n"
    "v_mul_f32 v135, v135, v238\n"
    "v_mul_f32 v136, v136, v238\n"
    "v_mul_f32 v137, v137, v238\n"
    "v_mul_f32 v138, v138, v238\n"
    "v_mul_f32 v139, v139, v238\n"
    "v_mul_f32 v140, v140, v238\n"
    "v_mul_f32 v141, v141, v238\n"
    "v_mul_f32 v142, v142, v238\n"
    "v_mul_f32 v143, v143, v238\n"
    "v_mul_f32 v144, v144, v238\n"
    "v_mul_f32 v145, v145, v238\n"
    "v_mul_f32 v146, v146, v238\n"
    "v_mul_f32 v147, v147, v238\n"
    "v_mul_f32 v148, v148, v238\n"
    "v_mul_f32 v149, v149, v238\n"
    "v_mul_f32 v150, v150, v238\n"
    "v_mul_f32 v151, v151, v238\n"
    "v_mul_f32 v152, v152, v238\n"
    "v_mul_f32 v153, v153, v238\n"
    "v_mul_f32 v154, v154, v238\n"
    "v_mul_f32 v155, v155, v238\n"
    "v_mul_f32 v156, v156, v238\n"
    "v_mul_f32 v157, v157, v238\n"
    "v_mul_f32 v158, v158, v238\n"
    "v_mul_f32 v159, v159, v238\n"
    "s_nop 1\n"
    "s_setpc_b64 s[96:97]\n"
    "L_no1_%=:\n"
    "s_memrealtime s[96:97]\n"
    "s_waitcnt lgkmcnt(0)\n"
    "s_sub_u32 s97, s35, s96\n"
    "s_sub_u32 s97, s97, 1\n"
    "s_cmp_lt_u32 s97, 58\n"
    "s_cbranch_scc1 L_no1_%=\n"
    "s_add_u32 s35, s96, 58\n"
    "s_branch L_no1r_%=\n"
    "L_no2_%=:\n"
    "s_memrealtime s[96:97]\n"
    "s_waitcnt lgkmcnt(0)\n"
    "s_sub_u32 s97, s35, s96\n"
    "s_sub_u32 s97, s97, 1\n"
    "s_cmp_lt_u32 s97, 58\n"
    "s_cbranch_scc1 L_no2_%=\n"
    "s_add_u32 s35, s96, 58\n"
    "s_branch L_no2r_%=\n"
    "L_p3b%=:\n"
    "s_barrier\n"
    "s_branch L_p3br%=\n"
    "L_no0_%=:\n"
    "s_memrealtime s[96:97]\n"
    "s_waitcnt lgkmcnt(0)\n"
    "s_sub_u32 s97, s35, s96\n"
    "s_sub_u32 s97, s97, 1\n"
    "s_cmp_lt_u32 s97, 58\n"
    "s_cbranch_scc1 L_no0_%=\n"
    "s_add_u32 s35, s96, 58\n"
    "s_branch L_no0r_%=\n"
    "L_rareB_h5%=:\n"
    "s_call_b64 s[96:97], L_rb0_1_0%=\n"
    "s_branch L_rareB_h5_r%=\n"
    "L_rareB_l0%=:\n"
    "s_call_b64 s[96:97], L_rb1_0_0%=\n"
    "s_branch L_rareB_l0_r%=\n"
    "L_rareB_l1%=:\n"
    "s_call_b64 s[96:97], L_rb2_1_0%=\n"
    "s_branch L_rareB_l1_r%=\n"
    "L_rareB_l2%=:\n"
    "s_call_b64 s[96:97], L_rb3_0_0%=\n"
    "s_branch L_rareB_l2_r%=\n"
    "L_rareB_l3%=:\n"
    "s_call_b64 s[96:97], L_rb4_1_0%=\n"
    "s_branch L_rareB_l3_r%=\n"
    "L_rareB_l4%=:\n"
    "s_call_b64 s[96:97], L_rb0_0_0%=\n"
    "s_branch L_rareB_l4_r%=\n"
    "L_rareB_l5%=:\n"
    "s_call_b64 s[96:97], L_rb1_1_0%=\n"
    "s_branch L_rareB_l5_r%=\n"
    "L_rareB_l6%=:\n"
    "s_call_b64 s[96:97], L_rb2_0_0%=\n"
    "s_branch L_rareB_l6_r%=\n"
    "L_rareB_l7%=:\n"
    "s_call_b64 s[96:97], L_rb3_1_0%=\n"
    "s_branch L_rareB_l7_r%=\n"
    "L_rareB_l8%=:\n"
    "s_call_b64 s[96:97], L_rb4_0_0%=\n"
    "s_branch L_rareB_l8_r%=\n"
    "L_rareB_l9%=:\n"
    "s_call_b64 s[96:97], L_rb0_1_0%=\n"
    "s_branch L_rareB_l9_r%=\n"
    "L_rareBu_l0%=:\n"
    "s_call_b64 s[96:97], L_rb1_0_0%=\n"
    "s_branch L_rareBu_l0_r%=\n"
    "L_rareBu_l1%=:\n"
    "s_call_b64 s[96:97], L_rb2_1_0%=\n"
    "s_branch L_rareBu_l1_r%=\n"
    "L_rareBu_l2%=:\n"
    "s_call_b64 s[96:97], L_rb3_0_0%=\n"
    "s_branch L_rareBu_l2_r%=\n"
    "L_rareBu_l3%=:\n"
    "s_call_b64 s[96:97], L_rb4_1_0%=\n"
    "s_branch L_rareBu_l3_r%=\n"
    "L_rareBu_l4%=:\n"
    "s_call_b64 s[96:97], L_rb0_0_0%=\n"
    "s_branch L_rareBu_l4_r%=\n"
    "L_rareBu_l5%=:\n"
    "s_call_b64 s[96:97], L_rb1_1_0%=\n"
    "s_branch L_rareBu_l5_r%=\n"
    "L_rareBu_l6%=:\n"
    "s_call_b64 s[96:97], L_rb2_0_0%=\n"
    "s_branch L_rareBu_l6_r%=\n"
    "L_rareBu_l7%=:\n"
    "s_call_b64 s[96:97], L_rb3_1_0%=\n"
    "s_branch L_rareBu_l7_r%=\n"
    "L_rareBu_l8%=:\n"
    "s_call_b64 s[96:97], L_rb4_0_0%=\n"
    "s_branch L_rareBu_l8_r%=\n"
    "L_rareBu_l9%=:\n"
    "s_call_b64 s[96:97], L_rb0_1_0%=\n"
    "s_branch L_rareBu_l9_r%=\n"
    "L_rareBt_l0%=:\n"
    "s_call_b64 s[96:97], L_rb1_0_0%=\n"
    "s_branch L_rareBt_l0_r%=\n"
    "L_rareBt_l1%=:\n"
    "s_call_b64 s[96:97], L_rb2_1_0%=\n"
    "s_branch L_rareBt_l1_r%=\n"
    "L_rareBt_l2%=:\n"
    "s_call_b64 s[96:97], L_rb3_0_0%=\n"
    "s_branch L_rareBt_l2_r%=\n"
    "L_rareBt_l3%=:\n"
    "s_call_b64 s[96:97], L_rb4_1_0%=\n"
    "s_branch L_rareBt_l3_r%=\n"
    "L_rareBt_l4%=:\n"
    "s_call_b64 s[96:97], L_rb0_0_0%=\n"
    "s_branch L_rareBt_l4_r%=\n"
    "L_rareBt_l5%=:\n"
    "s_call_b64 s[96:97], L_rb1_1_0%=\n"
    "s_branch L_rareBt_l5_r%=\n"
    "L_rareBt_l6%=:\n"
    "s_call_b64 s[96:97], L_rb2_0_0%=\n"
    "s_branch L_rareBt_l6_r%=\n"
    "L_rareBt_l7%=:\n"
    "s_call_b64 s[96:97], L_rb3_1_0%=\n"
    "s_branch L_rareBt_l7_r%=\n"
    "L_rareBt_l8%=:\n"
    "s_call_b64 s[96:97], L_rb4_0_0%=\n"
    "s_branch L_rareBt_l8_r%=\n"
    "L_rareBt_l9%=:\n"
    "s_call_b64 s[96:97], L_rb0_1_0%=\n"
    "s_branch L_rareBt_l9_r%=\n"
    "L_rareB_e66%=:\n"
    "s_call_b64 s[96:97], L_rb1_0_0%=\n"
    "s_branch L_rareB_e66_r%=\n"
    "L_rareB_e67%=:\n"
    "s_call_b64 s[96:97], L_rb2_1_0%=\n"
    "s_branch L_rareB_e67_r%=\n"
    "L_rareB_e68%=:\n"
    "s_call_b64 s[96:97], L_rb3_0_0%=\n"
    "s_branch L_rareB_e68_r%=\n"
    "L_rareB_e69%=:\n"
    "s_call_b64 s[96:97], L_rb4_1_0%=\n"
    "s_branch L_rareB_e69_r%=\n"
    "L_rareB_e70%=:\n"
    "s_call_b64 s[96:97], L_rb0_0_0%=\n"
    "s_branch L_rareB_e70_r%=\n"
    "L_rareB_e71%=:\n"
    "s_call_b64 s[96:97], L_rb1_1_0%=\n"
    "s_branch L_rareB_e71_r%=\n"
    "L_rareB_e72%=:\n"
    "s_call_b64 s[96:97], L_rb2_0_0%=\n"
    "s_branch L_rareB_e72_r%=\n"
    "L_rareB_e73%=:\n"
    "s_call_b64 s[96:97], L_rb3_1_0%=\n"
    "s_branch L_rareB_e73_r%=\n"
    "L_rareB_e74%=:\n"
    "s_call_b64 s[96:97], L_rb4_0_0%=\n"
    "s_branch L_rareB_e74_r%=\n"
    "L_rareB_e75%=:\n"
    "s_call_b64 s[96:97], L_rb0_1_0%=\n"
    "s_branch L_rareB_e75_r%=\n"
    "L_end%=:\n"
    :
    : "s"(__builtin_amdgcn_kernarg_segment_ptr()), "s"(wg), "s"(w), "s"(ldsb)
    : "v0", "v1", "v2", "v3", "v4", "v5", "v6", "v7", "v8", "v9", "v10", "v11", "v12", "v13", "v14", "v15", "v16", "v17", "v18", "v19", "v20", "v21", "v22", "v23", "v24", "v25", "v26", "v27", "v28", "v29", "v30", "v31", "v32", "v33", "v34", "v35", "v36", "v37", "v38", "v39", "v40", "v41", "v42", "v43", "v44", "v45", "v46", "v47", "v48", "v49", "v50", "v51", "v52", "v53", "v54", "v55", "v56", "v57", "v58", "v59", "v60", "v61", "v62", "v63", "v64", "v65", "v66", "v67", "v68", "v69", "v70", "v71", "v72", "v73", "v74", "v75", "v76", "v77", "v78", "v79", "v80", "v81", "v82", "v83", "v84", "v85", "v86", "v87", "v88", "v89", "v90", "v91", "v92", "v93", "v94", "v95", "v96", "v97", "v98", "v99", "v100", "v101", "v102", "v103", "v104", "v105", "v106", "v107", "v108", "v109", "v110", "v111", "v112", "v113", "v114", "v115", "v116", "v117", "v118", "v119", "v120", "v121", "v122", "v123", "v124", "v125", "v126", "v127", "v128", "v129", "v130", "v131", "v132", "v133", "v134", "v135", "v136", "v137", "v138", "v139", "v140", "v141", "v142", "v143", "v144", "v145", "v146", "v147", "v148", "v149", "v150", "v151", "v152", "v153", "v154", "v155", "v156", "v157", "v158", "v159", "v160", "v161", "v162", "v163", "v164", "v165", "v166", "v167", "v168", "v169", "v170", "v171", "v172", "v173", "v174", "v175", "v176", "v177", "v178", "v179", "v180", "v181", "v182", "v183", "v184", "v185", "v186", "v187", "v188", "v189", "v190", "v191", "v192", "v193", "v194", "v195", "v196", "v197", "v198", "v199", "v200", "v201", "v202", "v203", "v204", "v205", "v206", "v207", "v208", "v209", "v210", "v211", "v212", "v213", "v214", "v215", "v216", "v217", "v218", "v219", "v220", "v221", "v222", "v223", "v224", "v225", "v226", "v227", "v228", "v229", "v230", "v231", "v232", "v233", "v234", "v235", "v236", "v237", "v238", "v239", "v240", "v241", "v242", "v243", "v244", "v245", "v246", "v247", "v248", "v249", "v250", "v251", "v252", "v253", "v254", "v255", "a0", "a1", "a2", "a3", "a4", "a5", "a6", "a7", "a8", "a9", "a10", "a11", "a12", "a13", "a14", "a15", "a16", "a17", "a18", "a19", "a20", "a21", "a22", "a23", "a24", "a25", "a26", "a27", "a28", "a29", "a30", "a31", "a32", "a33", "a34", "a35", "a36", "a37", "a38", "a39", "a40", "a41", "a42", "a43", "a44", "a45", "a46", "a47", "a48", "a49", "a50", "a51", "a52", "a53", "a54", "a55", "a56", "a57", "a58", "a59", "a60", "a61", "a62", "a63", "a64", "a65", "a66", "a67", "a68", "a69", "a70", "a71", "a72", "a73", "a74", "a75", "a76", "a77", "a78", "a79", "a80", "a81", "a82", "a83", "a84", "a85", "a86", "a87", "a88", "a89", "a90", "a91", "a92", "a93", "a94", "a95", "a96", "a97", "a98", "a99", "a100", "a101", "a102", "a103", "a104", "a105", "a106", "a107", "a108", "a109", "a110", "a111", "a112", "a113", "a114", "a115", "a116", "a117", "a118", "a119", "a120", "a121", "a122", "a123", "a124", "a125", "a126", "a127", "a128", "a129", "a130", "a131", "a132", "a133", "a134", "a135", "a136", "a137", "a138", "a139", "a140", "a141", "a142", "a143", "a144", "a145", "a146", "a147", "a148", "a149", "a150", "a151", "a152", "a153", "a154", "a155", "a156", "a157", "a158", "a159", "a160", "a161", "a162", "a163", "a164", "a165", "a166", "a167", "a168", "a169", "a170", "a171", "a172", "a173", "a174", "a175", "a176", "a177", "a178", "a179", "a180", "a181", "a182", "a183", "a184", "a185", "a186", "a187", "a188", "a189", "a190", "a191", "a192", "a193", "a194", "a195", "a196", "a197", "a198", "a199", "a200", "a201", "a202", "a203", "a204", "a205", "a206", "a207", "s5", "s6", "s7", "s8", "s9", "s10", "s11", "s12", "s13", "s14", "s15", "s16", "s17", "s18", "s19", "s20", "s21", "s22", "s23", "s24", "s25", "s26", "s27", "s28", "s29", "s30", "s31", "s33", "s34", "s35", "s36", "s37", "s38", "s39", "s40", "s41", "s42", "s43", "s44", "s45", "s46", "s47", "s48", "s49", "s50", "s51", "s52", "s53", "s54", "s55", "s56", "s57", "s58", "s59", "s60", "s61", "s62", "s63", "s64", "s65", "s66", "s67", "s68", "s69", "s70", "s71", "s72", "s73", "s74", "s75", "s76", "s77", "s78", "s79", "s80", "s81", "s82", "s83", "s84", "s85", "s86", "s87", "s88", "s89", "s90", "s91", "s92", "s93", "s94", "s95", "s96", "s97", "s98", "s99", "vcc", "scc", "memory");
}

extern "C" void kernel_launch(void* const* d_in, const int* in_sizes, int n_in, void* d_out, int out_size, void* d_ws, size_t ws_size,
                              hipStream_t stream) {
  fused_attn_blocks<<<256, 256, 0, stream>>>((const char*)d_in[0], (const char*)d_in[1], (const char*)d_in[2], (const char*)d_in[3], (const char*)d_in[4],
                                             (const char*)d_in[5], (const char*)d_in[6], (const char*)d_in[7], (const char*)d_in[8], (const char*)d_in[9],
                                             (char*)d_out, (char*)d_ws);
}
```
